```python
import torch

def _softmax(x, axis=-1):
    return torch.softmax(x, dim=axis)
def _gelu(x):
    return torch.nn.functional.gelu(x, approximate="tanh")
def _silu(x):
    return torch.nn.functional.silu(x)
def _sigmoid(x):
    return torch.sigmoid(x)
def _layernorm(x, axis=-1, eps=1e-5):
    m = x.mean(dim=axis, keepdim=True)
    v = x.var(dim=axis, keepdim=True, unbiased=False)
    return (x - m) / torch.sqrt(v + eps)
def _scatter_add(tgt, idx, src, axis=0):
    return tgt.clone().index_add_(axis, idx, src)
def _segment_sum(src, seg, n):
    out = torch.zeros((n,) + src.shape[1:], dtype=src.dtype)
    return out.index_add_(0, seg, src)
def _pad(x, pw):
    flat = tuple(v for pair in reversed(pw) for v in pair)
    return torch.nn.functional.pad(x, flat)

kernel_name = 'parallel_sum__compressed_kv_cla__dense_activation_gelu__decode'

def setup_inputs():

    _w = (lambda a: (lambda t: t.to(torch.bfloat16) if t.is_floating_point() else t)(torch.as_tensor(a)))
    def _A_setup():
        B, H, Sq, Sk, D, M = 2, 16, 1, 4096, 256, 448
        Dc = 4 * D
        return {
            "x": _w((lambda *s: torch.randn(*s, dtype=torch.bfloat16) * 0.05)(B, Sq, M)),
            "c_kv": _w((lambda *s: torch.randn(*s, dtype=torch.bfloat16) * 0.8)(B, Sk, Dc)),
            "w_q": _w((lambda *s: torch.randn(*s, dtype=torch.bfloat16) * 3.2)(M, H, D) * (M ** -0.5)),
            "w_uk": _w((lambda *s: torch.randn(*s, dtype=torch.bfloat16) * 3.2)(Dc, H, D) * (Dc ** -0.5)),
            "w_uv": _w((lambda *s: torch.randn(*s, dtype=torch.bfloat16) * 0.8)(Dc, H, D) * (Dc ** -0.5)),
            "w_o": _w((lambda *s: torch.randn(*s, dtype=torch.bfloat16) * 0.8)(H, D, M) * ((H * D) ** -0.5)),
        }
    _A_in = _A_setup()

    def _B_setup():
        B, S, D, F = 2, 1, 448, 1792
        return {
            "x": _w((lambda *s: torch.randn(*s, dtype=torch.bfloat16) * 0.05)(B, S, D)),
            "w1": _w((lambda *s: torch.randn(*s, dtype=torch.bfloat16) * 1.6)(D, F) * (D ** -0.5)),
            "b1": _w((lambda *s: torch.randn(*s, dtype=torch.bfloat16) * 1.6)(F) * 0.01),
            "w2": _w((lambda *s: torch.randn(*s, dtype=torch.bfloat16) * 0.2)(F, D) * (F ** -0.5)),
            "b2": _w((lambda *s: torch.randn(*s, dtype=torch.bfloat16) * 0.2)(D) * 0.01),
        }
    _B_in = _B_setup()

    return {
        "A_x": _A_in["x"],
        "A_c_kv": _A_in["c_kv"],
        "A_w_q": _A_in["w_q"],
        "A_w_uk": _A_in["w_uk"],
        "A_w_uv": _A_in["w_uv"],
        "A_w_o": _A_in["w_o"],
        "B_x": _B_in["x"],
        "B_w1": _B_in["w1"],
        "B_b1": _B_in["b1"],
        "B_w2": _B_in["w2"],
        "B_b2": _B_in["b2"],
    }

def reference(A_x, A_c_kv, A_w_q, A_w_uk, A_w_uv, A_w_o, B_x, B_w1, B_b1, B_w2, B_b2):
    def _A_ref(x, c_kv, w_q, w_uk, w_uv, w_o):
        B, Sq, M = x.shape[0], x.shape[1], x.shape[2]
        Sk = c_kv.shape[1]
        H, D = w_q.shape[1], w_q.shape[2]
        q = torch.einsum("bqm,mhd->bhqd", x, w_q)
        k = torch.einsum("bkc,chd->bhkd", c_kv, w_uk)
        v = torch.einsum("bkc,chd->bhkd", c_kv, w_uv)
        scores = torch.einsum("bhqd,bhkd->bhqk", q, k) * (D ** -0.5)
        off = Sk - Sq
        qi = torch.arange(Sq)[:, None]
        kj = torch.arange(Sk)[None, :]
        cmask = kj <= (qi + off)
        scores = torch.where(cmask[None, None], scores, -1e9)
        p = _softmax(scores, axis=-1)
        ctx = torch.einsum("bhqk,bhkd->bhqd", p, v)
        return torch.einsum("bhqd,hdm->bqm", ctx, w_o)

    def _B_ref(x, w1, b1, w2, b2):
        h = torch.einsum("bsd,df->bsf", x, w1) + b1
        h = _gelu(h)
        return torch.einsum("bsf,fd->bsd", h, w2) + b2

    x = A_x
    xn = _layernorm(x, axis=-1)
    return x + _A_ref(xn, A_c_kv, A_w_q, A_w_uk, A_w_uv, A_w_o) + _B_ref(xn, B_w1, B_b1, B_w2, B_b2)
```

```cpp
#include <hip/hip_runtime.h>
#include <hip/hip_bf16.h>
#include <stdint.h>

#define B_ 2
#define H_ 16
#define SK 4096
#define D_ 256
#define M_ 448
#define DC 1024
#define F_ 1792

typedef unsigned short u16;
__device__ __forceinline__ float bf2f(u16 v) { return __uint_as_float(((unsigned)v) << 16); }
__device__ __forceinline__ u16 f2bf(float f) { __hip_bfloat16 b = __float2bfloat16(f); return *reinterpret_cast<u16*>(&b); }
__device__ __forceinline__ float rbf(float f) { return bf2f(f2bf(f)); }

__device__ void layernorm(const u16* x, float* xn  , float* red) {
  int t = threadIdx.x;
  if (t < 64) {
    for (int b = 0; b < 2; ++b) {
      float s = 0.f;
      for (int i = t; i < M_; i += 64) s += bf2f(x[b * M_ + i]);
      for (int o = 32; o; o >>= 1) s += __shfl_xor(s, o);
      float mean = s / M_;
      float v = 0.f;
      for (int i = t; i < M_; i += 64) { float d = bf2f(x[b * M_ + i]) - mean; v += d * d; }
      for (int o = 32; o; o >>= 1) v += __shfl_xor(v, o);
      float var = rbf(v / M_);
      float mb = rbf(mean);
      float den = rbf(sqrtf(rbf(var + 1e-5f)));
      for (int i = t; i < M_; i += 64) xn[b * M_ + i] = rbf(rbf(bf2f(x[b * M_ + i]) - mb) / den);
    }
  }
  __syncthreads();
}

__global__ void k_a(const u16* x, const u16* wq, const u16* w1, const u16* b1, float* q, float* h1) {
  __shared__ float xn[2 * M_];
  __shared__ float red[8];
  layernorm(x, xn, red);
  int n = blockIdx.x * blockDim.x + threadIdx.x;
  if (n < 4096) {
    float a0 = 0, a1 = 0;
    for (int m = 0; m < M_; ++m) { float w = bf2f(wq[m * 4096 + n]); a0 += xn[m] * w; a1 += xn[M_ + m] * w; }
    q[n] = rbf(a0); q[4096 + n] = rbf(a1);
  } else if (n < 4096 + F_) {
    int f = n - 4096;
    float a0 = 0, a1 = 0;
    for (int m = 0; m < M_; ++m) { float w = bf2f(w1[m * F_ + f]); a0 += xn[m] * w; a1 += xn[M_ + m] * w; }
    float bb = bf2f(b1[f]);
    float h[2] = {rbf(rbf(a0) + bb), rbf(rbf(a1) + bb)};
    for (int b = 0; b < 2; ++b) {
      float v = h[b];
      float g = 0.5f * v * (1.f + tanhf(0.7978845608028654f * (v + 0.044715f * v * v * v)));
      h1[b * F_ + f] = rbf(g);
    }
  }
}
__global__ void k_b(const float* q, const u16* wuk, float* qlat) {
  int wid = (blockIdx.x * blockDim.x + threadIdx.x) >> 6, l = threadIdx.x & 63;
  int c = wid >> 4, h = wid & 15;
  float a0 = 0, a1 = 0;
  for (int d = l; d < D_; d += 64) { float w = bf2f(wuk[(c * 16 + h) * 256 + d]); a0 += q[h * 256 + d] * w; a1 += q[4096 + h * 256 + d] * w; }
  for (int o = 32; o; o >>= 1) { a0 += __shfl_xor(a0, o); a1 += __shfl_xor(a1, o); }
  if (l == 0) { qlat[h * DC + c] = rbf(a0); qlat[16 * DC + h * DC + c] = rbf(a1); }
}
__global__ void k_c(const float* qlat, const u16* ckv, float* sc) {
  int wid = (blockIdx.x * blockDim.x + threadIdx.x) >> 6, l = threadIdx.x & 63;
  int b = wid >> 12, k = wid & 4095;
  float acc[16];
  for (int h = 0; h < 16; ++h) acc[h] = 0;
  for (int c = l; c < DC; c += 64) {
    float v = bf2f(ckv[((size_t)b * SK + k) * DC + c]);
    for (int h = 0; h < 16; ++h) acc[h] += qlat[(b * 16 + h) * DC + c] * v;
  }
  for (int h = 0; h < 16; ++h) {
    float a = acc[h];
    for (int o = 32; o; o >>= 1) a += __shfl_xor(a, o);
    if (l == 0) sc[(b * 16 + h) * SK + k] = a * 0.0625f;
  }
}
__global__ void k_d(const float* sc, const u16* ckv, float* ctxlat) {
  __shared__ float p[SK];
  __shared__ float red[16];
  int bh = blockIdx.x, b = bh >> 4, t = threadIdx.x;
  float m = -1e30f;
  for (int k = t; k < SK; k += 1024) m = fmaxf(m, sc[bh * SK + k]);
  for (int o = 32; o; o >>= 1) m = fmaxf(m, __shfl_xor(m, o));
  if ((t & 63) == 0) red[t >> 6] = m;
  __syncthreads();
  m = red[0]; for (int i = 1; i < 16; ++i) m = fmaxf(m, red[i]);
  __syncthreads();
  float s = 0;
  for (int k = t; k < SK; k += 1024) { float e = __expf(sc[bh * SK + k] - m); p[k] = e; s += e; }
  for (int o = 32; o; o >>= 1) s += __shfl_xor(s, o);
  if ((t & 63) == 0) red[t >> 6] = s;
  __syncthreads();
  s = 0; for (int i = 0; i < 16; ++i) s += red[i];
  float inv = 1.f / s;
  for (int k = t; k < SK; k += 1024) p[k] = rbf(p[k] * inv);
  __syncthreads();
  float a = 0;
  for (int k = 0; k < SK; ++k) a += p[k] * bf2f(ckv[((size_t)b * SK + k) * DC + t]);
  ctxlat[bh * DC + t] = a;
}
__global__ void k_e(const float* ctxlat, const u16* wuv, float* ctx) {
  int n = blockIdx.x * blockDim.x + threadIdx.x;
  int h = n >> 8;
  float a0 = 0, a1 = 0;
  for (int c = 0; c < DC; ++c) { float w = bf2f(wuv[c * 4096 + n]); a0 += ctxlat[h * DC + c] * w; a1 += ctxlat[(16 + h) * DC + c] * w; }
  ctx[n] = rbf(a0); ctx[4096 + n] = rbf(a1);
}
__global__ void k_f(const u16* x, const float* ctx, const u16* wo, const float* h1, const u16* w2, const u16* b2, u16* out) {
  int m = blockIdx.x, t = threadIdx.x;
  __shared__ float red[4][4];
  float a0 = 0, a1 = 0, y0 = 0, y1 = 0;
  for (int r = t; r < 4096; r += 256) { float w = bf2f(wo[r * M_ + m]); a0 += ctx[r] * w; a1 += ctx[4096 + r] * w; }
  for (int r = t; r < F_; r += 256) { float w = bf2f(w2[r * M_ + m]); y0 += h1[r] * w; y1 += h1[F_ + r] * w; }
  for (int o = 32; o; o >>= 1) { a0 += __shfl_xor(a0, o); a1 += __shfl_xor(a1, o); y0 += __shfl_xor(y0, o); y1 += __shfl_xor(y1, o); }
  if ((t & 63) == 0) { red[t >> 6][0] = a0; red[t >> 6][1] = a1; red[t >> 6][2] = y0; red[t >> 6][3] = y1; }
  __syncthreads();
  if (t < 2) {
    float a = red[0][t] + red[1][t] + red[2][t] + red[3][t];
    float y = red[0][2 + t] + red[1][2 + t] + red[2][2 + t] + red[3][2 + t];
    float yb = rbf(rbf(y) + bf2f(b2[m]));
    float o = rbf(rbf(bf2f(x[t * M_ + m]) + rbf(a)) + yb);
    out[t * M_ + m] = f2bf(o);
  }
}

extern "C" void kernel_launch(void* const* d_in, const int* in_sizes, int n_in,
                              void* d_out, int out_size, void* d_ws, size_t ws_size,
                              hipStream_t stream) {
  const u16* x = (const u16*)d_in[0];
  const u16* ckv = (const u16*)d_in[1];
  const u16* wq = (const u16*)d_in[2];
  const u16* wuk = (const u16*)d_in[3];
  const u16* wuv = (const u16*)d_in[4];
  const u16* wo = (const u16*)d_in[5];
  const u16* w1 = (const u16*)d_in[7];
  const u16* b1 = (const u16*)d_in[8];
  const u16* w2 = (const u16*)d_in[9];
  const u16* b2 = (const u16*)d_in[10];
  float* ws = (float*)d_ws;
  float* q = ws;
  float* h1 = q + 8192;
  float* qlat = h1 + 4096;
  float* sc = qlat + 32768;
  float* ctxlat = sc + 131072;
  float* ctx = ctxlat + 32768;
  k_a<<<(4096 + F_) / 64, 64, 0, stream>>>(x, wq, w1, b1, q, h1);
  k_b<<<DC * 16 / 4, 256, 0, stream>>>(q, wuk, qlat);
  k_c<<<2 * SK / 4, 256, 0, stream>>>(qlat, ckv, sc);
  k_d<<<32, 1024, 0, stream>>>(sc, ckv, ctxlat);
  k_e<<<4096 / 64, 64, 0, stream>>>(ctxlat, wuv, ctx);
  k_f<<<M_, 256, 0, stream>>>(x, ctx, wo, h1, w2, b2, (u16*)d_out);
}
```

```cpp
#include <hip/hip_runtime.h>
#include <hip/hip_bf16.h>
#include <stdint.h>


typedef unsigned short u16;
typedef unsigned int u32;
typedef unsigned long long u64;
typedef __attribute__((ext_vector_type(4))) unsigned v4u;
typedef __attribute__((ext_vector_type(2))) unsigned v2u;
typedef __attribute__((ext_vector_type(4))) float v4f;
typedef __attribute__((ext_vector_type(8))) __bf16 bf8;
typedef __attribute__((ext_vector_type(4))) short s4;

#define GAS __attribute__((address_space(1)))
#define LAS __attribute__((address_space(3)))
typedef GAS u32 gu32;
typedef GAS u64 gu64;
#define RLX_AGENT __ATOMIC_RELAXED, __HIP_MEMORY_SCOPE_AGENT

#define M_ 448
#define F_ 1792
#define DC 1024
#define SK 4096

constexpr u32 TAG = 0x13579BDFu;
constexpr u32 SPIN_CAP = 1u << 18;

constexpr size_t WS_QLAT = 0;
constexpr size_t WS_H1 = 65536;
constexpr size_t WS_CNT = 81920;
constexpr size_t WS_CTXG = 90112;
constexpr size_t WS_SCG = 131072;
constexpr size_t WS_ML = 1179648;
constexpr size_t WS_PART = 1310720;

__device__ __forceinline__ float bf2f(u16 v) { return __uint_as_float(((u32)v) << 16); }
__device__ __forceinline__ u16 f2bf(float f) { __hip_bfloat16 b = __float2bfloat16(f); return *reinterpret_cast<u16*>(&b); }
__device__ __forceinline__ float rbf(float f) { return bf2f(f2bf(f)); }
__device__ __forceinline__ float lo_f(u32 v) { return __uint_as_float(v << 16); }
__device__ __forceinline__ float hi_f(u32 v) { return __uint_as_float(v & 0xffff0000u); }
__device__ __forceinline__ v4u ld16(const u16* p) { return *reinterpret_cast<const v4u*>(p); }
__device__ __forceinline__ bf8 as_bf8(v4u v) { return __builtin_bit_cast(bf8, v); }

__device__ __forceinline__ float wave_sum(float v) {
#pragma unroll
  for (int o = 32; o; o >>= 1) v += __shfl_xor(v, o);
  return v;
}
__device__ __forceinline__ float wave_max(float v) {
#pragma unroll
  for (int o = 32; o; o >>= 1) v = fmaxf(v, __shfl_xor(v, o));
  return v;
}

__global__ __launch_bounds__(512, 2) void k1(const u16* __restrict__ x, const u16* __restrict__ wq,
                                             const u16* __restrict__ wuk, const u16* __restrict__ w1,
                                             const u16* __restrict__ b1, char* __restrict__ ws) {
  __shared__ float xn[2][M_];
  __shared__ __attribute__((aligned(16))) float qred[8][2][256];
  __shared__ __attribute__((aligned(16))) u16 qb[2][256];
  __shared__ float lred[4][2][16];
  __shared__ float hred[8][16];

  const int t = threadIdx.x, lane = t & 63;
  const int w = __builtin_amdgcn_readfirstlane(t >> 6);
  const int i = blockIdx.x, xcd = i & 7, r = i >> 3;
  const int h = xcd * 2 + (r >> 4), cs = r & 15;

  const int l32 = t & 31, rg = t >> 5;
  v4u wqv[28];
#pragma unroll
  for (int k = 0; k < 28; ++k) wqv[k] = ld16(wq + (size_t)(rg + 16 * k) * 4096 + h * 256 + l32 * 8);
  const int ct = w & 3, kh = w >> 2;
  v4u wukv[4];
#pragma unroll
  for (int s = 0; s < 4; ++s)
    wukv[s] = ld16(wuk + ((size_t)(cs * 64 + ct * 16 + (lane & 15)) * 16 + h) * 256 + kh * 128 + s * 32 + 8 * (lane >> 4));
  v4u w1v = {0u, 0u, 0u, 0u};
  const bool mlp = (i < 224);
  if (mlp && t < M_) w1v = ld16(w1 + (size_t)t * F_ + i * 8);

  *reinterpret_cast<u64*>(ws + WS_SCG + (size_t)i * 4096 + t * 8) = 0ull;
  if (t < 16) *reinterpret_cast<u64*>(ws + WS_CTXG + (size_t)i * 128 + t * 8) = 0ull;
  if (i < 33 && t >= 64 && t < 80) *reinterpret_cast<u64*>(ws + WS_CNT + (size_t)i * 128 + (t - 64) * 8) = 0ull;

  if (w < 2) {
    float xv[7];
#pragma unroll
    for (int k = 0; k < 7; ++k) xv[k] = bf2f(x[w * M_ + lane + 64 * k]);
    float s = 0.f;
#pragma unroll
    for (int k = 0; k < 7; ++k) s += xv[k];
    s = wave_sum(s);
    const float mean = s * (1.f / M_);
    float v = 0.f;
#pragma unroll
    for (int k = 0; k < 7; ++k) { float d = xv[k] - mean; v += d * d; }
    v = wave_sum(v);
    const float var = rbf(v * (1.f / M_));
    const float mb = rbf(mean);
    const float den = rbf(sqrtf(rbf(var + 1e-5f)));
#pragma unroll
    for (int k = 0; k < 7; ++k) xn[w][lane + 64 * k] = rbf(rbf(xv[k] - mb) / den);
  }
  __syncthreads();

  float a0[8], a1[8];
#pragma unroll
  for (int j = 0; j < 8; ++j) { a0[j] = 0.f; a1[j] = 0.f; }
#pragma unroll
  for (int k = 0; k < 28; ++k) {
    const int m = rg + 16 * k;
    const float x0 = xn[0][m], x1 = xn[1][m];
#pragma unroll
    for (int p = 0; p < 4; ++p) {
      const float wl = lo_f(wqv[k][p]), wh = hi_f(wqv[k][p]);
      a0[2 * p] += x0 * wl; a0[2 * p + 1] += x0 * wh;
      a1[2 * p] += x1 * wl; a1[2 * p + 1] += x1 * wh;
    }
  }
#pragma unroll
  for (int j = 0; j < 8; ++j) { a0[j] += __shfl_xor(a0[j], 32); a1[j] += __shfl_xor(a1[j], 32); }
  if (lane < 32) {
    *reinterpret_cast<v4f*>(&qred[w][0][l32 * 8]) = v4f{a0[0], a0[1], a0[2], a0[3]};
    *reinterpret_cast<v4f*>(&qred[w][0][l32 * 8 + 4]) = v4f{a0[4], a0[5], a0[6], a0[7]};
    *reinterpret_cast<v4f*>(&qred[w][1][l32 * 8]) = v4f{a1[0], a1[1], a1[2], a1[3]};
    *reinterpret_cast<v4f*>(&qred[w][1][l32 * 8 + 4]) = v4f{a1[4], a1[5], a1[6], a1[7]};
  }

  {
    const int tm = t < M_ ? t : 0;
    const float x0 = xn[0][tm], x1 = xn[1][tm];
    float hp[16];
#pragma unroll
    for (int p = 0; p < 4; ++p) {
      const float wl = lo_f(w1v[p]), wh = hi_f(w1v[p]);
      hp[2 * p] = x0 * wl; hp[2 * p + 1] = x0 * wh;
      hp[8 + 2 * p] = x1 * wl; hp[8 + 2 * p + 1] = x1 * wh;
    }
#pragma unroll
    for (int j = 0; j < 16; ++j) hp[j] = wave_sum(hp[j]);
    if (lane == 0) {
#pragma unroll
      for (int j = 0; j < 16; ++j) hred[w][j] = hp[j];
    }
  }
  __syncthreads();
  {
    const int b = t >> 8, d = t & 255;
    float s = 0.f;
#pragma unroll
    for (int ww = 0; ww < 8; ++ww) s += qred[ww][b][d];
    qb[b][d] = f2bf(s);
  }
  if (mlp && t < 16) {
    float s = 0.f;
#pragma unroll
    for (int ww = 0; ww < 8; ++ww) s += hred[ww][t];
    const int b = t >> 3, j = t & 7;
    const float hv = rbf(rbf(s) + bf2f(b1[i * 8 + j]));
    const float g = 0.5f * hv * (1.f + tanhf(0.7978845608028654f * (hv + 0.044715f * hv * hv * hv)));
    reinterpret_cast<float*>(ws + WS_H1)[b * F_ + i * 8 + j] = rbf(g);
  }
  __syncthreads();

  v4f acc = {0.f, 0.f, 0.f, 0.f};
  {
    const int row = lane & 15;
#pragma unroll
    for (int s = 0; s < 4; ++s) {
      v4u av = *reinterpret_cast<const v4u*>(&qb[row & 1][kh * 128 + s * 32 + 8 * (lane >> 4)]);
      if (row >= 2) av = v4u{0u, 0u, 0u, 0u};
      acc = __builtin_amdgcn_mfma_f32_16x16x32_bf16(as_bf8(av), as_bf8(wukv[s]), acc, 0, 0, 0);
    }
  }
  if (kh == 1 && lane < 16) { lred[ct][0][lane] = acc[0]; lred[ct][1][lane] = acc[1]; }
  __syncthreads();
  if (kh == 0 && lane < 16) {
    u16* ql = reinterpret_cast<u16*>(ws + WS_QLAT);
    const int c = cs * 64 + ct * 16 + lane;
    ql[(0 * 16 + h) * DC + c] = f2bf(acc[0] + lred[ct][0][lane]);
    ql[(1 * 16 + h) * DC + c] = f2bf(acc[1] + lred[ct][1][lane]);
  }
}

__device__ __forceinline__ u32 swz(u32 row) { return ((row & 3u) << 2) | ((row >> 2) & 3u); }
__device__ __forceinline__ u32 off_b(u32 row, u32 ch) { return 256u * row + 16u * (ch ^ swz(row)); }

struct __attribute__((aligned(16))) K2Lds {
  char kv[65536];
  float sred[4][2][16][16];
  float S[16][256];
  u16 P[16][264];
  float O[16][132];
  float ml[16][2];
  float ctxlat[2][1024];
  float cred[8][32];
  float fred[8][16];
};

__global__ __launch_bounds__(768, 3) void k2(const u16* __restrict__ x, const u16* __restrict__ ckv,
                                             const u16* __restrict__ wuv, const u16* __restrict__ wo,
                                             const u16* __restrict__ w2, const u16* __restrict__ b2,
                                             char* __restrict__ ws, u16* __restrict__ out) {
  __shared__ K2Lds L;
  const int t = threadIdx.x, lane = t & 63;
  const int w = __builtin_amdgcn_readfirstlane(t >> 6);
  const int i = blockIdx.x, xcd = i & 7, r = i >> 3;
  const int G = xcd * 4 + (r >> 3), b = G >> 4, g = G & 15, j = r & 7;
  const int b2i = r & 1, hd = xcd * 16 + (r >> 1), h2 = hd >> 3, ds = hd & 7;
  const bool fin = (i < 56);
  gu32* tmo = (gu32*)(ws + WS_CNT + 32 * 128);

  if (w < 8) {
    const int kt = w & 1, cq = w >> 1;
    const u16* ql = reinterpret_cast<const u16*>(ws + WS_QLAT) + (size_t)(b * 16 + (lane & 15)) * DC + cq * 256 + 8 * (lane >> 4);
    const u16* kr = ckv + ((size_t)b * SK + g * 256 + j * 32 + kt * 16 + (lane & 15)) * DC + cq * 256 + 8 * (lane >> 4);
    v4u av[8], bv[8], uv[8];
#pragma unroll
    for (int s = 0; s < 8; ++s) av[s] = ld16(ql + s * 32);
#pragma unroll
    for (int s = 0; s < 8; ++s) bv[s] = ld16(kr + s * 32);
    {
      const u16* up = wuv + ((size_t)(t >> 2) * 16 + h2) * 256 + ds * 32 + (t & 3) * 8;
#pragma unroll
      for (int p = 0; p < 8; ++p) uv[p] = ld16(up + (size_t)p * 128 * 4096);
    }
    {
      v4f acc = {0.f, 0.f, 0.f, 0.f};
#pragma unroll
      for (int s = 0; s < 8; ++s) acc = __builtin_amdgcn_mfma_f32_16x16x32_bf16(as_bf8(av[s]), as_bf8(bv[s]), acc, 0, 0, 0);
#pragma unroll
      for (int q = 0; q < 4; ++q) L.sred[cq][kt][(lane >> 4) * 4 + q][lane & 15] = acc[q];
    }
    __syncthreads();
    __syncthreads();
#pragma unroll
    for (int hh = 0; hh < 2; ++hh) {
      const int hq = 2 * w + hh;
      const v4f sv = *reinterpret_cast<const v4f*>(&L.S[hq][4 * lane]);
      const float m = wave_max(fmaxf(fmaxf(sv[0], sv[1]), fmaxf(sv[2], sv[3])));
      u16 pb[4];
      float l = 0.f;
#pragma unroll
      for (int q = 0; q < 4; ++q) { pb[q] = f2bf(__expf(sv[q] - m)); l += bf2f(pb[q]); }
      l = wave_sum(l);
      *reinterpret_cast<v2u*>(&L.P[hq][4 * lane]) = v2u{(u32)pb[0] | ((u32)pb[1] << 16), (u32)pb[2] | ((u32)pb[3] << 16)};
      if (lane == 0) { L.ml[hq][0] = m; L.ml[hq][1] = l; }
    }
    __syncthreads();
    {
      v4f acc = {0.f, 0.f, 0.f, 0.f};
      const u32 gq = lane >> 4, qq = (lane & 15) >> 2, pp = lane & 3;
#pragma unroll
      for (int ks = 0; ks < 8; ++ks) {
        const v4u pa = *reinterpret_cast<const v4u*>(&L.P[lane & 15][32 * ks + 8 * (lane >> 4)]);
        const s4 b0 = __builtin_amdgcn_ds_read_tr16_b64_v4i16((LAS s4*)(L.kv + off_b(32 * ks + 8 * gq + qq, 2 * w + (pp >> 1)) + 8 * (pp & 1)));
        const s4 b1 = __builtin_amdgcn_ds_read_tr16_b64_v4i16((LAS s4*)(L.kv + off_b(32 * ks + 8 * gq + 4 + qq, 2 * w + (pp >> 1)) + 8 * (pp & 1)));
        typedef __attribute__((ext_vector_type(8))) short s8;
        const s8 bb = {b0[0], b0[1], b0[2], b0[3], b1[0], b1[1], b1[2], b1[3]};
        acc = __builtin_amdgcn_mfma_f32_16x16x32_bf16(as_bf8(pa), __builtin_bit_cast(bf8, bb), acc, 0, 0, 0);
      }
#pragma unroll
      for (int q = 0; q < 4; ++q) L.O[(lane >> 4) * 4 + q][w * 16 + (lane & 15)] = acc[q];
    }
    __syncthreads();
    v4u ov[8], w2v[4];
    float hv0[4], hv1[4];
    if (fin) {
#pragma unroll
      for (int p = 0; p < 8; ++p) ov[p] = ld16(wo + (size_t)(t + 512 * p) * M_ + 8 * i);
      const float* h1 = reinterpret_cast<const float*>(ws + WS_H1);
#pragma unroll
      for (int p = 0; p < 4; ++p) {
        const int rr = t + 512 * p;
        const int rc = rr < F_ ? rr : 0;
        w2v[p] = ld16(w2 + (size_t)rc * M_ + 8 * i);
        hv0[p] = rr < F_ ? h1[rc] : 0.f;
        hv1[p] = rr < F_ ? h1[F_ + rc] : 0.f;
      }
    }
    __syncthreads();
    {
      const __amdgpu_buffer_rsrc_t rs = __builtin_amdgcn_make_buffer_rsrc(ws + WS_PART, 0, 2 * 16 * 16 * 4096, 0x00020000);
      const int half = w >> 2;
      const u32 base = (u32)((b2i * 16 + h2) * 16 + half * 8) * 4096u + (u32)(t & 255) * 16u;
      v4u pv[8];
#pragma unroll
      for (int gg = 0; gg < 8; ++gg) pv[gg] = __builtin_amdgcn_raw_buffer_load_b128(rs, base + gg * 4096u, 0, 16);
      gu64* mlp = (gu64*)(ws + WS_ML) + (b2i * 16 + h2) * 16;
      const u64 mlv = __hip_atomic_load(mlp + (lane & 15), RLX_AGENT);
      const float mg = __uint_as_float((u32)mlv), lg = __uint_as_float((u32)(mlv >> 32));
      const float Mx = wave_max(mg);
      const float wg = __expf(mg - Mx);
      const float Ls = wave_sum(wg * lg) * 0.25f;
      const float wn = wg / Ls;
      v4f acc = {0.f, 0.f, 0.f, 0.f};
#pragma unroll
      for (int gg = 0; gg < 8; ++gg) acc += __shfl(wn, half * 8 + gg) * __builtin_bit_cast(v4f, pv[gg]);
      *reinterpret_cast<v4f*>(&L.ctxlat[half][4 * (t & 255)]) = acc;
    }
    __syncthreads();
    {
      float ca[8];
#pragma unroll
      for (int q = 0; q < 8; ++q) ca[q] = 0.f;
#pragma unroll
      for (int p = 0; p < 8; ++p) {
        const float cv = L.ctxlat[0][(t >> 2) + 128 * p] + L.ctxlat[1][(t >> 2) + 128 * p];
#pragma unroll
        for (int q = 0; q < 4; ++q) { ca[2 * q] += cv * lo_f(uv[p][q]); ca[2 * q + 1] += cv * hi_f(uv[p][q]); }
      }
#pragma unroll
      for (int q = 0; q < 8; ++q) {
#pragma unroll
        for (int o = 4; o < 64; o <<= 1) ca[q] += __shfl_xor(ca[q], o);
      }
      if (lane < 4) {
#pragma unroll
        for (int q = 0; q < 8; ++q) L.cred[w][lane * 8 + q] = ca[q];
      }
    }
    __syncthreads();
    if (fin) {
      float* ctx = reinterpret_cast<float*>(L.kv);
      gu64* cg = (gu64*)(ws + WS_CTXG) + t;
      u32 v[8];
      for (u32 spins = 0;;) {
        bool ok = true;
#pragma unroll
        for (int k = 0; k < 8; ++k) { const u64 xv = __hip_atomic_load(cg + 512 * k, RLX_AGENT); v[k] = (u32)xv; ok &= (u32)(xv >> 32) == TAG; }
        if (__all(ok)) break;
        if (++spins > SPIN_CAP) { if (lane == 0) __hip_atomic_store(tmo, 3u, RLX_AGENT); break; }
        __builtin_amdgcn_s_sleep(1);
      }
#pragma unroll
      for (int k = 0; k < 8; ++k) {
        const int n = t + 512 * k;
        *reinterpret_cast<float2*>(&ctx[2 * n]) = make_float2(lo_f(v[k]), hi_f(v[k]));
      }
      __syncthreads();
      float f0[8], f1[8], y0[8], y1[8];
#pragma unroll
      for (int q = 0; q < 8; ++q) { f0[q] = 0.f; f1[q] = 0.f; y0[q] = 0.f; y1[q] = 0.f; }
#pragma unroll
      for (int p = 0; p < 8; ++p) {
        const float c0 = ctx[t + 512 * p], c1 = ctx[4096 + t + 512 * p];
#pragma unroll
        for (int q = 0; q < 4; ++q) {
          const float wl = lo_f(ov[p][q]), wh = hi_f(ov[p][q]);
          f0[2 * q] += c0 * wl; f0[2 * q + 1] += c0 * wh; f1[2 * q] += c1 * wl; f1[2 * q + 1] += c1 * wh;
        }
      }
#pragma unroll
      for (int p = 0; p < 4; ++p) {
#pragma unroll
        for (int q = 0; q < 4; ++q) {
          const float wl = lo_f(w2v[p][q]), wh = hi_f(w2v[p][q]);
          y0[2 * q] += hv0[p] * wl; y0[2 * q + 1] += hv0[p] * wh; y1[2 * q] += hv1[p] * wl; y1[2 * q + 1] += hv1[p] * wh;
        }
      }
      float fa[16];
#pragma unroll
      for (int q = 0; q < 8; ++q) { fa[q] = wave_sum(f0[q]); fa[8 + q] = wave_sum(f1[q]); }
      if (lane == 0) {
#pragma unroll
        for (int q = 0; q < 16; ++q) L.fred[w][q] = fa[q];
      }
#pragma unroll
      for (int q = 0; q < 8; ++q) { fa[q] = wave_sum(y0[q]); fa[8 + q] = wave_sum(y1[q]); }
      if (lane == 0) {
#pragma unroll
        for (int q = 0; q < 16; ++q) L.cred[w][q] = fa[q];
      }
      __syncthreads();
      if (t < 16) {
        float a = 0.f, y = 0.f;
#pragma unroll
        for (int ww = 0; ww < 8; ++ww) { a += L.fred[ww][t]; y += L.cred[ww][t]; }
        const int bb = t >> 3, m = 8 * i + (t & 7);
        const float yb = rbf(rbf(y) + bf2f(b2[m]));
        out[bb * M_ + m] = f2bf(rbf(bf2f(x[bb * M_ + m]) + rbf(a)) + yb);
      }
    }
  } else {
    const int cw = w - 8, ct = t - 512;
#pragma unroll
    for (int n = 0; n < 16; ++n) {
      const u32 brow = (u32)(cw * 16 + n) * 4u;
      const u32 row = brow + (lane >> 4);
      const u32 ch = (u32)(lane & 15) ^ swz(row);
      const u16* src = ckv + ((size_t)b * SK + g * 256 + row) * DC + j * 128 + ch * 8;
      __builtin_amdgcn_global_load_lds((const GAS void*)src, (LAS void*)(L.kv + brow * 256u), 16, 0, 0);
    }
    __syncthreads();
    {
      gu64* sg = (gu64*)(ws + WS_SCG) + (size_t)G * 4096;
#pragma unroll
      for (int u = 0; u < 2; ++u) {
        const int n = ct + 256 * u, hq = n >> 5, key = n & 31;
        const float s = (L.sred[0][key >> 4][hq][key & 15] + L.sred[1][key >> 4][hq][key & 15] +
                         L.sred[2][key >> 4][hq][key & 15] + L.sred[3][key >> 4][hq][key & 15]) * 0.0625f;
        __hip_atomic_store(sg + j * 512 + n, ((u64)TAG << 32) | __float_as_uint(s), RLX_AGENT);
      }
      u32 v[16];
      for (u32 spins = 0;;) {
        bool ok = true;
#pragma unroll
        for (int k = 0; k < 16; ++k) { const u64 xv = __hip_atomic_load(sg + ct + 256 * k, RLX_AGENT); v[k] = (u32)xv; ok &= (u32)(xv >> 32) == TAG; }
        if (__all(ok)) break;
        if (++spins > SPIN_CAP) { if (lane == 0) __hip_atomic_store(tmo, 1u, RLX_AGENT); break; }
        __builtin_amdgcn_s_sleep(1);
      }
#pragma unroll
      for (int k = 0; k < 16; ++k) {
        const int n = ct + 256 * (k & 1), p = k >> 1;
        L.S[n >> 5][p * 32 + (n & 31)] = __uint_as_float(v[k]);
      }
    }
    __syncthreads();
    asm volatile("s_waitcnt vmcnt(0)" ::: "memory");
    __syncthreads();
    __syncthreads();
    {
      const __amdgpu_buffer_rsrc_t rs = __builtin_amdgcn_make_buffer_rsrc(ws + WS_PART, 0, 2 * 16 * 16 * 4096, 0x00020000);
#pragma unroll
      for (int u = 0; u < 2; ++u) {
        const int n = ct + 256 * u, hq = n >> 5, c4 = n & 31;
        const v4f ov = *reinterpret_cast<const v4f*>(&L.O[hq][4 * c4]);
        const u32 off = (u32)(((b * 16 + hq) * 16 + g) * 4096 + j * 512 + c4 * 16);
        __builtin_amdgcn_raw_buffer_store_b128(__builtin_bit_cast(v4u, ov), rs, off, 0, 16);
      }
      if (j == 0 && ct < 16) {
        gu64* mlp = (gu64*)(ws + WS_ML) + (b * 16 + ct) * 16 + g;
        __hip_atomic_store(mlp, ((u64)__float_as_uint(L.ml[ct][1]) << 32) | __float_as_uint(L.ml[ct][0]), RLX_AGENT);
      }
      asm volatile("s_waitcnt vmcnt(0)" ::: "memory");
      if (lane == 0) __hip_atomic_fetch_add((gu32*)(ws + WS_CNT + (size_t)G * 128), 1u, RLX_AGENT);
      if (cw == 0) {
        gu32* cp = (gu32*)(ws + WS_CNT + (size_t)(b2i * 16 + (lane & 15)) * 128);
        for (u32 spins = 0;;) {
          const u32 c = __hip_atomic_load(cp, RLX_AGENT);
          if (__all(c == 32u)) break;
          if (++spins > SPIN_CAP) { if (lane == 0) __hip_atomic_store(tmo, 2u, RLX_AGENT); break; }
          __builtin_amdgcn_s_sleep(1);
        }
      }
    }
    __syncthreads();
    __syncthreads();
    __syncthreads();
    if (cw == 0 && lane < 16) {
      float s0 = 0.f, s1 = 0.f;
#pragma unroll
      for (int ww = 0; ww < 8; ++ww) { s0 += L.cred[ww][2 * lane]; s1 += L.cred[ww][2 * lane + 1]; }
      gu64* cg = (gu64*)(ws + WS_CTXG) + b2i * 2048 + (h2 * 256 + ds * 32) / 2 + lane;
      __hip_atomic_store(cg, ((u64)TAG << 32) | ((u32)f2bf(s1) << 16) | (u32)f2bf(s0), RLX_AGENT);
    }
    if (fin) {
      __syncthreads();
      __syncthreads();
    }
  }
}

extern "C" void kernel_launch(void* const* d_in, const int* in_sizes, int n_in,
                              void* d_out, int out_size, void* d_ws, size_t ws_size,
                              hipStream_t stream) {
  const u16* x = (const u16*)d_in[0];
  const u16* ckv = (const u16*)d_in[1];
  const u16* wq = (const u16*)d_in[2];
  const u16* wuk = (const u16*)d_in[3];
  const u16* wuv = (const u16*)d_in[4];
  const u16* wo = (const u16*)d_in[5];
  const u16* w1 = (const u16*)d_in[7];
  const u16* b1 = (const u16*)d_in[8];
  const u16* w2 = (const u16*)d_in[9];
  const u16* b2 = (const u16*)d_in[10];
  k1<<<256, 512, 0, stream>>>(x, wq, wuk, w1, b1, (char*)d_ws);
  k2<<<256, 768, 0, stream>>>(x, ckv, wuv, wo, w2, b2, (char*)d_ws, (u16*)d_out);
}
```

```cpp
#include <hip/hip_runtime.h>
#include <hip/hip_bf16.h>
#include <stdint.h>

typedef unsigned short u16;
typedef unsigned int u32;
typedef unsigned long long u64;
typedef __attribute__((ext_vector_type(4))) unsigned v4u;
typedef __attribute__((ext_vector_type(2))) unsigned v2u;
typedef __attribute__((ext_vector_type(4))) float v4f;
typedef __attribute__((ext_vector_type(8))) __bf16 bf8;
typedef __attribute__((ext_vector_type(4))) short s4;
typedef __attribute__((ext_vector_type(8))) short s8;

#define GAS __attribute__((address_space(1)))
#define LAS __attribute__((address_space(3)))
typedef GAS u32 gu32;
typedef GAS u64 gu64;
#define RLX_AGENT __ATOMIC_RELAXED, __HIP_MEMORY_SCOPE_AGENT

#define M_ 448
#define F_ 1792
#define DC 1024
#define SK 4096

constexpr u32 TAG = 0x13579BDFu;
constexpr u32 SPIN_CAP = 1u << 18;

constexpr size_t WS_Q = 0;
constexpr size_t WS_H1 = 16384;
constexpr size_t WS_QLAT = 32768;
constexpr size_t WS_CNT = 98304;
constexpr size_t WS_SCG = 131072;
constexpr size_t WS_OUTG = 1179648;
constexpr size_t WS_ML = 2097152;
constexpr size_t WS_PART = 2101248;

#define STAMP(k) do {} while (0)

__device__ __forceinline__ float bf2f(u16 v) { return __uint_as_float(((u32)v) << 16); }
__device__ __forceinline__ u16 f2bf(float f) { __hip_bfloat16 b = __float2bfloat16(f); return *reinterpret_cast<u16*>(&b); }
__device__ __forceinline__ float rbf(float f) { return bf2f(f2bf(f)); }
__device__ __forceinline__ float lo_f(u32 v) { return __uint_as_float(v << 16); }
__device__ __forceinline__ float hi_f(u32 v) { return __uint_as_float(v & 0xffff0000u); }
__device__ __forceinline__ v4u ld16(const u16* p) { return *reinterpret_cast<const v4u*>(p); }
__device__ __forceinline__ bf8 as_bf8(v4u v) { return __builtin_bit_cast(bf8, v); }

template <int CTRL, int RMASK>
__device__ __forceinline__ float dpp_add(float v) {
  const int o = __builtin_amdgcn_update_dpp(0, __float_as_int(v), CTRL, RMASK, 0xf, true);
  return v + __int_as_float(o);
}
template <int CTRL, int RMASK>
__device__ __forceinline__ float dpp_max(float v) {
  const int o = __builtin_amdgcn_update_dpp(__float_as_int(v), __float_as_int(v), CTRL, RMASK, 0xf, false);
  return fmaxf(v, __int_as_float(o));
}
__device__ __forceinline__ float wsum63(float v) {
  v = dpp_add<0x111, 0xf>(v);
  v = dpp_add<0x112, 0xf>(v);
  v = dpp_add<0x114, 0xf>(v);
  v = dpp_add<0x118, 0xf>(v);
  v = dpp_add<0x142, 0xa>(v);
  v = dpp_add<0x143, 0xc>(v);
  return v;
}
__device__ __forceinline__ float wmax63(float v) {
  v = dpp_max<0x111, 0xf>(v);
  v = dpp_max<0x112, 0xf>(v);
  v = dpp_max<0x114, 0xf>(v);
  v = dpp_max<0x118, 0xf>(v);
  v = dpp_max<0x142, 0xa>(v);
  v = dpp_max<0x143, 0xc>(v);
  return v;
}
__device__ __forceinline__ float bcast63(float v) { return __int_as_float(__builtin_amdgcn_readlane(__float_as_int(v), 63)); }
__device__ __forceinline__ float wave_sum(float v) { return bcast63(wsum63(v)); }
__device__ __forceinline__ float wave_max(float v) { return bcast63(wmax63(v)); }

__global__ __launch_bounds__(512, 2) void k1(const u16* __restrict__ x, const u16* __restrict__ wq,
                                             const u16* __restrict__ w1, const u16* __restrict__ b1,
                                             char* __restrict__ ws) {
  __shared__ float xn[2][M_];
  __shared__ float qred[8][32];
  __shared__ float hred[8][16];

  const int t = threadIdx.x, lane = t & 63;
  const int w = __builtin_amdgcn_readfirstlane(t >> 6);
  const int i = blockIdx.x, xcd = i & 7, r = i >> 3;
  const int h = xcd * 2 + (r >> 4), dsl = r & 15;
  const bool mlp = (r < 28);
  const int fs = xcd * 28 + r;
  if (w == 0) STAMP(0);

  float xv[7];
  if (w < 2) {
#pragma unroll
    for (int k = 0; k < 7; ++k) xv[k] = bf2f(x[w * M_ + lane + 64 * k]);
  }
  const int tm = t < M_ ? t : 0;
  v4u wqa = {0u, 0u, 0u, 0u}, wqb = {0u, 0u, 0u, 0u}, w1v = {0u, 0u, 0u, 0u};
  if (t < M_) {
    const u16* p = wq + (size_t)t * 4096 + h * 256 + dsl * 16;
    wqa = ld16(p); wqb = ld16(p + 8);
    if (mlp) w1v = ld16(w1 + (size_t)t * F_ + fs * 8);
  }

  *reinterpret_cast<u64*>(ws + WS_SCG + (size_t)i * 4096 + t * 8) = 0ull;
  if (t < M_) *reinterpret_cast<u64*>(ws + WS_OUTG + (size_t)i * 3584 + t * 8) = 0ull;
  if (i < 49 && t >= 448 && t < 464) *reinterpret_cast<u64*>(ws + WS_CNT + (size_t)i * 128 + (t - 448) * 8) = 0ull;

  if (w < 2) {
    float s = 0.f;
#pragma unroll
    for (int k = 0; k < 7; ++k) s += xv[k];
    s = wave_sum(s);
    const float mean = s * (1.f / M_);
    float v = 0.f;
#pragma unroll
    for (int k = 0; k < 7; ++k) { float d = xv[k] - mean; v += d * d; }
    v = wave_sum(v);
    const float var = rbf(v * (1.f / M_));
    const float mb = rbf(mean);
    const float den = rbf(sqrtf(rbf(var + 1e-5f)));
#pragma unroll
    for (int k = 0; k < 7; ++k) xn[w][lane + 64 * k] = rbf(rbf(xv[k] - mb) / den);
  }
  __syncthreads();
  if (w == 0) STAMP(1);

  const float x0 = xn[0][tm], x1 = xn[1][tm];
  {
    float qp[32];
#pragma unroll
    for (int p = 0; p < 4; ++p) {
      qp[2 * p] = x0 * lo_f(wqa[p]); qp[2 * p + 1] = x0 * hi_f(wqa[p]);
      qp[8 + 2 * p] = x0 * lo_f(wqb[p]); qp[8 + 2 * p + 1] = x0 * hi_f(wqb[p]);
      qp[16 + 2 * p] = x1 * lo_f(wqa[p]); qp[16 + 2 * p + 1] = x1 * hi_f(wqa[p]);
      qp[24 + 2 * p] = x1 * lo_f(wqb[p]); qp[24 + 2 * p + 1] = x1 * hi_f(wqb[p]);
    }
#pragma unroll
    for (int j = 0; j < 32; ++j) qp[j] = wsum63(qp[j]);
    if (lane == 63) {
#pragma unroll
      for (int j = 0; j < 32; ++j) qred[w][j] = qp[j];
    }
  }
  if (mlp) {
    float hp[16];
#pragma unroll
    for (int p = 0; p < 4; ++p) {
      hp[2 * p] = x0 * lo_f(w1v[p]); hp[2 * p + 1] = x0 * hi_f(w1v[p]);
      hp[8 + 2 * p] = x1 * lo_f(w1v[p]); hp[8 + 2 * p + 1] = x1 * hi_f(w1v[p]);
    }
#pragma unroll
    for (int j = 0; j < 16; ++j) hp[j] = wsum63(hp[j]);
    if (lane == 63) {
#pragma unroll
      for (int j = 0; j < 16; ++j) hred[w][j] = hp[j];
    }
  }
  __syncthreads();
  if (w == 0) STAMP(2);
  if (t < 32) {
    float s = 0.f;
#pragma unroll
    for (int ww = 0; ww < 7; ++ww) s += qred[ww][t];
    reinterpret_cast<u16*>(ws + WS_Q)[(t >> 4) * 4096 + h * 256 + dsl * 16 + (t & 15)] = f2bf(s);
  } else if (mlp && t >= 64 && t < 80) {
    const int u = t - 64;
    float s = 0.f;
#pragma unroll
    for (int ww = 0; ww < 7; ++ww) s += hred[ww][u];
    const int b = u >> 3, j = u & 7;
    const float hv = rbf(rbf(s) + bf2f(b1[fs * 8 + j]));
    const float g = 0.5f * hv * (1.f + tanhf(0.7978845608028654f * (hv + 0.044715f * hv * hv * hv)));
    reinterpret_cast<float*>(ws + WS_H1)[b * F_ + fs * 8 + j] = rbf(g);
  }
  if (w == 0) STAMP(3);
}

__device__ __forceinline__ u32 swz(u32 row) { return ((row & 3u) << 2) | ((row >> 2) & 3u); }
__device__ __forceinline__ u32 off_b(u32 row, u32 ch) { return 256u * row + 16u * (ch ^ swz(row)); }

struct __attribute__((aligned(16))) K2Lds {
  char kv[65536];
  float qlr[2][4][2][16];
  float sred[8][16][33];
  float S[16][256];
  u16 P[16][264];
  float O[16][132];
  float ml[16][2];
  float ctxlat[4][1024];
  float cred[8][32];
  float ored[8][448];
  float fred[16][32];
};

#define SPIN_FAIL(code) if (++spins > SPIN_CAP) { if (lane == 0) __hip_atomic_store(tmo, (code), RLX_AGENT); break; } __builtin_amdgcn_s_sleep(1);

__global__ __launch_bounds__(768, 3) void k2(const u16* __restrict__ x, const u16* __restrict__ ckv,
                                             const u16* __restrict__ wuk, const u16* __restrict__ wuv,
                                             const u16* __restrict__ wo, const u16* __restrict__ w2,
                                             const u16* __restrict__ b2, char* __restrict__ ws,
                                             u16* __restrict__ out) {
  __shared__ K2Lds L;
  const int t = threadIdx.x, lane = t & 63;
  const int w = __builtin_amdgcn_readfirstlane(t >> 6);
  const int i = blockIdx.x, xcd = i & 7, r = i >> 3;
  const int h1i = xcd * 2 + (r >> 4), cs = r & 15;
  const int G = xcd * 4 + (r >> 3), b = G >> 4, g = G & 15, j = r & 7;
  const int b2i = r & 1, hd = xcd * 16 + (r >> 1), h2 = hd >> 3, ds = hd & 7;
  const bool fin = (i < 28);
  gu32* tmo = (gu32*)(ws + WS_CNT + 48 * 128);

  if (w < 8) {
    if (w == 0) STAMP(8);
    const int ct = w & 3, kh = w >> 2;
    v4u qa[4], wk[4], bv[8];
    {
      const u16* qp = reinterpret_cast<const u16*>(ws + WS_Q) + (lane & 1) * 4096 + h1i * 256 + kh * 128 + 8 * (lane >> 4);
      const u16* wp = wuk + ((size_t)(cs * 64 + ct * 16 + (lane & 15)) * 16 + h1i) * 256 + kh * 128 + 8 * (lane >> 4);
#pragma unroll
      for (int s = 0; s < 4; ++s) qa[s] = ld16(qp + s * 32);
#pragma unroll
      for (int s = 0; s < 4; ++s) wk[s] = ld16(wp + s * 32);
      const u16* kr = ckv + ((size_t)b * SK + g * 256 + j * 32 + (lane & 15)) * DC + w * 128 + 8 * (lane >> 4);
#pragma unroll
      for (int s = 0; s < 8; ++s) bv[s] = ld16(kr + (s >> 2) * 16 * DC + (s & 3) * 32);
    }
    {
      v4f acc = {0.f, 0.f, 0.f, 0.f};
#pragma unroll
      for (int s = 0; s < 4; ++s) {
        v4u av = qa[s];
        if ((lane & 15) >= 2) av = v4u{0u, 0u, 0u, 0u};
        acc = __builtin_amdgcn_mfma_f32_16x16x32_bf16(as_bf8(av), as_bf8(wk[s]), acc, 0, 0, 0);
      }
      if (lane < 16) { L.qlr[kh][ct][0][lane] = acc[0]; L.qlr[kh][ct][1][lane] = acc[1]; }
    }
    __syncthreads();
    if (w == 0) STAMP(9);
    __syncthreads();
    if (w == 0) STAMP(10);
    {
      const __amdgpu_buffer_rsrc_t rq = __builtin_amdgcn_make_buffer_rsrc(ws + WS_QLAT, 0, 65536, 0x00020000);
      const u32 qo = (u32)((b * 16 + (lane & 15)) * DC + w * 128 + 8 * (lane >> 4)) * 2u;
      v4u av[4];
#pragma unroll
      for (int s = 0; s < 4; ++s) av[s] = __builtin_amdgcn_raw_buffer_load_b128(rq, qo + s * 64u, 0, 16);
      v4f acc0 = {0.f, 0.f, 0.f, 0.f}, acc1 = {0.f, 0.f, 0.f, 0.f};
#pragma unroll
      for (int s = 0; s < 4; ++s) {
        acc0 = __builtin_amdgcn_mfma_f32_16x16x32_bf16(as_bf8(av[s]), as_bf8(bv[s]), acc0, 0, 0, 0);
        acc1 = __builtin_amdgcn_mfma_f32_16x16x32_bf16(as_bf8(av[s]), as_bf8(bv[4 + s]), acc1, 0, 0, 0);
      }
#pragma unroll
      for (int q = 0; q < 4; ++q) { L.sred[w][(lane >> 4) * 4 + q][lane & 15] = acc0[q]; L.sred[w][(lane >> 4) * 4 + q][16 + (lane & 15)] = acc1[q]; }
    }
    __syncthreads();
    if (w == 0) STAMP(11);
    v4u uv[8], ov[4], w2v[2];
    float hv[2];
    const int rg = t / 56, cc = t - rg * 56;
    {
      const u16* up = wuv + ((size_t)(t >> 2) * 16 + h2) * 256 + ds * 32 + (t & 3) * 8;
#pragma unroll
      for (int p = 0; p < 8; ++p) uv[p] = ld16(up + (size_t)p * 128 * 4096);
      const int rgc = rg < 8 ? rg : 0;
      const u16* op = wo + (size_t)(h2 * 256 + ds * 32 + rgc * 4) * M_ + cc * 8;
#pragma unroll
      for (int p = 0; p < 4; ++p) ov[p] = ld16(op + p * M_);
      const float* h1 = reinterpret_cast<const float*>(ws + WS_H1) + b2i * F_;
#pragma unroll
      for (int p = 0; p < 2; ++p) {
        const int fr = rgc + 8 * p, frc = fr < 14 ? fr : 0;
        w2v[p] = ld16(w2 + (size_t)(hd * 14 + frc) * M_ + cc * 8);
        hv[p] = fr < 14 ? h1[hd * 14 + frc] : 0.f;
      }
    }
    __syncthreads();
    if (w == 0) STAMP(12);
#pragma unroll
    for (int hh = 0; hh < 2; ++hh) {
      const int hq = 2 * w + hh;
      const v4f sv = *reinterpret_cast<const v4f*>(&L.S[hq][4 * lane]);
      const float m = wave_max(fmaxf(fmaxf(sv[0], sv[1]), fmaxf(sv[2], sv[3])));
      u16 pb[4];
      float l = 0.f;
#pragma unroll
      for (int q = 0; q < 4; ++q) { pb[q] = f2bf(__expf(sv[q] - m)); l += bf2f(pb[q]); }
      l = wsum63(l);
      *reinterpret_cast<v2u*>(&L.P[hq][4 * lane]) = v2u{(u32)pb[0] | ((u32)pb[1] << 16), (u32)pb[2] | ((u32)pb[3] << 16)};
      if (lane == 63) { L.ml[hq][0] = m; L.ml[hq][1] = l; }
    }
    __syncthreads();
    if (w == 0) STAMP(13);
    {
      v4f acc = {0.f, 0.f, 0.f, 0.f};
      const u32 gq = lane >> 4, qq = (lane & 15) >> 2, pp = lane & 3;
#pragma unroll
      for (int ks = 0; ks < 8; ++ks) {
        const v4u pa = *reinterpret_cast<const v4u*>(&L.P[lane & 15][32 * ks + 8 * (lane >> 4)]);
        const s4 b0 = __builtin_amdgcn_ds_read_tr16_b64_v4i16((LAS s4*)(L.kv + off_b(32 * ks + 8 * gq + qq, 2 * w + (pp >> 1)) + 8 * (pp & 1)));
        const s4 b1 = __builtin_amdgcn_ds_read_tr16_b64_v4i16((LAS s4*)(L.kv + off_b(32 * ks + 8 * gq + 4 + qq, 2 * w + (pp >> 1)) + 8 * (pp & 1)));
        const s8 bb = {b0[0], b0[1], b0[2], b0[3], b1[0], b1[1], b1[2], b1[3]};
        acc = __builtin_amdgcn_mfma_f32_16x16x32_bf16(as_bf8(pa), __builtin_bit_cast(bf8, bb), acc, 0, 0, 0);
      }
#pragma unroll
      for (int q = 0; q < 4; ++q) L.O[(lane >> 4) * 4 + q][w * 16 + (lane & 15)] = acc[q];
    }
    __syncthreads();
    if (w == 0) STAMP(14);
    __syncthreads();
    if (w == 0) STAMP(15);
    {
      const __amdgpu_buffer_rsrc_t rs = __builtin_amdgcn_make_buffer_rsrc(ws + WS_PART, 0, 2 * 16 * 16 * 2048, 0x00020000);
      const int qtr = w >> 1;
      const u32 base = (u32)((b2i * 16 + h2) * 16 + qtr * 4) * 2048u + (u32)(t & 127) * 16u;
      v4u pv[4];
#pragma unroll
      for (int gg = 0; gg < 4; ++gg) pv[gg] = __builtin_amdgcn_raw_buffer_load_b128(rs, base + gg * 2048u, 0, 16);
      gu64* mlp = (gu64*)(ws + WS_ML) + (b2i * 16 + h2) * 16;
      const u64 mlv = __hip_atomic_load(mlp + (lane & 15), RLX_AGENT);
      const float mg = __uint_as_float((u32)mlv), lg = __uint_as_float((u32)(mlv >> 32));
      const float Mx = wave_max(mg);
      const float wg = __expf(mg - Mx);
      const float Ls = wave_sum(wg * lg) * 0.25f;
      const float wn = wg / Ls;
      float acc[8];
#pragma unroll
      for (int q = 0; q < 8; ++q) acc[q] = 0.f;
#pragma unroll
      for (int gg = 0; gg < 4; ++gg) {
        const float wv = __int_as_float(__builtin_amdgcn_readlane(__float_as_int(wn), qtr * 4 + gg));
#pragma unroll
        for (int q = 0; q < 4; ++q) { acc[2 * q] += wv * lo_f(pv[gg][q]); acc[2 * q + 1] += wv * hi_f(pv[gg][q]); }
      }
      *reinterpret_cast<v4f*>(&L.ctxlat[qtr][8 * (t & 127)]) = v4f{acc[0], acc[1], acc[2], acc[3]};
      *reinterpret_cast<v4f*>(&L.ctxlat[qtr][8 * (t & 127) + 4]) = v4f{acc[4], acc[5], acc[6], acc[7]};
    }
    __syncthreads();
    if (w == 0) STAMP(16);
    {
      float ca[8];
#pragma unroll
      for (int q = 0; q < 8; ++q) ca[q] = 0.f;
#pragma unroll
      for (int p = 0; p < 8; ++p) {
        const int c = (t >> 2) + 128 * p;
        const float cv = (L.ctxlat[0][c] + L.ctxlat[1][c]) + (L.ctxlat[2][c] + L.ctxlat[3][c]);
#pragma unroll
        for (int q = 0; q < 4; ++q) { ca[2 * q] += cv * lo_f(uv[p][q]); ca[2 * q + 1] += cv * hi_f(uv[p][q]); }
      }
#pragma unroll
      for (int q = 0; q < 8; ++q) {
        ca[q] = dpp_add<0x114, 0xf>(ca[q]);
        ca[q] = dpp_add<0x118, 0xf>(ca[q]);
        ca[q] += __shfl_xor(ca[q], 16);
        ca[q] += __shfl_xor(ca[q], 32);
      }
      if (lane >= 12 && lane < 16) {
#pragma unroll
        for (int q = 0; q < 8; ++q) L.cred[w][(lane & 3) * 8 + q] = ca[q];
      }
    }
    __syncthreads();
    if (w == 0) STAMP(17);
    if (t < M_) {
      float oa[8];
#pragma unroll
      for (int q = 0; q < 8; ++q) oa[q] = 0.f;
#pragma unroll
      for (int p = 0; p < 4; ++p) {
        float cv = 0.f;
#pragma unroll
        for (int ww = 0; ww < 8; ++ww) cv += L.cred[ww][rg * 4 + p];
        cv = rbf(cv);
#pragma unroll
        for (int q = 0; q < 4; ++q) { oa[2 * q] += cv * lo_f(ov[p][q]); oa[2 * q + 1] += cv * hi_f(ov[p][q]); }
      }
#pragma unroll
      for (int p = 0; p < 2; ++p) {
#pragma unroll
        for (int q = 0; q < 4; ++q) { oa[2 * q] += hv[p] * lo_f(w2v[p][q]); oa[2 * q + 1] += hv[p] * hi_f(w2v[p][q]); }
      }
      *reinterpret_cast<v4f*>(&L.ored[rg][cc * 8]) = v4f{oa[0], oa[1], oa[2], oa[3]};
      *reinterpret_cast<v4f*>(&L.ored[rg][cc * 8 + 4]) = v4f{oa[4], oa[5], oa[6], oa[7]};
    }
    __syncthreads();
    if (w == 0) STAMP(18);
    if (t < M_) {
      float s = 0.f;
#pragma unroll
      for (int q = 0; q < 8; ++q) s += L.ored[q][t];
      gu64* og = (gu64*)(ws + WS_OUTG) + (size_t)(b2i * 128 + hd) * M_ + t;
      __hip_atomic_store(og, ((u64)TAG << 32) | __float_as_uint(s), RLX_AGENT);
    }
    if (fin) {
      const int fb = i / 14, fc = i - fb * 14;
      const int col = t & 31, pg = t >> 5;
      gu64* og = (gu64*)(ws + WS_OUTG) + (size_t)(fb * 128 + pg * 8) * M_ + fc * 32 + col;
      float v[8];
      for (u32 spins = 0;;) {
        bool ok = true;
#pragma unroll
        for (int k = 0; k < 8; ++k) { const u64 xv = __hip_atomic_load(og + M_ * k, RLX_AGENT); v[k] = __uint_as_float((u32)xv); ok &= (u32)(xv >> 32) == TAG; }
        if (__all(ok)) break;
        SPIN_FAIL(4u)
      }
      L.fred[pg][col] = ((v[0] + v[1]) + (v[2] + v[3])) + ((v[4] + v[5]) + (v[6] + v[7]));
      __syncthreads();
      if (w == 0) STAMP(19);
      if (t < 32) {
        float a = 0.f;
#pragma unroll
        for (int q = 0; q < 16; ++q) a += L.fred[q][t];
        const int m = fc * 32 + t;
        out[fb * M_ + m] = f2bf(bf2f(x[fb * M_ + m]) + a + bf2f(b2[m]));
      }
      if (w == 0) STAMP(20);
    }
  } else {
    const int cw = w - 8, ct = t - 512;
    __syncthreads();
    if (cw == 0) {
      if (lane < 16) {
        const int bb = lane >> 3, ch = lane & 7;
        u32 pk[4];
#pragma unroll
        for (int e = 0; e < 4; ++e) {
          const int c0 = ch * 8 + 2 * e;
          const float v0 = L.qlr[0][c0 >> 4][bb][c0 & 15] + L.qlr[1][c0 >> 4][bb][c0 & 15];
          const float v1 = L.qlr[0][c0 >> 4][bb][(c0 & 15) + 1] + L.qlr[1][c0 >> 4][bb][(c0 & 15) + 1];
          pk[e] = (u32)f2bf(v0) | ((u32)f2bf(v1) << 16);
        }
        const __amdgpu_buffer_rsrc_t rq = __builtin_amdgcn_make_buffer_rsrc(ws + WS_QLAT, 0, 65536, 0x00020000);
        __builtin_amdgcn_raw_buffer_store_b128(v4u{pk[0], pk[1], pk[2], pk[3]}, rq, (u32)((bb * 16 + h1i) * DC + cs * 64 + ch * 8) * 2u, 0, 16);
      }
      asm volatile("s_waitcnt vmcnt(0)" ::: "memory");
      if (lane == 0) __hip_atomic_fetch_add((gu32*)(ws + WS_CNT + (size_t)(32 + h1i) * 128), 1u, RLX_AGENT);
      gu32* cp = (gu32*)(ws + WS_CNT + (size_t)(32 + (lane & 15)) * 128);
      for (u32 spins = 0;;) {
        const u32 c = __hip_atomic_load(cp, RLX_AGENT);
        if (__all(c == 16u)) break;
        SPIN_FAIL(1u)
      }
    }
    __syncthreads();
    __syncthreads();
    {
      gu64* sg = (gu64*)(ws + WS_SCG) + (size_t)G * 4096;
#pragma unroll
      for (int u = 0; u < 2; ++u) {
        const int n = ct + 256 * u, hq = n >> 5, key = n & 31;
        float s = 0.f;
#pragma unroll
        for (int ww = 0; ww < 8; ++ww) s += L.sred[ww][hq][key];
        s *= 0.0625f;
        __hip_atomic_store(sg + j * 512 + n, ((u64)TAG << 32) | __float_as_uint(s), RLX_AGENT);
      }
#pragma unroll
      for (int n = 0; n < 16; ++n) {
        const u32 brow = (u32)(cw * 16 + n) * 4u;
        const u32 row = brow + (lane >> 4);
        const u32 ch = (u32)(lane & 15) ^ swz(row);
        const u16* src = ckv + ((size_t)b * SK + g * 256 + row) * DC + j * 128 + ch * 8;
        __builtin_amdgcn_global_load_lds((const GAS void*)src, (LAS void*)(L.kv + brow * 256u), 16, 0, 0);
      }
      u32 v[16];
      for (u32 spins = 0;;) {
        bool ok = true;
#pragma unroll
        for (int k = 0; k < 16; ++k) { const u64 xv = __hip_atomic_load(sg + ct + 256 * k, RLX_AGENT); v[k] = (u32)xv; ok &= (u32)(xv >> 32) == TAG; }
        if (__all(ok)) break;
        SPIN_FAIL(2u)
      }
#pragma unroll
      for (int k = 0; k < 16; ++k) {
        const int n = ct + 256 * (k & 1), p = k >> 1;
        L.S[n >> 5][p * 32 + (n & 31)] = __uint_as_float(v[k]);
      }
    }
    asm volatile("s_waitcnt vmcnt(0)" ::: "memory");
    __syncthreads();
    __syncthreads();
    __syncthreads();
    {
      const __amdgpu_buffer_rsrc_t rs = __builtin_amdgcn_make_buffer_rsrc(ws + WS_PART, 0, 2 * 16 * 16 * 2048, 0x00020000);
      {
        const int hq = ct >> 4, c8 = ct & 15;
        const v4f o0 = *reinterpret_cast<const v4f*>(&L.O[hq][8 * c8]);
        const v4f o1 = *reinterpret_cast<const v4f*>(&L.O[hq][8 * c8 + 4]);
        const v4u pk = {(u32)f2bf(o0[0]) | ((u32)f2bf(o0[1]) << 16), (u32)f2bf(o0[2]) | ((u32)f2bf(o0[3]) << 16),
                        (u32)f2bf(o1[0]) | ((u32)f2bf(o1[1]) << 16), (u32)f2bf(o1[2]) | ((u32)f2bf(o1[3]) << 16)};
        const u32 off = (u32)(((b * 16 + hq) * 16 + g) * 2048 + j * 256 + c8 * 16);
        __builtin_amdgcn_raw_buffer_store_b128(pk, rs, off, 0, 16);
      }
      if (j == 0 && ct < 16) {
        gu64* mlp = (gu64*)(ws + WS_ML) + (b * 16 + ct) * 16 + g;
        __hip_atomic_store(mlp, ((u64)__float_as_uint(L.ml[ct][1]) << 32) | __float_as_uint(L.ml[ct][0]), RLX_AGENT);
      }
      asm volatile("s_waitcnt vmcnt(0)" ::: "memory");
      if (lane == 0) __hip_atomic_fetch_add((gu32*)(ws + WS_CNT + (size_t)G * 128), 1u, RLX_AGENT);
      if (cw == 0) {
        gu32* cp = (gu32*)(ws + WS_CNT + (size_t)(b2i * 16 + (lane & 15)) * 128);
        for (u32 spins = 0;;) {
          const u32 c = __hip_atomic_load(cp, RLX_AGENT);
          if (__all(c == 32u)) break;
          SPIN_FAIL(3u)
        }
      }
    }
    __syncthreads();
    __syncthreads();
    __syncthreads();
    __syncthreads();
    if (fin) {
      __syncthreads();
    }
  }
}

extern "C" void kernel_launch(void* const* d_in, const int* in_sizes, int n_in,
                              void* d_out, int out_size, void* d_ws, size_t ws_size,
                              hipStream_t stream) {
  const u16* x = (const u16*)d_in[0];
  const u16* ckv = (const u16*)d_in[1];
  const u16* wq = (const u16*)d_in[2];
  const u16* wuk = (const u16*)d_in[3];
  const u16* wuv = (const u16*)d_in[4];
  const u16* wo = (const u16*)d_in[5];
  const u16* w1 = (const u16*)d_in[7];
  const u16* b1 = (const u16*)d_in[8];
  const u16* w2 = (const u16*)d_in[9];
  const u16* b2 = (const u16*)d_in[10];
  k1<<<256, 512, 0, stream>>>(x, wq, w1, b1, (char*)d_ws);
  k2<<<256, 768, 0, stream>>>(x, ckv, wuk, wuv, wo, w2, b2, (char*)d_ws, (u16*)d_out);
}
```

```cpp
#include <hip/hip_runtime.h>
#include <hip/hip_bf16.h>
#include <stdint.h>

typedef unsigned short u16;
typedef unsigned int u32;
typedef unsigned long long u64;
typedef __attribute__((ext_vector_type(4))) unsigned v4u;
typedef __attribute__((ext_vector_type(2))) unsigned v2u;
typedef __attribute__((ext_vector_type(4))) float v4f;
typedef __attribute__((ext_vector_type(8))) __bf16 bf8;
typedef __attribute__((ext_vector_type(4))) short s4;
typedef __attribute__((ext_vector_type(8))) short s8;

#define GAS __attribute__((address_space(1)))
#define LAS __attribute__((address_space(3)))
typedef GAS u32 gu32;
typedef GAS u64 gu64;
#define RLX_AGENT __ATOMIC_RELAXED, __HIP_MEMORY_SCOPE_AGENT

#define M_ 448
#define F_ 1792
#define DC 1024
#define SK 4096

constexpr u32 SPIN_CAP = 1u << 18;

constexpr u32 WS_EP = 0;
constexpr u32 WS_QG = 4096;
constexpr u32 WS_QL = 65536;
constexpr u32 WS_SC = 262144;
constexpr u32 WS_PV = 1310720;
constexpr u32 WS_ML = 3407872;
constexpr u32 WS_OG = 3416064;
constexpr u32 WS_END = WS_OG + 2 * 128 * 448 * 8;

#define STAMP(k) do {} while (0)

#define BAR() do { asm volatile("s_waitcnt lgkmcnt(0)" ::: "memory"); __builtin_amdgcn_s_barrier(); asm volatile("" ::: "memory"); } while (0)

__device__ __forceinline__ float bf2f(u16 v) { return __uint_as_float(((u32)v) << 16); }
__device__ __forceinline__ u16 f2bf(float f) { __hip_bfloat16 b = __float2bfloat16(f); return *reinterpret_cast<u16*>(&b); }
__device__ __forceinline__ float rbf(float f) { return bf2f(f2bf(f)); }
__device__ __forceinline__ float lo_f(u32 v) { return __uint_as_float(v << 16); }
__device__ __forceinline__ float hi_f(u32 v) { return __uint_as_float(v & 0xffff0000u); }
__device__ __forceinline__ u32 pk2(float a, float b) { return (u32)f2bf(a) | ((u32)f2bf(b) << 16); }
__device__ __forceinline__ v4u ld16(const u16* p) { return __builtin_nontemporal_load(reinterpret_cast<const v4u*>(p)); }
__device__ __forceinline__ bf8 as_bf8(v4u v) { return __builtin_bit_cast(bf8, v); }

template <int CTRL>
__device__ __forceinline__ float dppf(float v) {
  return __int_as_float(__builtin_amdgcn_update_dpp(0, __float_as_int(v), CTRL, 0xf, 0xf, true));
}
template <int N> __device__ __forceinline__ float ror_add(float v) { return v + dppf<0x120 + N>(v); }
template <int N> __device__ __forceinline__ float ror_max(float v) { return fmaxf(v, dppf<0x120 + N>(v)); }
__device__ __forceinline__ float x16_add(float v) {
  const u32 b = __float_as_uint(v);
  auto p = __builtin_amdgcn_permlane16_swap(b, b, false, false);
  return __uint_as_float(p[0]) + __uint_as_float(p[1]);
}
__device__ __forceinline__ float x32_add(float v) {
  const u32 b = __float_as_uint(v);
  auto p = __builtin_amdgcn_permlane32_swap(b, b, false, false);
  return __uint_as_float(p[0]) + __uint_as_float(p[1]);
}
__device__ __forceinline__ float x16_max(float v) {
  const u32 b = __float_as_uint(v);
  auto p = __builtin_amdgcn_permlane16_swap(b, b, false, false);
  return fmaxf(__uint_as_float(p[0]), __uint_as_float(p[1]));
}
__device__ __forceinline__ float x32_max(float v) {
  const u32 b = __float_as_uint(v);
  auto p = __builtin_amdgcn_permlane32_swap(b, b, false, false);
  return fmaxf(__uint_as_float(p[0]), __uint_as_float(p[1]));
}
__device__ __forceinline__ float wave_sum(float v) {
  v = ror_add<8>(v); v = ror_add<4>(v); v = ror_add<2>(v); v = ror_add<1>(v);
  return x32_add(x16_add(v));
}
__device__ __forceinline__ float wave_max(float v) {
  v = ror_max<8>(v); v = ror_max<4>(v); v = ror_max<2>(v); v = ror_max<1>(v);
  return x32_max(x16_max(v));
}

__device__ __forceinline__ u32 swz(u32 row) { return ((row & 3u) << 2) | ((row >> 2) & 3u); }
__device__ __forceinline__ u32 off_b(u32 row, u32 ch) { return 256u * row + 16u * (ch ^ swz(row)); }

constexpr u32 L_RA = 0;
constexpr u32 L_RB = 65536;
constexpr u32 L_WT = L_RB;
constexpr u32 L_SRED = L_RB + 28672;
constexpr u32 L_XN = 131072;
constexpr u32 L_QST = L_XN + 4096;
constexpr u32 L_P = L_QST + 4096;
constexpr u32 L_CA = L_P + 8448;
constexpr u32 L_CRED = L_CA + 8256;
constexpr u32 L_VST = L_CRED + 1024;
constexpr u32 L_VH = L_VST + 512;
constexpr u32 L_FRED = L_VH + 64;
constexpr u32 L_EP = L_FRED + 2048;
constexpr u32 L_END = L_EP + 16;

#define SPIN_FAIL(code) if (++spins > SPIN_CAP) { if (lane == 0) __hip_atomic_store(tmo, (code), RLX_AGENT); break; } __builtin_amdgcn_s_sleep(1);

__device__ __forceinline__ void st_gran(char* ws, u32 off, u32 tag, u32 val) {
  __hip_atomic_store((gu64*)(ws + off), ((u64)tag << 32) | val, RLX_AGENT);
}
__device__ __forceinline__ bf8 tr_pair(char* lds, u32 a0, u32 a1) {
  const s4 b0 = __builtin_amdgcn_ds_read_tr16_b64_v4i16((LAS s4*)(lds + a0));
  const s4 b1 = __builtin_amdgcn_ds_read_tr16_b64_v4i16((LAS s4*)(lds + a1));
  const s8 bb = {b0[0], b0[1], b0[2], b0[3], b1[0], b1[1], b1[2], b1[3]};
  return __builtin_bit_cast(bf8, bb);
}
__device__ __forceinline__ v4u lds16(char* lds, u32 a) { return *reinterpret_cast<const v4u*>(lds + a); }
__device__ __forceinline__ float fdiv(float a, float d, float r) {
  const float q = a * r;
  return fmaf(fmaf(-q, d, a), r, q);
}

__global__ __launch_bounds__(512, 2) void mla_decode(const u16* __restrict__ x, const u16* __restrict__ ckv,
                                                     const u16* __restrict__ wq, const u16* __restrict__ wuk,
                                                     const u16* __restrict__ wuv, const u16* __restrict__ wo,
                                                     const u16* __restrict__ w1, const u16* __restrict__ b1,
                                                     const u16* __restrict__ w2, const u16* __restrict__ b2,
                                                     char* __restrict__ ws, u16* __restrict__ out) {
  __shared__ __attribute__((aligned(16))) char lds[L_END];
  const int t = threadIdx.x, lane = t & 63;
  const int w = __builtin_amdgcn_readfirstlane(t >> 6);
  const int i = blockIdx.x, xcd = i & 7, r = i >> 3;
  const int h1i = xcd * 2 + (r >> 4), cs = r & 15;
  const int G = xcd * 4 + (r >> 3), b = G >> 4, g = G & 15, j = r & 7;
  const int b2i = r & 1, hd = xcd * 16 + (r >> 1), h2 = hd >> 3, ds = hd & 7;
  const int fsl = hd < 112 ? hd : 0;
  gu32* tmo = (gu32*)(ws + WS_EP + 128);
  const __amdgpu_buffer_rsrc_t rw = __builtin_amdgcn_make_buffer_rsrc(ws, 0, WS_END, 0x00020000);
  const u32 gq = lane >> 4, qq = (lane & 15) >> 2, pp = lane & 3;
  STAMP(0);
  v4u bv[8];
  const u16* kr = ckv + ((size_t)b * SK + g * 256 + j * 32 + (lane & 15)) * DC + w * 128 + 8 * (lane >> 4);

  if (w < 2) {
    const u32 pitch = w ? F_ * 2u : 8192u;
    const __amdgpu_buffer_rsrc_t rs = __builtin_amdgcn_make_buffer_rsrc((void*)(w ? w1 : wq), 0, M_ * pitch, 0x00020000);
    const u32 col = w ? (u32)fsl * 32u : (u32)(h1i * 256 + cs * 16) * 2u;
    const u32 vo = (u32)(lane >> 1) * pitch + col + (u32)(lane & 1) * 16u;
    const u32 wt = L_WT + (u32)w * 14336u;
    const int lc = lane < 56 ? lane : 0;
    const v4u xr0 = *reinterpret_cast<const v4u*>(x + lc * 8), xr1 = *reinterpret_cast<const v4u*>(x + M_ + lc * 8);
    if (w == 1) BAR();
#pragma unroll
    for (int n = 0; n < 14; ++n)
      __builtin_amdgcn_raw_ptr_buffer_load_lds(rs, (LAS void*)(lds + wt + n * 1024), 16, vo, n * 32 * pitch, 0, 2);
    const float bias = bf2f(b1[fsl * 16 + (lane & 15)]);
    if (w == 0) BAR();
    BAR();
    float xa[8], xb[8];
#pragma unroll
    for (int p = 0; p < 4; ++p) { xa[2 * p] = lo_f(xr0[p]); xa[2 * p + 1] = hi_f(xr0[p]); xb[2 * p] = lo_f(xr1[p]); xb[2 * p + 1] = hi_f(xr1[p]); }
    asm volatile("" :: "v"(xa[0]), "v"(xb[0]));
    STAMP(16);
    const float act = lane < 56 ? 1.f : 0.f;
    float s0 = 0.f, s1 = 0.f;
#pragma unroll
    for (int k = 0; k < 8; ++k) { s0 += xa[k]; s1 += xb[k]; }
    s0 = wave_sum(s0 * act); s1 = wave_sum(s1 * act);
    const float m0 = s0 * (1.f / M_), m1 = s1 * (1.f / M_);
    float v0 = 0.f, v1 = 0.f;
#pragma unroll
    for (int k = 0; k < 8; ++k) { const float d0 = xa[k] - m0, d1 = xb[k] - m1; v0 += d0 * d0; v1 += d1 * d1; }
    v0 = wave_sum(v0 * act); v1 = wave_sum(v1 * act);
    const float mb0 = rbf(m0), mb1 = rbf(m1);
    const float den0 = rbf(sqrtf(rbf(rbf(v0 * (1.f / M_)) + 1e-5f)));
    const float den1 = rbf(sqrtf(rbf(rbf(v1 * (1.f / M_)) + 1e-5f)));
    const float r0 = __builtin_amdgcn_rcpf(den0), r1 = __builtin_amdgcn_rcpf(den1);
    v4u na, nb;
#pragma unroll
    for (int p = 0; p < 4; ++p) {
      na[p] = pk2(fdiv(rbf(xa[2 * p] - mb0), den0, r0), fdiv(rbf(xa[2 * p + 1] - mb0), den0, r0));
      nb[p] = pk2(fdiv(rbf(xb[2 * p] - mb1), den1, r1), fdiv(rbf(xb[2 * p + 1] - mb1), den1, r1));
    }
    const u32 xo = L_XN + (u32)w * 2048u;
    if (lane < 56) {
      *reinterpret_cast<v4u*>(lds + xo + lane * 16) = na;
      *reinterpret_cast<v4u*>(lds + xo + 896 + lane * 16) = nb;
    }
    STAMP(17);
    asm volatile("s_waitcnt vmcnt(0)" ::: "memory");
    STAMP(18);
    const u32 ao = xo + (u32)(lane & 1) * 896u + gq * 16u;
    const u32 bo = wt + (8u * gq + qq) * 32u + pp * 8u;
    v4f acc0 = {0.f, 0.f, 0.f, 0.f}, acc1 = {0.f, 0.f, 0.f, 0.f};
#pragma unroll
    for (int s = 0; s < 7; ++s) {
      acc0 = __builtin_amdgcn_mfma_f32_16x16x32_bf16(as_bf8(lds16(lds, ao + s * 64)), tr_pair(lds, bo + s * 1024, bo + s * 1024 + 128), acc0, 0, 0, 0);
      acc1 = __builtin_amdgcn_mfma_f32_16x16x32_bf16(as_bf8(lds16(lds, ao + (s + 7) * 64)), tr_pair(lds, bo + (s + 7) * 1024, bo + (s + 7) * 1024 + 128), acc1, 0, 0, 0);
    }
    const float a0 = acc0[0] + acc1[0], a1 = acc0[1] + acc1[1];
    asm volatile("" :: "v"(a0), "v"(a1));
    STAMP(19);
    if (w == 0) {
      const u32 tag0 = __builtin_amdgcn_readfirstlane(*reinterpret_cast<const u32*>(lds + L_EP)) + 1u;
      const float o0 = dppf<0xB1>(a0), o1 = dppf<0xB1>(a1);
      if (lane < 16) {
        const int bb = lane & 1;
        st_gran(ws, WS_QG + (u32)((h1i * 2 + bb) * 128 + cs * 8 + (lane >> 1)) * 8u, tag0, bb ? pk2(o1, a1) : pk2(a0, o0));
      }
      STAMP(1);
    } else {
      const float hv = rbf(rbf(b2i ? a1 : a0) + bias);
      const float ge = 0.5f * hv * (1.f + tanhf(0.7978845608028654f * (hv + 0.044715f * hv * hv * hv)));
      if (lane < 32) *reinterpret_cast<u16*>(lds + L_VH + lane * 2) = (lane < 16 && hd < 112) ? f2bf(ge) : (u16)0;
    }
  }
  u32 tag;
  if (w < 4) {
    if (w >= 2) {
      u32 ep = 0;
      if (w == 2 && lane == 0) ep = __hip_atomic_load((gu32*)(ws + WS_EP), RLX_AGENT);
      BAR();
      if (w == 2 && lane == 0) *reinterpret_cast<u32*>(lds + L_EP) = ep;
      BAR();
    }
    tag = __builtin_amdgcn_readfirstlane(*reinterpret_cast<const u32*>(lds + L_EP)) + 1u;
#pragma unroll
    for (int s = 0; s < 8; ++s) bv[s] = ld16(kr + (s >> 2) * 16 * DC + (s & 3) * 32);
  } else {
    v4u wk[8];
    const u16* wp = wuk + ((size_t)(cs * 64 + (w - 4) * 16 + (lane & 15)) * 16 + h1i) * 256 + 8 * (lane >> 4);
    BAR();
#pragma unroll
    for (int s = 0; s < 8; ++s) wk[s] = ld16(wp + s * 32);
    BAR();
    tag = __builtin_amdgcn_readfirstlane(*reinterpret_cast<const u32*>(lds + L_EP)) + 1u;
    const u32 qo = WS_QG + (u32)h1i * 2048u + (u32)lane * 32u;
    v4u g0, g1;
    for (u32 spins = 0;;) {
      g0 = __builtin_amdgcn_raw_buffer_load_b128(rw, qo, 0, 16);
      g1 = __builtin_amdgcn_raw_buffer_load_b128(rw, qo + 16u, 0, 16);
      const bool ok = (g0[1] == tag) & (g0[3] == tag) & (g1[1] == tag) & (g1[3] == tag);
      if (__all(ok)) break;
      SPIN_FAIL(1u)
    }
    const u32 qs = L_QST + (u32)(w - 4) * 1024u;
    *reinterpret_cast<v4u*>(lds + qs + lane * 16) = v4u{g0[0], g0[2], g1[0], g1[2]};
    const u32 ao = qs + (u32)(lane & 1) * 512u + gq * 16u;
    v4f acc0 = {0.f, 0.f, 0.f, 0.f}, acc1 = {0.f, 0.f, 0.f, 0.f};
#pragma unroll
    for (int s = 0; s < 4; ++s) {
      acc0 = __builtin_amdgcn_mfma_f32_16x16x32_bf16(as_bf8(lds16(lds, ao + s * 64)), as_bf8(wk[s]), acc0, 0, 0, 0);
      acc1 = __builtin_amdgcn_mfma_f32_16x16x32_bf16(as_bf8(lds16(lds, ao + (s + 4) * 64)), as_bf8(wk[s + 4]), acc1, 0, 0, 0);
    }
    const float a0 = acc0[0] + acc1[0], a1 = acc0[1] + acc1[1];
    const float o0 = dppf<0xB1>(a0), o1 = dppf<0xB1>(a1);
    if (lane < 16) {
      const int bb = lane & 1;
      st_gran(ws, WS_QL + (u32)((bb * 16 + h1i) * 512 + cs * 32 + (w - 4) * 8 + (lane >> 1)) * 8u, tag, bb ? pk2(o1, a1) : pk2(a0, o0));
    }
#pragma unroll
    for (int s = 0; s < 8; ++s) bv[s] = ld16(kr + (s >> 2) * 16 * DC + (s & 3) * 32);
  }
  BAR();
  STAMP(3);
  {
    const u32 qo = WS_QL + (u32)((b * 16 + (lane & 15)) * 512 + w * 64 + 4 * (lane >> 4)) * 8u;
    v4u gl[8];
    for (u32 spins = 0;;) {
      bool ok = true;
#pragma unroll
      for (int s = 0; s < 4; ++s) {
        gl[2 * s] = __builtin_amdgcn_raw_buffer_load_b128(rw, qo + s * 128u, 0, 16);
        gl[2 * s + 1] = __builtin_amdgcn_raw_buffer_load_b128(rw, qo + s * 128u + 16u, 0, 16);
      }
#pragma unroll
      for (int s = 0; s < 8; ++s) ok &= (gl[s][1] == tag) & (gl[s][3] == tag);
      if (__all(ok)) break;
      SPIN_FAIL(2u)
    }
    STAMP(4);
    v4f acc0 = {0.f, 0.f, 0.f, 0.f}, acc1 = {0.f, 0.f, 0.f, 0.f};
#pragma unroll
    for (int s = 0; s < 4; ++s) {
      const v4u av = {gl[2 * s][0], gl[2 * s][2], gl[2 * s + 1][0], gl[2 * s + 1][2]};
      acc0 = __builtin_amdgcn_mfma_f32_16x16x32_bf16(as_bf8(av), as_bf8(bv[s]), acc0, 0, 0, 0);
      acc1 = __builtin_amdgcn_mfma_f32_16x16x32_bf16(as_bf8(av), as_bf8(bv[4 + s]), acc1, 0, 0, 0);
    }
    float* sr = reinterpret_cast<float*>(lds + L_SRED) + (w * 16 + gq * 4) * 33 + (lane & 15);
#pragma unroll
    for (int q = 0; q < 4; ++q) { sr[q * 33] = acc0[q]; sr[q * 33 + 16] = acc1[q]; }
  }
  BAR();
  STAMP(5);
  {
    const int hq = t >> 5, key = t & 31;
    const float* sr = reinterpret_cast<const float*>(lds + L_SRED) + hq * 33 + key;
    float s = 0.f;
#pragma unroll
    for (int ww = 0; ww < 8; ++ww) s += sr[ww * 16 * 33];
    s *= 0.0625f;
    st_gran(ws, WS_SC + (u32)((G * 16 + hq) * 256 + j * 32 + key) * 8u, tag, __float_as_uint(s));
  }
  {
    const __amdgpu_buffer_rsrc_t rk = __builtin_amdgcn_make_buffer_rsrc((void*)ckv, 0, 2u * SK * DC * 2u, 0x00020000);
#pragma unroll
    for (int n = 0; n < 8; ++n) {
      const u32 brow = (u32)(w * 8 + n) * 4u;
      const u32 ch = (u32)(lane & 15) ^ ((gq << 2) | (u32)(n & 3));
      const u32 vo = gq * 2048u + ch * 16u;
      __builtin_amdgcn_raw_ptr_buffer_load_lds(rk, (LAS void*)(lds + L_RA + brow * 256u), 16, vo, (u32)((b * SK + g * 256) * 2048 + j * 256) + brow * 2048u, 0, 2);
    }
  }
  {
    float ml[4];
    const u32 so = WS_SC + (u32)((G * 16 + 2 * w) * 256 + 4 * lane) * 8u;
    v4u gs[4];
    for (u32 spins = 0;;) {
      bool ok = true;
#pragma unroll
      for (int hh = 0; hh < 2; ++hh) {
        gs[2 * hh] = __builtin_amdgcn_raw_buffer_load_b128(rw, so + hh * 2048u, 0, 16);
        gs[2 * hh + 1] = __builtin_amdgcn_raw_buffer_load_b128(rw, so + hh * 2048u + 16u, 0, 16);
      }
#pragma unroll
      for (int s = 0; s < 4; ++s) ok &= (gs[s][1] == tag) & (gs[s][3] == tag);
      if (__all(ok)) break;
      SPIN_FAIL(3u)
    }
    STAMP(6);
#pragma unroll
    for (int hh = 0; hh < 2; ++hh) {
      const float sv[4] = {__uint_as_float(gs[2 * hh][0]), __uint_as_float(gs[2 * hh][2]), __uint_as_float(gs[2 * hh + 1][0]), __uint_as_float(gs[2 * hh + 1][2])};
      const float m = wave_max(fmaxf(fmaxf(sv[0], sv[1]), fmaxf(sv[2], sv[3])));
      const u32 p0 = pk2(__expf(sv[0] - m), __expf(sv[1] - m)), p1 = pk2(__expf(sv[2] - m), __expf(sv[3] - m));
      const float l = wave_sum((lo_f(p0) + hi_f(p0)) + (lo_f(p1) + hi_f(p1)));
      *reinterpret_cast<v2u*>(lds + L_P + (2 * w + hh) * 528 + lane * 8) = v2u{p0, p1};
      ml[2 * hh] = m; ml[2 * hh + 1] = l;
    }
    if (j == 0 && lane < 4) {
      const float v = lane == 0 ? ml[0] : lane == 1 ? ml[1] : lane == 2 ? ml[2] : ml[3];
      st_gran(ws, WS_ML + (u32)(((b * 16 + 2 * w + (lane >> 1)) * 16 + g) * 2 + (lane & 1)) * 8u, tag, __float_as_uint(v));
    }
  }
  asm volatile("s_waitcnt vmcnt(0)" ::: "memory");
  BAR();
  STAMP(7);
  {
    v4f acc0 = {0.f, 0.f, 0.f, 0.f}, acc1 = {0.f, 0.f, 0.f, 0.f};
    const u32 po = L_P + (u32)(lane & 15) * 528u + gq * 16u;
    const u32 k0 = L_RA + off_b(8 * gq + qq, 2 * w + (pp >> 1)) + 8 * (pp & 1);
    const u32 k1 = L_RA + off_b(8 * gq + 4 + qq, 2 * w + (pp >> 1)) + 8 * (pp & 1);
#pragma unroll
    for (int ks = 0; ks < 4; ++ks) {
      acc0 = __builtin_amdgcn_mfma_f32_16x16x32_bf16(as_bf8(lds16(lds, po + ks * 64)), tr_pair(lds, k0 + ks * 8192, k1 + ks * 8192), acc0, 0, 0, 0);
      acc1 = __builtin_amdgcn_mfma_f32_16x16x32_bf16(as_bf8(lds16(lds, po + (ks + 4) * 64)), tr_pair(lds, k0 + (ks + 4) * 8192, k1 + (ks + 4) * 8192), acc1, 0, 0, 0);
    }
    const v4f acc = acc0 + acc1;
    const int od = lane & 1;
    const float s0 = od ? acc[0] : acc[2], s1 = od ? acc[1] : acc[3];
    const float n0 = dppf<0xB1>(s0), n1 = dppf<0xB1>(s1);
    const u32 g0 = od ? pk2(n0, acc[2]) : pk2(acc[0], n0);
    const u32 g1 = od ? pk2(n1, acc[3]) : pk2(acc[1], n1);
    const int hq = (lane >> 4) * 4 + 2 * od;
    const u32 go = WS_PV + (u32)(((b * 16 + hq) * 16 + g) * 512 + j * 64 + w * 8 + ((lane & 15) >> 1)) * 8u;
    st_gran(ws, go, tag, g0);
    st_gran(ws, go + 16u * 512u * 8u, tag, g1);
  }
  BAR();
  STAMP(8);
  {
    const __amdgpu_buffer_rsrc_t ru = __builtin_amdgcn_make_buffer_rsrc((void*)wuv, 0, 1024u * 8192u, 0x00020000);
    const u32 vo = (u32)(lane >> 2) * 8192u + (u32)(h2 * 512 + ds * 64) + pp * 16u;
#pragma unroll
    for (int n = 0; n < 8; ++n)
      __builtin_amdgcn_raw_ptr_buffer_load_lds(ru, (LAS void*)(lds + L_RB + (w * 8 + n) * 1024), 16, vo, (u32)(w * 8 + n) * 131072u, 0, 2);
    if (w < 7) {
      const __amdgpu_buffer_rsrc_t ro = __builtin_amdgcn_make_buffer_rsrc((void*)wo, 0, 4096u * 896u, 0x00020000);
      const __amdgpu_buffer_rsrc_t r2 = __builtin_amdgcn_make_buffer_rsrc((void*)w2, 0, 1792u * 896u, 0x00020000);
#pragma unroll
      for (int n = 0; n < 4; ++n)
        __builtin_amdgcn_raw_ptr_buffer_load_lds(ro, (LAS void*)(lds + L_RA + (w * 4 + n) * 1024), 16, lane * 16, (u32)(hd * 32) * 896u + (u32)(w * 4 + n) * 1024u, 0, 2);
#pragma unroll
      for (int n = 0; n < 2; ++n)
        __builtin_amdgcn_raw_ptr_buffer_load_lds(r2, (LAS void*)(lds + L_RA + 28672 + (w * 2 + n) * 1024), 16, lane * 16, (u32)(fsl * 16) * 896u + (u32)(w * 2 + n) * 1024u, 0, 2);
    }
  }
  {
    const int gh = w >> 2;
    const u32 po = WS_PV + (u32)(((b2i * 16 + h2) * 16 + gh * 8) * 512 + (t & 255) * 2) * 8u;
    gu64* mlp = (gu64*)(ws + WS_ML) + (b2i * 16 + h2) * 32 + (lane & 31);
    v4u pv[8];
    u64 mlv;
    for (u32 spins = 0;;) {
      bool ok = true;
#pragma unroll
      for (int gg = 0; gg < 8; ++gg) pv[gg] = __builtin_amdgcn_raw_buffer_load_b128(rw, po + gg * 4096u, 0, 16);
      mlv = __hip_atomic_load(mlp, RLX_AGENT);
#pragma unroll
      for (int gg = 0; gg < 8; ++gg) ok &= (pv[gg][1] == tag) & (pv[gg][3] == tag);
      ok &= (u32)(mlv >> 32) == tag;
      if (__all(ok)) break;
      SPIN_FAIL(4u)
    }
    STAMP(9);
    const float mv = __uint_as_float((u32)mlv);
    const float other = dppf<0xB1>(mv);
    const float mg = (lane & 1) ? other : mv, lg = (lane & 1) ? mv : other;
    const float Mx = wave_max(mg);
    const float wg = __expf(mg - Mx);
    const float Ls = wave_sum(wg * lg) * 0.25f;
    const float wn = wg / Ls;
    float acc[4] = {0.f, 0.f, 0.f, 0.f};
#pragma unroll
    for (int gg = 0; gg < 8; ++gg) {
      const float wv = __int_as_float(__builtin_amdgcn_readlane(__float_as_int(wn), (gh * 8 + gg) * 2));
      acc[0] += wv * lo_f(pv[gg][0]); acc[1] += wv * hi_f(pv[gg][0]);
      acc[2] += wv * lo_f(pv[gg][2]); acc[3] += wv * hi_f(pv[gg][2]);
    }
    const u32 h0 = pk2(acc[0], acc[1]), h1v = pk2(acc[2], acc[3]);
    const u32 l0 = pk2(acc[0] - lo_f(h0), acc[1] - hi_f(h0)), l1 = pk2(acc[2] - lo_f(h1v), acc[3] - hi_f(h1v));
    const u32 co = L_CA + (u32)(2 * gh) * 2064u + (u32)(t & 255) * 8u;
    *reinterpret_cast<v2u*>(lds + co) = v2u{h0, h1v};
    *reinterpret_cast<v2u*>(lds + co + 2064) = v2u{l0, l1};
  }
  BAR();
  STAMP(10);
  {
    v4f acc0 = {0.f, 0.f, 0.f, 0.f}, acc1 = {0.f, 0.f, 0.f, 0.f};
    const u32 ao = L_CA + (u32)(lane & 3) * 2064u + (u32)w * 256u + gq * 16u;
    const u32 bo = L_RB + (u32)(w * 128 + 8 * gq + qq) * 64u + pp * 8u;
#pragma unroll
    for (int s = 0; s < 4; ++s) {
      const bf8 av = as_bf8(lds16(lds, ao + s * 64));
      acc0 = __builtin_amdgcn_mfma_f32_16x16x32_bf16(av, tr_pair(lds, bo + s * 2048, bo + s * 2048 + 256), acc0, 0, 0, 0);
      acc1 = __builtin_amdgcn_mfma_f32_16x16x32_bf16(av, tr_pair(lds, bo + s * 2048 + 32, bo + s * 2048 + 288), acc1, 0, 0, 0);
    }
    if (lane < 16) {
      float* cr = reinterpret_cast<float*>(lds + L_CRED) + w * 32 + lane;
      cr[0] = (acc0[0] + acc0[1]) + (acc0[2] + acc0[3]);
      cr[16] = (acc1[0] + acc1[1]) + (acc1[2] + acc1[3]);
    }
  }
  BAR();
  STAMP(11);
  if (w < 7) {
    const float* cr = reinterpret_cast<const float*>(lds + L_CRED) + (lane & 31);
    float cv = 0.f;
#pragma unroll
    for (int ww = 0; ww < 8; ++ww) cv += cr[ww * 32];
    *reinterpret_cast<u16*>(lds + L_VST + w * 64 + (lane & 31) * 2) = f2bf(cv);
    const bf8 a0 = as_bf8(lds16(lds, L_VST + w * 64 + gq * 16)), a1 = as_bf8(lds16(lds, L_VH + gq * 16));
    const u32 bo0 = L_RA + (8u * gq + qq) * 896u + (u32)w * 128u + pp * 8u;
    const u32 bo1 = L_RA + 28672u + ((8u * gq + qq) & 15u) * 896u + (u32)w * 128u + pp * 8u;
#pragma unroll
    for (int n = 0; n < 4; ++n) {
      v4f acc = {0.f, 0.f, 0.f, 0.f};
      acc = __builtin_amdgcn_mfma_f32_16x16x32_bf16(a0, tr_pair(lds, bo0 + n * 32, bo0 + n * 32 + 3584), acc, 0, 0, 0);
      acc = __builtin_amdgcn_mfma_f32_16x16x32_bf16(a1, tr_pair(lds, bo1 + n * 32, bo1 + n * 32 + 3584), acc, 0, 0, 0);
      if (lane < 16) st_gran(ws, WS_OG + (u32)((b2i * 128 + hd) * M_ + w * 64 + n * 16 + lane) * 8u, tag, __float_as_uint(acc[0]));
    }
  }
  STAMP(12);
  if (i < 28) {
    const int fb = i / 14, fc = i - fb * 14;
    const int col = t & 31, pg = t >> 5;
    gu64* og = (gu64*)(ws + WS_OG) + (size_t)(fb * 128 + pg * 8) * M_ + fc * 32 + col;
    float v[8];
    for (u32 spins = 0;;) {
      bool ok = true;
#pragma unroll
      for (int k = 0; k < 8; ++k) { const u64 xv = __hip_atomic_load(og + M_ * k, RLX_AGENT); v[k] = __uint_as_float((u32)xv); ok &= (u32)(xv >> 32) == tag; }
      if (__all(ok)) break;
      SPIN_FAIL(5u)
    }
    reinterpret_cast<float*>(lds + L_FRED)[pg * 32 + col] = ((v[0] + v[1]) + (v[2] + v[3])) + ((v[4] + v[5]) + (v[6] + v[7]));
    BAR();
    STAMP(13);
    if (t < 32) {
      float a = 0.f;
#pragma unroll
      for (int q = 0; q < 16; ++q) a += reinterpret_cast<const float*>(lds + L_FRED)[q * 32 + t];
      const int m = fc * 32 + t;
      out[fb * M_ + m] = f2bf(bf2f(x[fb * M_ + m]) + a + bf2f(b2[m]));
    }
    if (i == 0 && t == 0) __hip_atomic_store((gu32*)(ws + WS_EP), tag, RLX_AGENT);
    STAMP(14);
  }
}

extern "C" void kernel_launch(void* const* d_in, const int* in_sizes, int n_in,
                              void* d_out, int out_size, void* d_ws, size_t ws_size,
                              hipStream_t stream) {
  const u16* x = (const u16*)d_in[0];
  const u16* ckv = (const u16*)d_in[1];
  const u16* wq = (const u16*)d_in[2];
  const u16* wuk = (const u16*)d_in[3];
  const u16* wuv = (const u16*)d_in[4];
  const u16* wo = (const u16*)d_in[5];
  const u16* w1 = (const u16*)d_in[7];
  const u16* b1 = (const u16*)d_in[8];
  const u16* w2 = (const u16*)d_in[9];
  const u16* b2 = (const u16*)d_in[10];
  mla_decode<<<256, 512, 0, stream>>>(x, ckv, wq, wuk, wuv, wo, w1, b1, w2, b2, (char*)d_ws, (u16*)d_out);
}
```

```cpp
#include <hip/hip_runtime.h>
#include <hip/hip_bf16.h>
#include <stdint.h>

typedef unsigned short u16;
typedef unsigned int u32;
typedef unsigned long long u64;
typedef __attribute__((ext_vector_type(4))) unsigned v4u;
typedef __attribute__((ext_vector_type(2))) unsigned v2u;
typedef __attribute__((ext_vector_type(4))) float v4f;
typedef __attribute__((ext_vector_type(8))) __bf16 bf8;
typedef __attribute__((ext_vector_type(4))) short s4;
typedef __attribute__((ext_vector_type(8))) short s8;

#define GAS __attribute__((address_space(1)))
#define LAS __attribute__((address_space(3)))
typedef GAS u32 gu32;
typedef GAS u64 gu64;
#define RLX_AGENT __ATOMIC_RELAXED, __HIP_MEMORY_SCOPE_AGENT

#define M_ 448
#define F_ 1792
#define DC 1024
#define SK 4096

constexpr u32 SPIN_CAP = 1u << 18;

constexpr u32 WS_EP = 0;
constexpr u32 WS_QG = 4096;
constexpr u32 WS_QL = 65536;
constexpr u32 WS_SC = 262144;
constexpr u32 WS_PV = 1310720;
constexpr u32 WS_ML = 3407872;
constexpr u32 WS_CP = 3416064;
constexpr u32 WS_OG = 4464640;
constexpr u32 WS_END = WS_OG + 2 * 128 * 448 * 8;

#define STAMP(k) do {} while (0)
#define STAMP_DECL
#define STAMP_FLUSH do {} while (0)

#define BAR() do { asm volatile("s_waitcnt lgkmcnt(0)" ::: "memory"); __builtin_amdgcn_s_barrier(); asm volatile("" ::: "memory"); } while (0)

__device__ __forceinline__ float bf2f(u16 v) { return __uint_as_float(((u32)v) << 16); }
__device__ __forceinline__ u16 f2bf(float f) { __hip_bfloat16 b = __float2bfloat16(f); return *reinterpret_cast<u16*>(&b); }
__device__ __forceinline__ float rbf(float f) { return bf2f(f2bf(f)); }
__device__ __forceinline__ float lo_f(u32 v) { return __uint_as_float(v << 16); }
__device__ __forceinline__ float hi_f(u32 v) { return __uint_as_float(v & 0xffff0000u); }
__device__ __forceinline__ u32 pk2(float a, float b) { return (u32)f2bf(a) | ((u32)f2bf(b) << 16); }
__device__ __forceinline__ v4u ld16(const u16* p) { return __builtin_nontemporal_load((const GAS v4u*)p); }
__device__ __forceinline__ v4u ldp16(const u16* p) { return *(const GAS v4u*)p; }
__device__ __forceinline__ u16 ldh(const u16* p) { return *(const GAS u16*)p; }
__device__ __forceinline__ bf8 as_bf8(v4u v) { return __builtin_bit_cast(bf8, v); }

template <int CTRL>
__device__ __forceinline__ float dppf(float v) {
  return __int_as_float(__builtin_amdgcn_update_dpp(0, __float_as_int(v), CTRL, 0xf, 0xf, true));
}
template <int N> __device__ __forceinline__ float ror_add(float v) { return v + dppf<0x120 + N>(v); }
template <int N> __device__ __forceinline__ float ror_max(float v) { return fmaxf(v, dppf<0x120 + N>(v)); }
__device__ __forceinline__ float x16_add(float v) {
  const u32 b = __float_as_uint(v);
  auto p = __builtin_amdgcn_permlane16_swap(b, b, false, false);
  return __uint_as_float(p[0]) + __uint_as_float(p[1]);
}
__device__ __forceinline__ float x32_add(float v) {
  const u32 b = __float_as_uint(v);
  auto p = __builtin_amdgcn_permlane32_swap(b, b, false, false);
  return __uint_as_float(p[0]) + __uint_as_float(p[1]);
}
__device__ __forceinline__ float x16_max(float v) {
  const u32 b = __float_as_uint(v);
  auto p = __builtin_amdgcn_permlane16_swap(b, b, false, false);
  return fmaxf(__uint_as_float(p[0]), __uint_as_float(p[1]));
}
__device__ __forceinline__ float x32_max(float v) {
  const u32 b = __float_as_uint(v);
  auto p = __builtin_amdgcn_permlane32_swap(b, b, false, false);
  return fmaxf(__uint_as_float(p[0]), __uint_as_float(p[1]));
}
__device__ __forceinline__ float wave_sum(float v) {
  v = ror_add<8>(v); v = ror_add<4>(v); v = ror_add<2>(v); v = ror_add<1>(v);
  return x32_add(x16_add(v));
}
__device__ __forceinline__ float wave_max(float v) {
  v = ror_max<8>(v); v = ror_max<4>(v); v = ror_max<2>(v); v = ror_max<1>(v);
  return x32_max(x16_max(v));
}

__device__ __forceinline__ u32 swz(u32 row) { return ((row & 3u) << 2) | ((row >> 2) & 3u); }
__device__ __forceinline__ u32 off_b(u32 row, u32 ch) { return 256u * row + 16u * (ch ^ swz(row)); }

constexpr u32 L_RA = 0;
constexpr u32 L_UV = 65536;
constexpr u32 L_WO = 98304;
constexpr u32 L_WQ = L_WO;
constexpr u32 L_W1 = L_WO + 14336;
constexpr u32 L_W2 = 126976;
constexpr u32 L_XN = 141312;
constexpr u32 L_P = 143104;
constexpr u32 L_QST = L_P;
constexpr u32 L_SRED = L_P;
constexpr u32 L_DP = L_P + 16896;
constexpr u32 L_CR = L_DP + 2048;
constexpr u32 L_VST = L_CR + 512;
constexpr u32 L_VH = L_VST + 512;
constexpr u32 L_FR = L_VH + 64;
constexpr u32 L_FLAG = L_FR + 256;
constexpr u32 L_END = L_FLAG + 128;
static_assert(L_END <= 163840, "LDS");
constexpr u32 F_PROG = L_FLAG;
constexpr u32 F_PULL = L_FLAG + 4;
constexpr u32 F_UK = L_FLAG + 36;
constexpr u32 F_RO = L_FLAG + 40;
constexpr u32 F_SR = L_FLAG + 44;
constexpr u32 F_B = L_FLAG + 48;
constexpr u32 F_QP = L_FLAG + 68;
constexpr u32 F_XN = L_FLAG + 72;
constexpr int P_WQ = 14, P_UK = 46, P_W1 = 60, P_ROWS = 124, P_COL = 188, P_UV = 220, P_W2 = 234, P_WO = 262;

#define WIN 16
#define AUX_COL 2
#define PSLP 2
#define PRETOUCH 0
#define CPRIO 2
#define SPRIO 3
constexpr u32 LSPIN = 1u << 22;

#define SPIN_FAIL(code) if (++spins > SPIN_CAP) { if (lane == 0) __hip_atomic_store(tmo, (code), RLX_AGENT); break; } __builtin_amdgcn_s_sleep(PSLP); asm volatile("" ::: "memory");

#define POLL1(v, off, code) __builtin_amdgcn_s_setprio(0); _Pragma("unroll 1") for (u32 spins = 0;;) { v = __builtin_amdgcn_raw_buffer_load_b128(rw, (off), 0, 16); if (__all((v[1] == tag) & (v[3] == tag))) break; SPIN_FAIL(code) } __builtin_amdgcn_s_setprio(CPRIO);

#define POLLN(v, N, off, stride, code) { u32 pend = (1u << (N)) - 1u; __builtin_amdgcn_s_setprio(0); _Pragma("unroll 1") for (u32 spins = 0;;) { \
    _Pragma("unroll") for (int s_ = 0; s_ < (N); ++s_) if (pend >> s_ & 1u) v[s_] = __builtin_amdgcn_raw_buffer_load_b128(rw, (off) + s_ * (stride), 0, 16); \
    _Pragma("unroll") for (int s_ = 0; s_ < (N); ++s_) if (pend >> s_ & 1u) { if (__all((v[s_][1] == tag) & (v[s_][3] == tag))) pend &= ~(1u << s_); } \
    if (!pend) break; SPIN_FAIL(code) } __builtin_amdgcn_s_setprio(CPRIO); }

__device__ __forceinline__ void st_gran(char* ws, u32 off, u32 tag, u32 val) {
  __hip_atomic_store((gu64*)(ws + off), ((u64)tag << 32) | val, RLX_AGENT);
}
__device__ __forceinline__ bf8 tr_pair(char* lds, u32 a0, u32 a1) {
  const s4 b0 = __builtin_amdgcn_ds_read_tr16_b64_v4i16((LAS s4*)(lds + a0));
  const s4 b1 = __builtin_amdgcn_ds_read_tr16_b64_v4i16((LAS s4*)(lds + a1));
  const s8 bb = {b0[0], b0[1], b0[2], b0[3], b1[0], b1[1], b1[2], b1[3]};
  return __builtin_bit_cast(bf8, bb);
}
__device__ __forceinline__ v4u lds16(char* lds, u32 a) { return *reinterpret_cast<const v4u*>(lds + a); }
__device__ __forceinline__ v4f lds16f(char* lds, u32 a) { return *reinterpret_cast<const v4f*>(lds + a); }
__device__ __forceinline__ float fdiv(float a, float d, float r) {
  const float q = a * r;
  return fmaf(fmaf(-q, d, a), r, q);
}
__device__ __forceinline__ v4u frag(v4u v, int pa) {
  v4u o;
#pragma unroll
  for (int e = 0; e < 4; ++e) o[e] = (u32)__builtin_amdgcn_ds_bpermute(pa, (int)v[e]);
  return o;
}
__device__ __forceinline__ void gemv448(char* lds, u32 wt, int lane, float& a0, float& a1) {
  const u32 gq = lane >> 4, qq = (lane & 15) >> 2, pp = lane & 3;
  const u32 ao = L_XN + (u32)(lane & 1) * 896u + gq * 16u;
  const u32 bo = wt + (8u * gq + qq) * 32u + pp * 8u;
  v4f acc0 = {0.f, 0.f, 0.f, 0.f}, acc1 = {0.f, 0.f, 0.f, 0.f};
#pragma unroll
  for (int s = 0; s < 7; ++s) {
    acc0 = __builtin_amdgcn_mfma_f32_16x16x32_bf16(as_bf8(lds16(lds, ao + s * 64)), tr_pair(lds, bo + s * 1024, bo + s * 1024 + 128), acc0, 0, 0, 0);
    acc1 = __builtin_amdgcn_mfma_f32_16x16x32_bf16(as_bf8(lds16(lds, ao + (s + 7) * 64)), tr_pair(lds, bo + (s + 7) * 1024, bo + (s + 7) * 1024 + 128), acc1, 0, 0, 0);
  }
  a0 = acc0[0] + acc1[0]; a1 = acc0[1] + acc1[1];
}

__device__ __forceinline__ void fl_write(u32 a, u32 v) { asm volatile("ds_write_b32 %0, %1" :: "v"(a), "v"(v) : "memory"); }
__device__ __forceinline__ void fl_add(u32 a, int lane) { if (lane == 0) asm volatile("ds_add_u32 %0, %1" :: "v"(a), "v"(1u) : "memory"); }
__device__ __forceinline__ int fl_read(u32 a) {
  u32 v; asm volatile("ds_read_b32 %0, %1\n s_waitcnt lgkmcnt(0)" : "=v"(v) : "v"(a) : "memory");
  return (int)__builtin_amdgcn_readfirstlane(v);
}
__device__ __forceinline__ void fl_wait(u32 a, int v) {
#pragma unroll 1
  for (u32 n = 0; n < LSPIN; ++n) { if (fl_read(a) >= v) break; __builtin_amdgcn_s_sleep(0); }
}
__device__ __forceinline__ void sbar(u32 a, int lane) { fl_add(a, lane); fl_wait(a, 8); }
template <int K> __device__ __forceinline__ void drain(u32 fp, int q, int lo) {
  asm volatile("s_waitcnt vmcnt(%0)" :: "n"(K) : "memory");
  fl_write(fp, (u32)(q - K > lo ? q - K : lo));
  if constexpr (K > 0) drain<K - 1>(fp, q, lo);
}
__device__ __forceinline__ void gate(u32 fp, int q, u32 a, int v, int& lo) {
  if (fl_read(a) >= v) return;
  if (lo < q) drain<WIN - 2>(fp, q, lo);
  lo = q;
  fl_wait(a, v);
}
template <int N, int U, int XM, int SEG, int AUX>
__device__ __forceinline__ void stream(char* lds, u32 lb, const void* base, u32 bytes, u32 vb, u32 sb, u32 ss, u32 ld, int q0, int& lo) {
  const __amdgpu_buffer_rsrc_t rs = __builtin_amdgcn_make_buffer_rsrc((void*)base, 0, bytes, 0x00020000);
#pragma unroll 1
  for (u32 k = 0; k < (u32)(N / U); ++k) {
    const int qk = q0 + (int)(k * U);
    if (SEG == 2) gate(lb + F_PROG, qk, lb + F_PULL + k * 4u, 1, lo);
    const u32 sk = SEG == 1 ? sb + (k < 3u ? k + 1u : k == 3u ? 0u : k) * 256u : sb + k * (U * ss);
#pragma unroll
    for (u32 n = 0; n < (u32)U; ++n) {
      const int q = qk + (int)n;
      asm volatile("s_waitcnt vmcnt(%0)" :: "n"(WIN - 1) : "memory");
      fl_write(lb + F_PROG, (u32)(q - (WIN - 1) > lo ? q - (WIN - 1) : lo));
      const u32 xm = XM == 1 ? (n & 3u) << 4 : XM == 2 ? ((2u * n) & 15u) << 4 : XM == 3 ? (((n & 1u) << 2) | (((n >> 2) & 1u) << 3)) << 4 : 0u;
      __builtin_amdgcn_raw_ptr_buffer_load_lds(rs, (LAS void*)(lds + ld + k * (U * 1024u) + n * 1024u), 16, vb ^ xm, sk + n * ss, 0, AUX);
    }
  }
}

__global__ __launch_bounds__(576) void mla_decode(const u16* x, const u16* ckv, const u16* wq, const u16* wuk,
                                                  const u16* wuv, const u16* wo, const u16* w1, const u16* b1,
                                                  const u16* w2, const u16* b2, char* ws, u16* out) {
  __shared__ __attribute__((aligned(16))) char lds[L_END];
  const int t = threadIdx.x, lane = t & 63;
  const int w = __builtin_amdgcn_readfirstlane(t >> 6);
  const int i = blockIdx.x, xcd = i & 7, r = i >> 3;
  const int h1i = xcd * 2 + (r >> 4), cs = r & 15;
  const int G = xcd * 4 + (r >> 3), b = G >> 4, g = G & 15, j = r & 7;
  const int b2i = r & 1, hd = xcd * 16 + (r >> 1), ds = hd & 7;
  const int fsl = hd < 112 ? hd : 0;
  STAMP_DECL
  STAMP(0);
  asm volatile("" : "+s"(x), "+s"(ckv), "+s"(wq), "+s"(wuk), "+s"(wuv), "+s"(wo), "+s"(w1), "+s"(b1), "+s"(w2), "+s"(b2), "+s"(ws), "+s"(out));
  gu32* tmo = (gu32*)(ws + WS_EP + 128);
  const __amdgpu_buffer_rsrc_t rw = __builtin_amdgcn_make_buffer_rsrc(ws, 0, WS_END, 0x00020000);
  const u32 gq = lane >> 4, qq = (lane & 15) >> 2, pp = lane & 3;

  const u32 lb = (u32)(uintptr_t)(LAS char*)lds;

  if (w == 8) {
    __builtin_amdgcn_s_setprio(SPRIO);
    if (lane < 32) fl_write(lb + L_FLAG + lane * 4, 0u);
    BAR();
    int lo = 0;
    const u32 hb = (u32)lane >> 5;
    const u32 vk = gq * 2048u + (((u32)lane & 15u) ^ (gq << 2)) * 16u;
    stream<14, 7, 0, 0, 0>(lds, lb, wq, M_ * 8192u, (u32)(lane >> 1) * 8192u + (u32)(lane & 1) * 16u, (u32)(h1i * 256 + cs * 16) * 2u, 32u * 8192u, L_WQ, 0, lo);
    stream<32, 8, 2, 0, 2>(lds, lb, wuk, 1024u * 8192u, hb * 8192u + (((u32)lane & 31u) ^ hb) * 16u, (u32)(cs * 64) * 8192u + (u32)h1i * 512u, 16384u, L_UV, P_WQ, lo);
    STAMP(24);
    stream<14, 7, 0, 0, 0>(lds, lb, w1, M_ * F_ * 2u, (u32)(lane >> 1) * (F_ * 2u) + (u32)(lane & 1) * 16u, (u32)fsl * 32u, 32u * F_ * 2u, L_W1, P_UK, lo);
    stream<64, 8, 1, 1, 0>(lds, lb, ckv, 2u * SK * DC * 2u, vk, (u32)(b * SK + g * 256 + j * 32) * 2048u, 8192u, L_RA, P_W1, lo);
    STAMP(25);
    stream<64, 8, 1, 2, AUX_COL>(lds, lb, ckv, 2u * SK * DC * 2u, vk, (u32)(b * SK + g * 256) * 2048u + (u32)j * 256u, 8192u, L_RA, P_ROWS, lo);
    STAMP(26);
    gate(lb + F_PROG, P_COL, lb + F_UK, 4, lo);
    stream<32, 8, 3, 0, 2>(lds, lb, wuv, 1024u * 8192u, hb * 8192u + (((u32)lane & 31u) ^ (hb << 1)) * 16u, (u32)(cs * 64) * 8192u + (u32)h1i * 512u, 16384u, L_UV, P_COL, lo);
    stream<14, 7, 0, 0, 2>(lds, lb, w2, 1792u * 896u, (u32)lane * 16u, (u32)(fsl * 16) * 896u, 1024u, L_W2, P_UV, lo);
    STAMP(27);
    gate(lb + F_PROG, P_W2, lb + F_RO, 2, lo);
    STAMP(28);
    stream<28, 7, 0, 0, 2>(lds, lb, wo, 4096u * 896u, (u32)lane * 16u, (u32)(hd * 32) * 896u, 1024u, L_WO, P_W2, lo);
    drain<WIN - 1>(lb + F_PROG, P_WO, lo);
    STAMP(29);
    STAMP_FLUSH;
    return;
  }

  __builtin_amdgcn_s_setprio(CPRIO);
  const u32 epv = __builtin_amdgcn_raw_buffer_load_b32(rw, WS_EP, 0, 0);
  const int pa = (4 * (lane & 15) + (lane >> 4)) * 4;
  float bias = 0.f;
  u32 tag;
  if (w == 0) {
    const int lc = lane < 56 ? lane : 0;
    const v4u xr0 = ldp16(x + lc * 8), xr1 = ldp16(x + M_ + lc * 8);
    STAMP(21);
    BAR();
    float xa[8], xb[8];
#pragma unroll
    for (int p = 0; p < 4; ++p) { xa[2 * p] = lo_f(xr0[p]); xa[2 * p + 1] = hi_f(xr0[p]); xb[2 * p] = lo_f(xr1[p]); xb[2 * p + 1] = hi_f(xr1[p]); }
    const float act = lane < 56 ? 1.f : 0.f;
    float s0 = 0.f, s1 = 0.f;
#pragma unroll
    for (int k = 0; k < 8; ++k) { s0 += xa[k]; s1 += xb[k]; }
    s0 = wave_sum(s0 * act); s1 = wave_sum(s1 * act);
    const float m0 = s0 * (1.f / M_), m1 = s1 * (1.f / M_);
    float v0 = 0.f, v1 = 0.f;
#pragma unroll
    for (int k = 0; k < 8; ++k) { const float d0 = xa[k] - m0, d1 = xb[k] - m1; v0 += d0 * d0; v1 += d1 * d1; }
    v0 = wave_sum(v0 * act); v1 = wave_sum(v1 * act);
    const float mb0 = rbf(m0), mb1 = rbf(m1);
    const float den0 = rbf(sqrtf(rbf(rbf(v0 * (1.f / M_)) + 1e-5f)));
    const float den1 = rbf(sqrtf(rbf(rbf(v1 * (1.f / M_)) + 1e-5f)));
    const float r0 = __builtin_amdgcn_rcpf(den0), r1 = __builtin_amdgcn_rcpf(den1);
    v4u na, nb;
#pragma unroll
    for (int p = 0; p < 4; ++p) {
      na[p] = pk2(fdiv(rbf(xa[2 * p] - mb0), den0, r0), fdiv(rbf(xa[2 * p + 1] - mb0), den0, r0));
      nb[p] = pk2(fdiv(rbf(xb[2 * p] - mb1), den1, r1), fdiv(rbf(xb[2 * p + 1] - mb1), den1, r1));
    }
    if (lane < 56) {
      *reinterpret_cast<v4u*>(lds + L_XN + lane * 16) = na;
      *reinterpret_cast<v4u*>(lds + L_XN + 896 + lane * 16) = nb;
    }
    fl_add(lb + F_XN, lane);
    STAMP(17);
    fl_wait(lb + F_PROG, P_WQ);
    STAMP(18);
    float a0, a1;
    gemv448(lds, L_WQ, lane, a0, a1);
    tag = __builtin_amdgcn_readfirstlane(epv) + 1u;
    const float o0 = dppf<0xB1>(a0), o1 = dppf<0xB1>(a1);
    if (lane < 16) {
      const int bb = lane & 1;
      st_gran(ws, WS_QG + (u32)((h1i * 2 + bb) * 128 + cs * 8 + (lane >> 1)) * 8u, tag, bb ? pk2(o1, a1) : pk2(a0, o0));
    }
    STAMP(1);
    fl_add(lb + F_RO, lane);
  } else if (w < 4) {
    if (w == 1) bias = bf2f(ldh(b1 + fsl * 16 + (lane & 15)));
    BAR();
    tag = __builtin_amdgcn_readfirstlane(epv) + 1u;
  } else {
    v4u wk[8];
    BAR();
    fl_wait(lb + F_PROG, P_UK);
    {
      const u32 ko = L_UV + (u32)((w - 4) * 16 + (lane & 15)) * 512u;
#pragma unroll
      for (int s = 0; s < 8; ++s) wk[s] = lds16(lds, ko + ((((u32)s * 4u + gq) ^ ((u32)lane & 15u)) * 16u));
    }
    fl_add(lb + F_UK, lane);
    STAMP(19);
    tag = __builtin_amdgcn_readfirstlane(epv) + 1u;
    const u32 qo = WS_QG + (u32)h1i * 2048u + (u32)lane * 16u;
    v4u ga[2];
    POLLN(ga, 2, qo, 1024u, 1u)
    const v4u g0 = ga[0], g1 = ga[1];
    STAMP(20);
    const u32 qs = L_QST + (u32)(w - 4) * 1024u;
    *reinterpret_cast<v2u*>(lds + qs + lane * 8) = v2u{g0[0], g0[2]};
    *reinterpret_cast<v2u*>(lds + qs + 512 + lane * 8) = v2u{g1[0], g1[2]};
    const u32 ao = qs + (u32)(lane & 1) * 512u + gq * 16u;
    v4f acc0 = {0.f, 0.f, 0.f, 0.f}, acc1 = {0.f, 0.f, 0.f, 0.f};
#pragma unroll
    for (int s = 0; s < 4; ++s) {
      acc0 = __builtin_amdgcn_mfma_f32_16x16x32_bf16(as_bf8(lds16(lds, ao + s * 64)), as_bf8(wk[s]), acc0, 0, 0, 0);
      acc1 = __builtin_amdgcn_mfma_f32_16x16x32_bf16(as_bf8(lds16(lds, ao + (s + 4) * 64)), as_bf8(wk[s + 4]), acc1, 0, 0, 0);
    }
    const float a0 = acc0[0] + acc1[0], a1 = acc0[1] + acc1[1];
    const float o0 = dppf<0xB1>(a0), o1 = dppf<0xB1>(a1);
    if (lane < 16) {
      const int bb = lane & 1, wq4 = w - 4;
      const u32 qa = (u32)bb * 65536u + (u32)((cs * 2 + (wq4 >> 1)) * 2 + ((lane >> 2) & 1)) * 1024u + (u32)h1i * 64u
                     + (u32)((wq4 & 1) * 2 + (lane >> 3)) * 16u + (u32)((lane >> 1) & 1) * 8u;
      st_gran(ws, WS_QL + qa, tag, bb ? pk2(o1, a1) : pk2(a0, o0));
    }
    STAMP(2);
    fl_add(lb + F_QP, lane);
  }
  if (w == 1) {
    float a0, a1;
    fl_wait(lb + F_XN, 1);
    fl_wait(lb + F_PROG, P_W1);
    gemv448(lds, L_W1, lane, a0, a1);
    fl_add(lb + F_RO, lane);
    const float hv = rbf(rbf(b2i ? a1 : a0) + bias);
    const float ge = 0.5f * hv * (1.f + tanhf(0.7978845608028654f * (hv + 0.044715f * hv * hv * hv)));
    if (lane < 32) *reinterpret_cast<u16*>(lds + L_VH + lane * 2) = (lane < 16 && hd < 112) ? f2bf(ge) : (u16)0;
  }
  v4u bv[8];
  {
    const int slot = w == 0 ? 3 : w < 4 ? w - 1 : w;
    fl_wait(lb + F_PROG, P_W1 + 8 * (slot + 1));
    const u32 key = (u32)lane & 15u;
    const u32 ro = L_RA + (u32)slot * 8192u + key * 256u;
#pragma unroll
    for (int s = 0; s < 8; ++s) bv[s] = lds16(lds, ro + (u32)(s >> 2) * 4096u + ((((u32)(s & 3) * 4u + gq) ^ swz(key)) * 16u));
    fl_add(lb + F_PULL + (u32)slot * 4u, lane);
  }
  STAMP(3);
  {
    const u32 qo = WS_QL + (u32)b * 65536u + (u32)w * 8192u + (u32)lane * 16u;
    v4u gl[8];
    fl_wait(lb + F_QP, 4);
    POLLN(gl, 8, qo, 1024u, 2u)
    STAMP(4);
    v4f acc0 = {0.f, 0.f, 0.f, 0.f}, acc1 = {0.f, 0.f, 0.f, 0.f};
#pragma unroll
    for (int s = 0; s < 4; ++s) {
      const v4u av = frag(v4u{gl[2 * s][0], gl[2 * s][2], gl[2 * s + 1][0], gl[2 * s + 1][2]}, pa);
      acc0 = __builtin_amdgcn_mfma_f32_16x16x32_bf16(as_bf8(av), as_bf8(bv[s]), acc0, 0, 0, 0);
      acc1 = __builtin_amdgcn_mfma_f32_16x16x32_bf16(as_bf8(av), as_bf8(bv[4 + s]), acc1, 0, 0, 0);
    }
    float* sr = reinterpret_cast<float*>(lds + L_SRED) + (w * 16 + gq * 4) * 33 + (lane & 15);
#pragma unroll
    for (int q = 0; q < 4; ++q) { sr[q * 33] = acc0[q]; sr[q * 33 + 16] = acc1[q]; }
  }
  sbar(lb + F_B, lane);
  STAMP(5);
  {
    const int hq = t >> 5, key = t & 31;
    const float* sr = reinterpret_cast<const float*>(lds + L_SRED) + hq * 33 + key;
    float s = 0.f;
#pragma unroll
    for (int ww = 0; ww < 8; ++ww) s += sr[ww * 16 * 33];
    s *= 0.0625f;
    st_gran(ws, WS_SC + (u32)((G * 16 + hq) * 256 + j * 32 + key) * 8u, tag, __float_as_uint(s));
    fl_add(lb + F_SR, lane);
  }
  {
    float ml[4];
    const u32 so = WS_SC + (u32)((G * 16 + 2 * w) * 256 + 2 * lane) * 8u;
    v4u gs[4];
    POLLN(gs, 4, so, 1024u, 3u)
    STAMP(6);
    fl_wait(lb + F_SR, 8);
#pragma unroll
    for (int hh = 0; hh < 2; ++hh) {
      const float sv[4] = {__uint_as_float(gs[2 * hh][0]), __uint_as_float(gs[2 * hh][2]), __uint_as_float(gs[2 * hh + 1][0]), __uint_as_float(gs[2 * hh + 1][2])};
      const float m = wave_max(fmaxf(fmaxf(sv[0], sv[1]), fmaxf(sv[2], sv[3])));
      const u32 p0 = pk2(__expf(sv[0] - m), __expf(sv[1] - m)), p1 = pk2(__expf(sv[2] - m), __expf(sv[3] - m));
      const float l = wave_sum((lo_f(p0) + hi_f(p0)) + (lo_f(p1) + hi_f(p1)));
      *reinterpret_cast<u32*>(lds + L_P + (2 * w + hh) * 528 + lane * 4) = p0;
      *reinterpret_cast<u32*>(lds + L_P + (2 * w + hh) * 528 + 256 + lane * 4) = p1;
      ml[2 * hh] = m; ml[2 * hh + 1] = l;
    }
    if (j == 0 && lane < 4) {
      const float v = lane == 0 ? ml[0] : lane == 1 ? ml[1] : lane == 2 ? ml[2] : ml[3];
      st_gran(ws, WS_ML + (u32)(((b * 16 + 2 * w + (lane >> 1)) * 16 + g) * 2 + (lane & 1)) * 8u, tag, __float_as_uint(v));
    }
  }
  sbar(lb + F_B + 4, lane);
  STAMP(22);
  fl_wait(lb + F_PROG, P_COL);
  STAMP(7);
  {
    v4f acc0 = {0.f, 0.f, 0.f, 0.f}, acc1 = {0.f, 0.f, 0.f, 0.f};
    const u32 po = L_P + (u32)(lane & 15) * 528u + gq * 16u;
    const u32 k0 = L_RA + off_b(8 * gq + qq, 2 * w + (pp >> 1)) + 8 * (pp & 1);
    const u32 k1 = L_RA + off_b(8 * gq + 4 + qq, 2 * w + (pp >> 1)) + 8 * (pp & 1);
#pragma unroll
    for (int ks = 0; ks < 4; ++ks) {
      acc0 = __builtin_amdgcn_mfma_f32_16x16x32_bf16(as_bf8(lds16(lds, po + ks * 64)), tr_pair(lds, k0 + ks * 8192, k1 + ks * 8192), acc0, 0, 0, 0);
      acc1 = __builtin_amdgcn_mfma_f32_16x16x32_bf16(as_bf8(lds16(lds, po + (ks + 4) * 64)), tr_pair(lds, k0 + (ks + 4) * 8192, k1 + (ks + 4) * 8192), acc1, 0, 0, 0);
    }
    const v4f acc = acc0 + acc1;
    const int hi8 = (lane >> 3) & 1;
    const float s0 = hi8 ? acc[0] : acc[2], s1 = hi8 ? acc[1] : acc[3];
    const float n0 = dppf<0x128>(s0), n1 = dppf<0x128>(s1);
    const u32 g0 = hi8 ? pk2(n0, acc[2]) : pk2(acc[0], n0);
    const u32 g1 = hi8 ? pk2(n1, acc[3]) : pk2(acc[1], n1);
    const int hq = (lane >> 4) * 4 + 2 * hi8;
    const u32 go = WS_PV + (u32)(((b * 16 + hq) * 16 + g) * 512 + j * 64 + w * 8 + (lane & 7)) * 8u;
    st_gran(ws, go, tag, g0);
    st_gran(ws, go + 16u * 512u * 8u, tag, g1);
  }
  STAMP(8);
  {
    const int bq = w >> 2;
    const u32 gg = (u32)(w & 3) * 4u + gq;
    const u32 po = WS_PV + (((u32)(bq * 16 + h1i) * 16u + gg) * 512u + (u32)cs * 32u + (u32)(lane & 15) * 2u) * 8u;
    gu64* mlp = (gu64*)(ws + WS_ML) + (bq * 16 + h1i) * 32 + (lane & 31);
    v4u pv;
    u64 mlv;
#pragma unroll 1
    for (u32 spins = 0;;) {
      mlv = __hip_atomic_load(mlp, RLX_AGENT);
      if (__all((u32)(mlv >> 32) == tag)) break;
      SPIN_FAIL(4u)
    }
    const float mv = __uint_as_float((u32)mlv);
    const float other = dppf<0xB1>(mv);
    const float mg = (lane & 1) ? other : mv, lg = (lane & 1) ? mv : other;
    const float Mx = wave_max(mg);
    const float wg = __expf(mg - Mx);
    const float Ls = wave_sum(wg * lg) * 0.25f;
    const float wn = wg / Ls;
    const float wv = __int_as_float(__builtin_amdgcn_ds_bpermute((int)gg * 8, __float_as_int(wn)));
    POLL1(pv, po, 4u)
    STAMP(9);
    float a[4] = {wv * lo_f(pv[0]), wv * lo_f(pv[2]), wv * hi_f(pv[0]), wv * hi_f(pv[2])};
#pragma unroll
    for (int q = 0; q < 4; ++q) a[q] = x32_add(x16_add(a[q]));
    if (lane < 16) {
      float* dp = reinterpret_cast<float*>(lds + L_DP) + w * 64 + (lane >> 2) * 16 + (lane & 3) * 2;
      dp[0] = a[0]; dp[1] = a[1]; dp[8] = a[2]; dp[9] = a[3];
    }
  }
  sbar(lb + F_B + 8, lane);
  STAMP(23);
  fl_wait(lb + F_PROG, P_UV);
  STAMP(10);
  {
    v4f acc0 = {0.f, 0.f, 0.f, 0.f}, acc1 = {0.f, 0.f, 0.f, 0.f};
    const u32 dpo = L_DP + (u32)((lane >> 2) & 1) * 1024u + gq * 32u;
    const u32 rr = 8u * gq + qq;
    const u32 f = (qq << 1) | ((gq & 1u) << 3);
    const u32 bo0 = L_UV + rr * 512u + ((((u32)w * 4u + (pp >> 1)) ^ f) * 16u) + (pp & 1u) * 8u;
    const u32 bo1 = L_UV + rr * 512u + ((((u32)w * 4u + 2u + (pp >> 1)) ^ f) * 16u) + (pp & 1u) * 8u;
#pragma unroll
    for (int s = 0; s < 2; ++s) {
      v4f c0 = {0.f, 0.f, 0.f, 0.f}, c1 = {0.f, 0.f, 0.f, 0.f};
#pragma unroll
      for (int ww = 0; ww < 4; ++ww) { c0 += lds16f(lds, dpo + ww * 256 + s * 128); c1 += lds16f(lds, dpo + ww * 256 + s * 128 + 16); }
      v4u av = {pk2(c0[0], c0[1]), pk2(c0[2], c0[3]), pk2(c1[0], c1[1]), pk2(c1[2], c1[3])};
      if (lane & 1) {
        av = v4u{pk2(c0[0] - lo_f(av[0]), c0[1] - hi_f(av[0])), pk2(c0[2] - lo_f(av[1]), c0[3] - hi_f(av[1])),
                 pk2(c1[0] - lo_f(av[2]), c1[1] - hi_f(av[2])), pk2(c1[2] - lo_f(av[3]), c1[3] - hi_f(av[3]))};
      }
      acc0 = __builtin_amdgcn_mfma_f32_16x16x32_bf16(as_bf8(av), tr_pair(lds, bo0 + s * 16384, bo0 + s * 16384 + 2048), acc0, 0, 0, 0);
      acc1 = __builtin_amdgcn_mfma_f32_16x16x32_bf16(as_bf8(av), tr_pair(lds, bo1 + s * 16384, bo1 + s * 16384 + 2048), acc1, 0, 0, 0);
    }
    if (lane < 32) {
      const u32 co = WS_CP + ((((u32)(h1i * 2) + gq) * 8u + (u32)w) * 16u + (u32)cs) * 256u + (u32)(lane & 15) * 8u;
      st_gran(ws, co, tag, __float_as_uint(acc0[0] + acc0[1]));
      st_gran(ws, co + 128u, tag, __float_as_uint(acc1[0] + acc1[1]));
    }
  }
  STAMP(11);
  if (w < 4) {
    const u32 co = WS_CP + (u32)((h1i * 2 + b2i) * 8 + ds) * 4096u + (u32)t * 16u;
    v4u cv;
    POLL1(cv, co, 6u)
    const float c0 = x32_add(x16_add(__uint_as_float(cv[0]))), c1 = x32_add(x16_add(__uint_as_float(cv[2])));
    if (lane < 16) *reinterpret_cast<v2u*>(lds + L_CR + w * 128 + lane * 8) = v2u{__float_as_uint(c0), __float_as_uint(c1)};
  }
  STAMP(15);
  sbar(lb + F_B + 12, lane);
  fl_wait(lb + F_PROG, P_WO);
  STAMP(16);
  if (w < 7) {
    const float* cr = reinterpret_cast<const float*>(lds + L_CR) + (lane & 31);
    const float cv = (cr[0] + cr[32]) + (cr[64] + cr[96]);
    *reinterpret_cast<u16*>(lds + L_VST + w * 64 + (lane & 31) * 2) = f2bf(cv);
    const bf8 a0 = as_bf8(lds16(lds, L_VST + w * 64 + gq * 16)), a1 = as_bf8(lds16(lds, L_VH + gq * 16));
    const u32 bo0 = L_WO + (8u * gq + qq) * 896u + (u32)w * 128u + pp * 8u;
    const u32 bo1 = L_W2 + ((8u * gq + qq) & 15u) * 896u + (u32)w * 128u + pp * 8u;
#pragma unroll
    for (int n = 0; n < 4; ++n) {
      v4f acc = {0.f, 0.f, 0.f, 0.f};
      acc = __builtin_amdgcn_mfma_f32_16x16x32_bf16(a0, tr_pair(lds, bo0 + n * 32, bo0 + n * 32 + 3584), acc, 0, 0, 0);
      acc = __builtin_amdgcn_mfma_f32_16x16x32_bf16(a1, tr_pair(lds, bo1 + n * 32, bo1 + n * 32 + 3584), acc, 0, 0, 0);
      if (lane < 16) st_gran(ws, WS_OG + (u32)((b2i * 128 + hd) * M_ + w * 64 + n * 16 + lane) * 8u, tag, __float_as_uint(acc[0]));
    }
  }
  STAMP(12);
  if (i < 112) {
    const int fb = i / 56, fc = i - fb * 56;
    const u32 oo = WS_OG + ((u32)(fb * 128 + (t >> 2)) * M_ + (u32)(fc * 8 + (t & 3) * 2)) * 8u;
    float xr = 0.f;
    if (t < 8) xr = bf2f(ldh(x + fb * M_ + fc * 8 + t)) + 0.f;
    float br = 0.f;
    if (t < 8) br = bf2f(ldh(b2 + fc * 8 + t));
    v4u ov;
    POLL1(ov, oo, 5u)
    float c0 = __uint_as_float(ov[0]), c1 = __uint_as_float(ov[2]);
    c0 = ror_add<4>(c0); c1 = ror_add<4>(c1);
    c0 = ror_add<8>(c0); c1 = ror_add<8>(c1);
    c0 = x32_add(x16_add(c0)); c1 = x32_add(x16_add(c1));
    if (lane < 4) *reinterpret_cast<v2u*>(lds + L_FR + w * 32 + lane * 8) = v2u{__float_as_uint(c0), __float_as_uint(c1)};
    sbar(lb + F_B + 16, lane);
    STAMP(13);
    if (t < 8) {
      float a = 0.f;
#pragma unroll
      for (int q = 0; q < 8; ++q) a += reinterpret_cast<const float*>(lds + L_FR)[q * 8 + t];
      *(GAS u16*)(out + fb * M_ + fc * 8 + t) = f2bf(xr + a + br);
    }
    if (i == 0 && t == 0) __hip_atomic_store((gu32*)(ws + WS_EP), tag, RLX_AGENT);
    STAMP(14);
  }
  STAMP_FLUSH;
}

extern "C" void kernel_launch(void* const* d_in, const int* in_sizes, int n_in,
                              void* d_out, int out_size, void* d_ws, size_t ws_size,
                              hipStream_t stream) {
  const u16* x = (const u16*)d_in[0];
  const u16* ckv = (const u16*)d_in[1];
  const u16* wq = (const u16*)d_in[2];
  const u16* wuk = (const u16*)d_in[3];
  const u16* wuv = (const u16*)d_in[4];
  const u16* wo = (const u16*)d_in[5];
  const u16* w1 = (const u16*)d_in[7];
  const u16* b1 = (const u16*)d_in[8];
  const u16* w2 = (const u16*)d_in[9];
  const u16* b2 = (const u16*)d_in[10];
  mla_decode<<<256, 576, 0, stream>>>(x, ckv, wq, wuk, wuv, wo, w1, b1, w2, b2, (char*)d_ws, (u16*)d_out);
}
```

```cpp
#define MEAS 0
#define MWG 0
#define MWAVE 0
#include <hip/hip_runtime.h>
#include <hip/hip_bf16.h>
#include <stdint.h>

typedef unsigned short u16;
typedef unsigned int u32;
typedef unsigned long long u64;
typedef __attribute__((ext_vector_type(4))) unsigned v4u;
typedef __attribute__((ext_vector_type(2))) unsigned v2u;
typedef __attribute__((ext_vector_type(4))) float v4f;
typedef __attribute__((ext_vector_type(8))) __bf16 bf8;
typedef __attribute__((ext_vector_type(4))) short s4;
typedef __attribute__((ext_vector_type(8))) short s8;

#define GAS __attribute__((address_space(1)))
#define LAS __attribute__((address_space(3)))
typedef GAS u32 gu32;
typedef GAS u64 gu64;
#define RLX_AGENT __ATOMIC_RELAXED, __HIP_MEMORY_SCOPE_AGENT

#define M_ 448
#define F_ 1792
#define DC 1024
#define SK 4096

constexpr u32 SPIN_CAP = 1u << 18;

constexpr u32 WS_EP = 0;
constexpr u32 WS_QG = 4096;
constexpr u32 WS_QL = 65536;
constexpr u32 WS_SC = 262144;
constexpr u32 WS_PV = 1310720;
constexpr u32 WS_ML = 3407872;
constexpr u32 WS_CP = 3416064;
constexpr u32 WS_OG = 4464640;
constexpr u32 WS_END = WS_OG + 2 * 128 * 448 * 8;

#define STAMP(k) do { if ((k) == 0) st0 = __builtin_amdgcn_s_memrealtime(); if ((k) == MEAS) st1 = __builtin_amdgcn_s_memrealtime(); } while (0)
#define STAMP_DECL u64 st0 = 0, st1 = 0;
#define STAMP_FLUSH do { if (blockIdx.x == MWG && w == MWAVE) { const u64 te = __builtin_amdgcn_s_memrealtime() + 2 * (st1 - st0); for (u32 n_ = 0; n_ < 100000u; ++n_) { if (__builtin_amdgcn_s_memrealtime() >= te) break; __builtin_amdgcn_s_sleep(1); } } } while (0)

#define BAR() do { asm volatile("s_waitcnt lgkmcnt(0)" ::: "memory"); __builtin_amdgcn_s_barrier(); asm volatile("" ::: "memory"); } while (0)

__device__ __forceinline__ float bf2f(u16 v) { return __uint_as_float(((u32)v) << 16); }
__device__ __forceinline__ u16 f2bf(float f) { __hip_bfloat16 b = __float2bfloat16(f); return *reinterpret_cast<u16*>(&b); }
__device__ __forceinline__ float rbf(float f) { return bf2f(f2bf(f)); }
__device__ __forceinline__ float lo_f(u32 v) { return __uint_as_float(v << 16); }
__device__ __forceinline__ float hi_f(u32 v) { return __uint_as_float(v & 0xffff0000u); }
__device__ __forceinline__ u32 pk2(float a, float b) { return (u32)f2bf(a) | ((u32)f2bf(b) << 16); }
__device__ __forceinline__ v4u ld16(const u16* p) { return __builtin_nontemporal_load((const GAS v4u*)p); }
__device__ __forceinline__ v4u ldp16(const u16* p) { return *(const GAS v4u*)p; }
__device__ __forceinline__ u16 ldh(const u16* p) { return *(const GAS u16*)p; }
__device__ __forceinline__ bf8 as_bf8(v4u v) { return __builtin_bit_cast(bf8, v); }

template <int CTRL>
__device__ __forceinline__ float dppf(float v) {
  return __int_as_float(__builtin_amdgcn_update_dpp(0, __float_as_int(v), CTRL, 0xf, 0xf, true));
}
template <int N> __device__ __forceinline__ float ror_add(float v) { return v + dppf<0x120 + N>(v); }
template <int N> __device__ __forceinline__ float ror_max(float v) { return fmaxf(v, dppf<0x120 + N>(v)); }
__device__ __forceinline__ float x16_add(float v) {
  const u32 b = __float_as_uint(v);
  auto p = __builtin_amdgcn_permlane16_swap(b, b, false, false);
  return __uint_as_float(p[0]) + __uint_as_float(p[1]);
}
__device__ __forceinline__ float x32_add(float v) {
  const u32 b = __float_as_uint(v);
  auto p = __builtin_amdgcn_permlane32_swap(b, b, false, false);
  return __uint_as_float(p[0]) + __uint_as_float(p[1]);
}
__device__ __forceinline__ float x16_max(float v) {
  const u32 b = __float_as_uint(v);
  auto p = __builtin_amdgcn_permlane16_swap(b, b, false, false);
  return fmaxf(__uint_as_float(p[0]), __uint_as_float(p[1]));
}
__device__ __forceinline__ float x32_max(float v) {
  const u32 b = __float_as_uint(v);
  auto p = __builtin_amdgcn_permlane32_swap(b, b, false, false);
  return fmaxf(__uint_as_float(p[0]), __uint_as_float(p[1]));
}
__device__ __forceinline__ float wave_sum(float v) {
  v = ror_add<8>(v); v = ror_add<4>(v); v = ror_add<2>(v); v = ror_add<1>(v);
  return x32_add(x16_add(v));
}
__device__ __forceinline__ float wave_max(float v) {
  v = ror_max<8>(v); v = ror_max<4>(v); v = ror_max<2>(v); v = ror_max<1>(v);
  return x32_max(x16_max(v));
}

__device__ __forceinline__ u32 swz(u32 row) { return ((row & 3u) << 2) | ((row >> 2) & 3u); }
__device__ __forceinline__ u32 off_b(u32 row, u32 ch) { return 256u * row + 16u * (ch ^ swz(row)); }

constexpr u32 L_RA = 0;
constexpr u32 L_UV = 65536;
constexpr u32 L_WO = 98304;
constexpr u32 L_WQ = L_WO;
constexpr u32 L_W1 = L_WO + 14336;
constexpr u32 L_W2 = 126976;
constexpr u32 L_XN = 141312;
constexpr u32 L_P = 143104;
constexpr u32 L_QST = L_P;
constexpr u32 L_SRED = L_P;
constexpr u32 L_DP = L_P + 16896;
constexpr u32 L_CR = L_DP + 2048;
constexpr u32 L_VST = L_CR + 512;
constexpr u32 L_VH = L_VST + 512;
constexpr u32 L_FR = L_VH + 64;
constexpr u32 L_FLAG = L_FR + 256;
constexpr u32 L_END = L_FLAG + 128;
static_assert(L_END <= 163840, "LDS");
constexpr u32 F_PROG = L_FLAG;
constexpr u32 F_PULL = L_FLAG + 4;
constexpr u32 F_UK = L_FLAG + 36;
constexpr u32 F_RO = L_FLAG + 40;
constexpr u32 F_SR = L_FLAG + 44;
constexpr u32 F_B = L_FLAG + 48;
constexpr u32 F_QP = L_FLAG + 68;
constexpr u32 F_XN = L_FLAG + 72;
constexpr int P_WQ = 14, P_UK = 46, P_W1 = 60, P_ROWS = 124, P_COL = 188, P_UV = 220, P_W2 = 234, P_WO = 262;

#define WIN 16
#define AUX_COL 2
#define PSLP 2
#define PRETOUCH 0
#define CPRIO 2
#define SPRIO 3
constexpr u32 LSPIN = 1u << 22;

#define SPIN_FAIL(code) if (++spins > SPIN_CAP) { if (lane == 0) __hip_atomic_store(tmo, (code), RLX_AGENT); break; } __builtin_amdgcn_s_sleep(PSLP); asm volatile("" ::: "memory");

#define POLL1(v, off, code) __builtin_amdgcn_s_setprio(0); _Pragma("unroll 1") for (u32 spins = 0;;) { v = __builtin_amdgcn_raw_buffer_load_b128(rw, (off), 0, 16); if (__all((v[1] == tag) & (v[3] == tag))) break; SPIN_FAIL(code) } __builtin_amdgcn_s_setprio(CPRIO);

#define POLLN(v, N, off, stride, code) { u32 pend = (1u << (N)) - 1u; __builtin_amdgcn_s_setprio(0); _Pragma("unroll 1") for (u32 spins = 0;;) { \
    _Pragma("unroll") for (int s_ = 0; s_ < (N); ++s_) if (pend >> s_ & 1u) v[s_] = __builtin_amdgcn_raw_buffer_load_b128(rw, (off) + s_ * (stride), 0, 16); \
    _Pragma("unroll") for (int s_ = 0; s_ < (N); ++s_) if (pend >> s_ & 1u) { if (__all((v[s_][1] == tag) & (v[s_][3] == tag))) pend &= ~(1u << s_); } \
    if (!pend) break; SPIN_FAIL(code) } __builtin_amdgcn_s_setprio(CPRIO); }

__device__ __forceinline__ void st_gran(char* ws, u32 off, u32 tag, u32 val) {
  __hip_atomic_store((gu64*)(ws + off), ((u64)tag << 32) | val, RLX_AGENT);
}
__device__ __forceinline__ bf8 tr_pair(char* lds, u32 a0, u32 a1) {
  const s4 b0 = __builtin_amdgcn_ds_read_tr16_b64_v4i16((LAS s4*)(lds + a0));
  const s4 b1 = __builtin_amdgcn_ds_read_tr16_b64_v4i16((LAS s4*)(lds + a1));
  const s8 bb = {b0[0], b0[1], b0[2], b0[3], b1[0], b1[1], b1[2], b1[3]};
  return __builtin_bit_cast(bf8, bb);
}
__device__ __forceinline__ v4u lds16(char* lds, u32 a) { return *reinterpret_cast<const v4u*>(lds + a); }
__device__ __forceinline__ v4f lds16f(char* lds, u32 a) { return *reinterpret_cast<const v4f*>(lds + a); }
__device__ __forceinline__ float fdiv(float a, float d, float r) {
  const float q = a * r;
  return fmaf(fmaf(-q, d, a), r, q);
}
__device__ __forceinline__ v4u frag(v4u v, int pa) {
  v4u o;
#pragma unroll
  for (int e = 0; e < 4; ++e) o[e] = (u32)__builtin_amdgcn_ds_bpermute(pa, (int)v[e]);
  return o;
}
__device__ __forceinline__ void gemv448(char* lds, u32 wt, int lane, float& a0, float& a1) {
  const u32 gq = lane >> 4, qq = (lane & 15) >> 2, pp = lane & 3;
  const u32 ao = L_XN + (u32)(lane & 1) * 896u + gq * 16u;
  const u32 bo = wt + (8u * gq + qq) * 32u + pp * 8u;
  v4f acc0 = {0.f, 0.f, 0.f, 0.f}, acc1 = {0.f, 0.f, 0.f, 0.f};
#pragma unroll
  for (int s = 0; s < 7; ++s) {
    acc0 = __builtin_amdgcn_mfma_f32_16x16x32_bf16(as_bf8(lds16(lds, ao + s * 64)), tr_pair(lds, bo + s * 1024, bo + s * 1024 + 128), acc0, 0, 0, 0);
    acc1 = __builtin_amdgcn_mfma_f32_16x16x32_bf16(as_bf8(lds16(lds, ao + (s + 7) * 64)), tr_pair(lds, bo + (s + 7) * 1024, bo + (s + 7) * 1024 + 128), acc1, 0, 0, 0);
  }
  a0 = acc0[0] + acc1[0]; a1 = acc0[1] + acc1[1];
}

__device__ __forceinline__ void fl_write(u32 a, u32 v) { asm volatile("ds_write_b32 %0, %1" :: "v"(a), "v"(v) : "memory"); }
__device__ __forceinline__ void fl_add(u32 a, int lane) { if (lane == 0) asm volatile("ds_add_u32 %0, %1" :: "v"(a), "v"(1u) : "memory"); }
__device__ __forceinline__ int fl_read(u32 a) {
  u32 v; asm volatile("ds_read_b32 %0, %1\n s_waitcnt lgkmcnt(0)" : "=v"(v) : "v"(a) : "memory");
  return (int)__builtin_amdgcn_readfirstlane(v);
}
__device__ __forceinline__ void fl_wait(u32 a, int v) {
#pragma unroll 1
  for (u32 n = 0; n < LSPIN; ++n) { if (fl_read(a) >= v) break; __builtin_amdgcn_s_sleep(0); }
}
__device__ __forceinline__ void sbar(u32 a, int lane) { fl_add(a, lane); fl_wait(a, 8); }
template <int K> __device__ __forceinline__ void drain(u32 fp, int q, int lo) {
  asm volatile("s_waitcnt vmcnt(%0)" :: "n"(K) : "memory");
  fl_write(fp, (u32)(q - K > lo ? q - K : lo));
  if constexpr (K > 0) drain<K - 1>(fp, q, lo);
}
__device__ __forceinline__ void gate(u32 fp, int q, u32 a, int v, int& lo) {
  if (fl_read(a) >= v) return;
  if (lo < q) drain<WIN - 2>(fp, q, lo);
  lo = q;
  fl_wait(a, v);
}
template <int N, int U, int XM, int SEG, int AUX>
__device__ __forceinline__ void stream(char* lds, u32 lb, const void* base, u32 bytes, u32 vb, u32 sb, u32 ss, u32 ld, int q0, int& lo) {
  const __amdgpu_buffer_rsrc_t rs = __builtin_amdgcn_make_buffer_rsrc((void*)base, 0, bytes, 0x00020000);
#pragma unroll 1
  for (u32 k = 0; k < (u32)(N / U); ++k) {
    const int qk = q0 + (int)(k * U);
    if (SEG == 2) gate(lb + F_PROG, qk, lb + F_PULL + k * 4u, 1, lo);
    const u32 sk = SEG == 1 ? sb + (k < 3u ? k + 1u : k == 3u ? 0u : k) * 256u : sb + k * (U * ss);
#pragma unroll
    for (u32 n = 0; n < (u32)U; ++n) {
      const int q = qk + (int)n;
      asm volatile("s_waitcnt vmcnt(%0)" :: "n"(WIN - 1) : "memory");
      fl_write(lb + F_PROG, (u32)(q - (WIN - 1) > lo ? q - (WIN - 1) : lo));
      const u32 xm = XM == 1 ? (n & 3u) << 4 : XM == 2 ? ((2u * n) & 15u) << 4 : XM == 3 ? (((n & 1u) << 2) | (((n >> 2) & 1u) << 3)) << 4 : 0u;
      __builtin_amdgcn_raw_ptr_buffer_load_lds(rs, (LAS void*)(lds + ld + k * (U * 1024u) + n * 1024u), 16, vb ^ xm, sk + n * ss, 0, AUX);
    }
  }
}

__global__ __launch_bounds__(576) void mla_decode(const u16* x, const u16* ckv, const u16* wq, const u16* wuk,
                                                  const u16* wuv, const u16* wo, const u16* w1, const u16* b1,
                                                  const u16* w2, const u16* b2, char* ws, u16* out) {
  __shared__ __attribute__((aligned(16))) char lds[L_END];
  const int t = threadIdx.x, lane = t & 63;
  const int w = __builtin_amdgcn_readfirstlane(t >> 6);
  const int i = blockIdx.x, xcd = i & 7, r = i >> 3;
  const int h1i = xcd * 2 + (r >> 4), cs = r & 15;
  const int G = xcd * 4 + (r >> 3), b = G >> 4, g = G & 15, j = r & 7;
  const int b2i = r & 1, hd = xcd * 16 + (r >> 1), ds = hd & 7;
  const int fsl = hd < 112 ? hd : 0;
  STAMP_DECL
  STAMP(0);
  asm volatile("" : "+s"(x), "+s"(ckv), "+s"(wq), "+s"(wuk), "+s"(wuv), "+s"(wo), "+s"(w1), "+s"(b1), "+s"(w2), "+s"(b2), "+s"(ws), "+s"(out));
  gu32* tmo = (gu32*)(ws + WS_EP + 128);
  const __amdgpu_buffer_rsrc_t rw = __builtin_amdgcn_make_buffer_rsrc(ws, 0, WS_END, 0x00020000);
  const u32 gq = lane >> 4, qq = (lane & 15) >> 2, pp = lane & 3;

  const u32 lb = (u32)(uintptr_t)(LAS char*)lds;

  if (w == 8) {
    __builtin_amdgcn_s_setprio(SPRIO);
    if (lane < 32) fl_write(lb + L_FLAG + lane * 4, 0u);
    BAR();
    int lo = 0;
    const u32 hb = (u32)lane >> 5;
    const u32 vk = gq * 2048u + (((u32)lane & 15u) ^ (gq << 2)) * 16u;
    stream<14, 7, 0, 0, 0>(lds, lb, wq, M_ * 8192u, (u32)(lane >> 1) * 8192u + (u32)(lane & 1) * 16u, (u32)(h1i * 256 + cs * 16) * 2u, 32u * 8192u, L_WQ, 0, lo);
    stream<32, 8, 2, 0, 2>(lds, lb, wuk, 1024u * 8192u, hb * 8192u + (((u32)lane & 31u) ^ hb) * 16u, (u32)(cs * 64) * 8192u + (u32)h1i * 512u, 16384u, L_UV, P_WQ, lo);
    STAMP(24);
    stream<14, 7, 0, 0, 0>(lds, lb, w1, M_ * F_ * 2u, (u32)(lane >> 1) * (F_ * 2u) + (u32)(lane & 1) * 16u, (u32)fsl * 32u, 32u * F_ * 2u, L_W1, P_UK, lo);
    stream<64, 8, 1, 1, 0>(lds, lb, ckv, 2u * SK * DC * 2u, vk, (u32)(b * SK + g * 256 + j * 32) * 2048u, 8192u, L_RA, P_W1, lo);
    STAMP(25);
    stream<64, 8, 1, 2, AUX_COL>(lds, lb, ckv, 2u * SK * DC * 2u, vk, (u32)(b * SK + g * 256) * 2048u + (u32)j * 256u, 8192u, L_RA, P_ROWS, lo);
    STAMP(26);
    gate(lb + F_PROG, P_COL, lb + F_UK, 4, lo);
    stream<32, 8, 3, 0, 2>(lds, lb, wuv, 1024u * 8192u, hb * 8192u + (((u32)lane & 31u) ^ (hb << 1)) * 16u, (u32)(cs * 64) * 8192u + (u32)h1i * 512u, 16384u, L_UV, P_COL, lo);
    stream<14, 7, 0, 0, 2>(lds, lb, w2, 1792u * 896u, (u32)lane * 16u, (u32)(fsl * 16) * 896u, 1024u, L_W2, P_UV, lo);
    STAMP(27);
    gate(lb + F_PROG, P_W2, lb + F_RO, 2, lo);
    STAMP(28);
    stream<28, 7, 0, 0, 2>(lds, lb, wo, 4096u * 896u, (u32)lane * 16u, (u32)(hd * 32) * 896u, 1024u, L_WO, P_W2, lo);
    drain<WIN - 1>(lb + F_PROG, P_WO, lo);
    STAMP(29);
    STAMP_FLUSH;
    return;
  }

  __builtin_amdgcn_s_setprio(CPRIO);
  const u32 epv = __builtin_amdgcn_raw_buffer_load_b32(rw, WS_EP, 0, 0);
  const int pa = (4 * (lane & 15) + (lane >> 4)) * 4;
  float bias = 0.f;
  u32 tag;
  if (w == 0) {
    const int lc = lane < 56 ? lane : 0;
    const v4u xr0 = ldp16(x + lc * 8), xr1 = ldp16(x + M_ + lc * 8);
    STAMP(21);
    BAR();
    float xa[8], xb[8];
#pragma unroll
    for (int p = 0; p < 4; ++p) { xa[2 * p] = lo_f(xr0[p]); xa[2 * p + 1] = hi_f(xr0[p]); xb[2 * p] = lo_f(xr1[p]); xb[2 * p + 1] = hi_f(xr1[p]); }
    const float act = lane < 56 ? 1.f : 0.f;
    float s0 = 0.f, s1 = 0.f;
#pragma unroll
    for (int k = 0; k < 8; ++k) { s0 += xa[k]; s1 += xb[k]; }
    s0 = wave_sum(s0 * act); s1 = wave_sum(s1 * act);
    const float m0 = s0 * (1.f / M_), m1 = s1 * (1.f / M_);
    float v0 = 0.f, v1 = 0.f;
#pragma unroll
    for (int k = 0; k < 8; ++k) { const float d0 = xa[k] - m0, d1 = xb[k] - m1; v0 += d0 * d0; v1 += d1 * d1; }
    v0 = wave_sum(v0 * act); v1 = wave_sum(v1 * act);
    const float mb0 = rbf(m0), mb1 = rbf(m1);
    const float den0 = rbf(sqrtf(rbf(rbf(v0 * (1.f / M_)) + 1e-5f)));
    const float den1 = rbf(sqrtf(rbf(rbf(v1 * (1.f / M_)) + 1e-5f)));
    const float r0 = __builtin_amdgcn_rcpf(den0), r1 = __builtin_amdgcn_rcpf(den1);
    v4u na, nb;
#pragma unroll
    for (int p = 0; p < 4; ++p) {
      na[p] = pk2(fdiv(rbf(xa[2 * p] - mb0), den0, r0), fdiv(rbf(xa[2 * p + 1] - mb0), den0, r0));
      nb[p] = pk2(fdiv(rbf(xb[2 * p] - mb1), den1, r1), fdiv(rbf(xb[2 * p + 1] - mb1), den1, r1));
    }
    if (lane < 56) {
      *reinterpret_cast<v4u*>(lds + L_XN + lane * 16) = na;
      *reinterpret_cast<v4u*>(lds + L_XN + 896 + lane * 16) = nb;
    }
    fl_add(lb + F_XN, lane);
    STAMP(17);
    fl_wait(lb + F_PROG, P_WQ);
    STAMP(18);
    float a0, a1;
    gemv448(lds, L_WQ, lane, a0, a1);
    tag = __builtin_amdgcn_readfirstlane(epv) + 1u;
    const float o0 = dppf<0xB1>(a0), o1 = dppf<0xB1>(a1);
    if (lane < 16) {
      const int bb = lane & 1;
      st_gran(ws, WS_QG + (u32)((h1i * 2 + bb) * 128 + cs * 8 + (lane >> 1)) * 8u, tag, bb ? pk2(o1, a1) : pk2(a0, o0));
    }
    STAMP(1);
    fl_add(lb + F_RO, lane);
  } else if (w < 4) {
    if (w == 1) bias = bf2f(ldh(b1 + fsl * 16 + (lane & 15)));
    BAR();
    tag = __builtin_amdgcn_readfirstlane(epv) + 1u;
  } else {
    v4u wk[8];
    BAR();
    fl_wait(lb + F_PROG, P_UK);
    {
      const u32 ko = L_UV + (u32)((w - 4) * 16 + (lane & 15)) * 512u;
#pragma unroll
      for (int s = 0; s < 8; ++s) wk[s] = lds16(lds, ko + ((((u32)s * 4u + gq) ^ ((u32)lane & 15u)) * 16u));
    }
    fl_add(lb + F_UK, lane);
    STAMP(19);
    tag = __builtin_amdgcn_readfirstlane(epv) + 1u;
    const u32 qo = WS_QG + (u32)h1i * 2048u + (u32)lane * 16u;
    v4u ga[2];
    POLLN(ga, 2, qo, 1024u, 1u)
    const v4u g0 = ga[0], g1 = ga[1];
    STAMP(20);
    const u32 qs = L_QST + (u32)(w - 4) * 1024u;
    *reinterpret_cast<v2u*>(lds + qs + lane * 8) = v2u{g0[0], g0[2]};
    *reinterpret_cast<v2u*>(lds + qs + 512 + lane * 8) = v2u{g1[0], g1[2]};
    const u32 ao = qs + (u32)(lane & 1) * 512u + gq * 16u;
    v4f acc0 = {0.f, 0.f, 0.f, 0.f}, acc1 = {0.f, 0.f, 0.f, 0.f};
#pragma unroll
    for (int s = 0; s < 4; ++s) {
      acc0 = __builtin_amdgcn_mfma_f32_16x16x32_bf16(as_bf8(lds16(lds, ao + s * 64)), as_bf8(wk[s]), acc0, 0, 0, 0);
      acc1 = __builtin_amdgcn_mfma_f32_16x16x32_bf16(as_bf8(lds16(lds, ao + (s + 4) * 64)), as_bf8(wk[s + 4]), acc1, 0, 0, 0);
    }
    const float a0 = acc0[0] + acc1[0], a1 = acc0[1] + acc1[1];
    const float o0 = dppf<0xB1>(a0), o1 = dppf<0xB1>(a1);
    if (lane < 16) {
      const int bb = lane & 1, wq4 = w - 4;
      const u32 qa = (u32)bb * 65536u + (u32)((cs * 2 + (wq4 >> 1)) * 2 + ((lane >> 2) & 1)) * 1024u + (u32)h1i * 64u
                     + (u32)((wq4 & 1) * 2 + (lane >> 3)) * 16u + (u32)((lane >> 1) & 1) * 8u;
      st_gran(ws, WS_QL + qa, tag, bb ? pk2(o1, a1) : pk2(a0, o0));
    }
    STAMP(2);
    fl_add(lb + F_QP, lane);
  }
  if (w == 1) {
    float a0, a1;
    fl_wait(lb + F_XN, 1);
    fl_wait(lb + F_PROG, P_W1);
    gemv448(lds, L_W1, lane, a0, a1);
    fl_add(lb + F_RO, lane);
    const float hv = rbf(rbf(b2i ? a1 : a0) + bias);
    const float ge = 0.5f * hv * (1.f + tanhf(0.7978845608028654f * (hv + 0.044715f * hv * hv * hv)));
    if (lane < 32) *reinterpret_cast<u16*>(lds + L_VH + lane * 2) = (lane < 16 && hd < 112) ? f2bf(ge) : (u16)0;
  }
  v4u bv[8];
  {
    const int slot = w == 0 ? 3 : w < 4 ? w - 1 : w;
    fl_wait(lb + F_PROG, P_W1 + 8 * (slot + 1));
    const u32 key = (u32)lane & 15u;
    const u32 ro = L_RA + (u32)slot * 8192u + key * 256u;
#pragma unroll
    for (int s = 0; s < 8; ++s) bv[s] = lds16(lds, ro + (u32)(s >> 2) * 4096u + ((((u32)(s & 3) * 4u + gq) ^ swz(key)) * 16u));
    fl_add(lb + F_PULL + (u32)slot * 4u, lane);
  }
  STAMP(3);
  {
    const u32 qo = WS_QL + (u32)b * 65536u + (u32)w * 8192u + (u32)lane * 16u;
    v4u gl[8];
    fl_wait(lb + F_QP, 4);
    POLLN(gl, 8, qo, 1024u, 2u)
    STAMP(4);
    v4f acc0 = {0.f, 0.f, 0.f, 0.f}, acc1 = {0.f, 0.f, 0.f, 0.f};
#pragma unroll
    for (int s = 0; s < 4; ++s) {
      const v4u av = frag(v4u{gl[2 * s][0], gl[2 * s][2], gl[2 * s + 1][0], gl[2 * s + 1][2]}, pa);
      acc0 = __builtin_amdgcn_mfma_f32_16x16x32_bf16(as_bf8(av), as_bf8(bv[s]), acc0, 0, 0, 0);
      acc1 = __builtin_amdgcn_mfma_f32_16x16x32_bf16(as_bf8(av), as_bf8(bv[4 + s]), acc1, 0, 0, 0);
    }
    float* sr = reinterpret_cast<float*>(lds + L_SRED) + (w * 16 + gq * 4) * 33 + (lane & 15);
#pragma unroll
    for (int q = 0; q < 4; ++q) { sr[q * 33] = acc0[q]; sr[q * 33 + 16] = acc1[q]; }
  }
  sbar(lb + F_B, lane);
  STAMP(5);
  {
    const int hq = t >> 5, key = t & 31;
    const float* sr = reinterpret_cast<const float*>(lds + L_SRED) + hq * 33 + key;
    float s = 0.f;
#pragma unroll
    for (int ww = 0; ww < 8; ++ww) s += sr[ww * 16 * 33];
    s *= 0.0625f;
    st_gran(ws, WS_SC + (u32)((G * 16 + hq) * 256 + j * 32 + key) * 8u, tag, __float_as_uint(s));
    fl_add(lb + F_SR, lane);
  }
  {
    float ml[4];
    const u32 so = WS_SC + (u32)((G * 16 + 2 * w) * 256 + 2 * lane) * 8u;
    v4u gs[4];
    POLLN(gs, 4, so, 1024u, 3u)
    STAMP(6);
    fl_wait(lb + F_SR, 8);
#pragma unroll
    for (int hh = 0; hh < 2; ++hh) {
      const float sv[4] = {__uint_as_float(gs[2 * hh][0]), __uint_as_float(gs[2 * hh][2]), __uint_as_float(gs[2 * hh + 1][0]), __uint_as_float(gs[2 * hh + 1][2])};
      const float m = wave_max(fmaxf(fmaxf(sv[0], sv[1]), fmaxf(sv[2], sv[3])));
      const u32 p0 = pk2(__expf(sv[0] - m), __expf(sv[1] - m)), p1 = pk2(__expf(sv[2] - m), __expf(sv[3] - m));
      const float l = wave_sum((lo_f(p0) + hi_f(p0)) + (lo_f(p1) + hi_f(p1)));
      *reinterpret_cast<u32*>(lds + L_P + (2 * w + hh) * 528 + lane * 4) = p0;
      *reinterpret_cast<u32*>(lds + L_P + (2 * w + hh) * 528 + 256 + lane * 4) = p1;
      ml[2 * hh] = m; ml[2 * hh + 1] = l;
    }
    if (j == 0 && lane < 4) {
      const float v = lane == 0 ? ml[0] : lane == 1 ? ml[1] : lane == 2 ? ml[2] : ml[3];
      st_gran(ws, WS_ML + (u32)(((b * 16 + 2 * w + (lane >> 1)) * 16 + g) * 2 + (lane & 1)) * 8u, tag, __float_as_uint(v));
    }
  }
  sbar(lb + F_B + 4, lane);
  STAMP(22);
  fl_wait(lb + F_PROG, P_COL);
  STAMP(7);
  {
    v4f acc0 = {0.f, 0.f, 0.f, 0.f}, acc1 = {0.f, 0.f, 0.f, 0.f};
    const u32 po = L_P + (u32)(lane & 15) * 528u + gq * 16u;
    const u32 k0 = L_RA + off_b(8 * gq + qq, 2 * w + (pp >> 1)) + 8 * (pp & 1);
    const u32 k1 = L_RA + off_b(8 * gq + 4 + qq, 2 * w + (pp >> 1)) + 8 * (pp & 1);
#pragma unroll
    for (int ks = 0; ks < 4; ++ks) {
      acc0 = __builtin_amdgcn_mfma_f32_16x16x32_bf16(as_bf8(lds16(lds, po + ks * 64)), tr_pair(lds, k0 + ks * 8192, k1 + ks * 8192), acc0, 0, 0, 0);
      acc1 = __builtin_amdgcn_mfma_f32_16x16x32_bf16(as_bf8(lds16(lds, po + (ks + 4) * 64)), tr_pair(lds, k0 + (ks + 4) * 8192, k1 + (ks + 4) * 8192), acc1, 0, 0, 0);
    }
    const v4f acc = acc0 + acc1;
    const int hi8 = (lane >> 3) & 1;
    const float s0 = hi8 ? acc[0] : acc[2], s1 = hi8 ? acc[1] : acc[3];
    const float n0 = dppf<0x128>(s0), n1 = dppf<0x128>(s1);
    const u32 g0 = hi8 ? pk2(n0, acc[2]) : pk2(acc[0], n0);
    const u32 g1 = hi8 ? pk2(n1, acc[3]) : pk2(acc[1], n1);
    const int hq = (lane >> 4) * 4 + 2 * hi8;
    const u32 go = WS_PV + (u32)(((b * 16 + hq) * 16 + g) * 512 + j * 64 + w * 8 + (lane & 7)) * 8u;
    st_gran(ws, go, tag, g0);
    st_gran(ws, go + 16u * 512u * 8u, tag, g1);
  }
  STAMP(8);
  {
    const int bq = w >> 2;
    const u32 gg = (u32)(w & 3) * 4u + gq;
    const u32 po = WS_PV + (((u32)(bq * 16 + h1i) * 16u + gg) * 512u + (u32)cs * 32u + (u32)(lane & 15) * 2u) * 8u;
    gu64* mlp = (gu64*)(ws + WS_ML) + (bq * 16 + h1i) * 32 + (lane & 31);
    v4u pv;
    u64 mlv;
#pragma unroll 1
    for (u32 spins = 0;;) {
      mlv = __hip_atomic_load(mlp, RLX_AGENT);
      if (__all((u32)(mlv >> 32) == tag)) break;
      SPIN_FAIL(4u)
    }
    const float mv = __uint_as_float((u32)mlv);
    const float other = dppf<0xB1>(mv);
    const float mg = (lane & 1) ? other : mv, lg = (lane & 1) ? mv : other;
    const float Mx = wave_max(mg);
    const float wg = __expf(mg - Mx);
    const float Ls = wave_sum(wg * lg) * 0.25f;
    const float wn = wg / Ls;
    const float wv = __int_as_float(__builtin_amdgcn_ds_bpermute((int)gg * 8, __float_as_int(wn)));
    POLL1(pv, po, 4u)
    STAMP(9);
    float a[4] = {wv * lo_f(pv[0]), wv * lo_f(pv[2]), wv * hi_f(pv[0]), wv * hi_f(pv[2])};
#pragma unroll
    for (int q = 0; q < 4; ++q) a[q] = x32_add(x16_add(a[q]));
    if (lane < 16) {
      float* dp = reinterpret_cast<float*>(lds + L_DP) + w * 64 + (lane >> 2) * 16 + (lane & 3) * 2;
      dp[0] = a[0]; dp[1] = a[1]; dp[8] = a[2]; dp[9] = a[3];
    }
  }
  sbar(lb + F_B + 8, lane);
  STAMP(23);
  fl_wait(lb + F_PROG, P_UV);
  STAMP(10);
  {
    v4f acc0 = {0.f, 0.f, 0.f, 0.f}, acc1 = {0.f, 0.f, 0.f, 0.f};
    const u32 dpo = L_DP + (u32)((lane >> 2) & 1) * 1024u + gq * 32u;
    const u32 rr = 8u * gq + qq;
    const u32 f = (qq << 1) | ((gq & 1u) << 3);
    const u32 bo0 = L_UV + rr * 512u + ((((u32)w * 4u + (pp >> 1)) ^ f) * 16u) + (pp & 1u) * 8u;
    const u32 bo1 = L_UV + rr * 512u + ((((u32)w * 4u + 2u + (pp >> 1)) ^ f) * 16u) + (pp & 1u) * 8u;
#pragma unroll
    for (int s = 0; s < 2; ++s) {
      v4f c0 = {0.f, 0.f, 0.f, 0.f}, c1 = {0.f, 0.f, 0.f, 0.f};
#pragma unroll
      for (int ww = 0; ww < 4; ++ww) { c0 += lds16f(lds, dpo + ww * 256 + s * 128); c1 += lds16f(lds, dpo + ww * 256 + s * 128 + 16); }
      v4u av = {pk2(c0[0], c0[1]), pk2(c0[2], c0[3]), pk2(c1[0], c1[1]), pk2(c1[2], c1[3])};
      if (lane & 1) {
        av = v4u{pk2(c0[0] - lo_f(av[0]), c0[1] - hi_f(av[0])), pk2(c0[2] - lo_f(av[1]), c0[3] - hi_f(av[1])),
                 pk2(c1[0] - lo_f(av[2]), c1[1] - hi_f(av[2])), pk2(c1[2] - lo_f(av[3]), c1[3] - hi_f(av[3]))};
      }
      acc0 = __builtin_amdgcn_mfma_f32_16x16x32_bf16(as_bf8(av), tr_pair(lds, bo0 + s * 16384, bo0 + s * 16384 + 2048), acc0, 0, 0, 0);
      acc1 = __builtin_amdgcn_mfma_f32_16x16x32_bf16(as_bf8(av), tr_pair(lds, bo1 + s * 16384, bo1 + s * 16384 + 2048), acc1, 0, 0, 0);
    }
    if (lane < 32) {
      const u32 co = WS_CP + ((((u32)(h1i * 2) + gq) * 8u + (u32)w) * 16u + (u32)cs) * 256u + (u32)(lane & 15) * 8u;
      st_gran(ws, co, tag, __float_as_uint(acc0[0] + acc0[1]));
      st_gran(ws, co + 128u, tag, __float_as_uint(acc1[0] + acc1[1]));
    }
  }
  STAMP(11);
  if (w < 4) {
    const u32 co = WS_CP + (u32)((h1i * 2 + b2i) * 8 + ds) * 4096u + (u32)t * 16u;
    v4u cv;
    POLL1(cv, co, 6u)
    const float c0 = x32_add(x16_add(__uint_as_float(cv[0]))), c1 = x32_add(x16_add(__uint_as_float(cv[2])));
    if (lane < 16) *reinterpret_cast<v2u*>(lds + L_CR + w * 128 + lane * 8) = v2u{__float_as_uint(c0), __float_as_uint(c1)};
  }
  STAMP(15);
  sbar(lb + F_B + 12, lane);
  fl_wait(lb + F_PROG, P_WO);
  STAMP(16);
  if (w < 7) {
    const float* cr = reinterpret_cast<const float*>(lds + L_CR) + (lane & 31);
    const float cv = (cr[0] + cr[32]) + (cr[64] + cr[96]);
    *reinterpret_cast<u16*>(lds + L_VST + w * 64 + (lane & 31) * 2) = f2bf(cv);
    const bf8 a0 = as_bf8(lds16(lds, L_VST + w * 64 + gq * 16)), a1 = as_bf8(lds16(lds, L_VH + gq * 16));
    const u32 bo0 = L_WO + (8u * gq + qq) * 896u + (u32)w * 128u + pp * 8u;
    const u32 bo1 = L_W2 + ((8u * gq + qq) & 15u) * 896u + (u32)w * 128u + pp * 8u;
#pragma unroll
    for (int n = 0; n < 4; ++n) {
      v4f acc = {0.f, 0.f, 0.f, 0.f};
      acc = __builtin_amdgcn_mfma_f32_16x16x32_bf16(a0, tr_pair(lds, bo0 + n * 32, bo0 + n * 32 + 3584), acc, 0, 0, 0);
      acc = __builtin_amdgcn_mfma_f32_16x16x32_bf16(a1, tr_pair(lds, bo1 + n * 32, bo1 + n * 32 + 3584), acc, 0, 0, 0);
      if (lane < 16) st_gran(ws, WS_OG + (u32)((b2i * 128 + hd) * M_ + w * 64 + n * 16 + lane) * 8u, tag, __float_as_uint(acc[0]));
    }
  }
  STAMP(12);
  if (i < 112) {
    const int fb = i / 56, fc = i - fb * 56;
    const u32 oo = WS_OG + ((u32)(fb * 128 + (t >> 2)) * M_ + (u32)(fc * 8 + (t & 3) * 2)) * 8u;
    float xr = 0.f;
    if (t < 8) xr = bf2f(ldh(x + fb * M_ + fc * 8 + t)) + 0.f;
    float br = 0.f;
    if (t < 8) br = bf2f(ldh(b2 + fc * 8 + t));
    v4u ov;
    POLL1(ov, oo, 5u)
    float c0 = __uint_as_float(ov[0]), c1 = __uint_as_float(ov[2]);
    c0 = ror_add<4>(c0); c1 = ror_add<4>(c1);
    c0 = ror_add<8>(c0); c1 = ror_add<8>(c1);
    c0 = x32_add(x16_add(c0)); c1 = x32_add(x16_add(c1));
    if (lane < 4) *reinterpret_cast<v2u*>(lds + L_FR + w * 32 + lane * 8) = v2u{__float_as_uint(c0), __float_as_uint(c1)};
    sbar(lb + F_B + 16, lane);
    STAMP(13);
    if (t < 8) {
      float a = 0.f;
#pragma unroll
      for (int q = 0; q < 8; ++q) a += reinterpret_cast<const float*>(lds + L_FR)[q * 8 + t];
      *(GAS u16*)(out + fb * M_ + fc * 8 + t) = f2bf(xr + a + br);
    }
    if (i == 0 && t == 0) __hip_atomic_store((gu32*)(ws + WS_EP), tag, RLX_AGENT);
    STAMP(14);
  }
  STAMP_FLUSH;
}

extern "C" void kernel_launch(void* const* d_in, const int* in_sizes, int n_in,
                              void* d_out, int out_size, void* d_ws, size_t ws_size,
                              hipStream_t stream) {
  const u16* x = (const u16*)d_in[0];
  const u16* ckv = (const u16*)d_in[1];
  const u16* wq = (const u16*)d_in[2];
  const u16* wuk = (const u16*)d_in[3];
  const u16* wuv = (const u16*)d_in[4];
  const u16* wo = (const u16*)d_in[5];
  const u16* w1 = (const u16*)d_in[7];
  const u16* b1 = (const u16*)d_in[8];
  const u16* w2 = (const u16*)d_in[9];
  const u16* b2 = (const u16*)d_in[10];
  mla_decode<<<256, 576, 0, stream>>>(x, ckv, wq, wuk, wuv, wo, w1, b1, w2, b2, (char*)d_ws, (u16*)d_out);
}
```

```cpp
#include <hip/hip_runtime.h>
#include <hip/hip_bf16.h>
#include <stdint.h>

typedef unsigned short u16;
typedef unsigned int u32;
typedef unsigned long long u64;
typedef __attribute__((ext_vector_type(4))) unsigned v4u;
typedef __attribute__((ext_vector_type(2))) unsigned v2u;
typedef __attribute__((ext_vector_type(4))) float v4f;
typedef __attribute__((ext_vector_type(8))) __bf16 bf8;
typedef __attribute__((ext_vector_type(4))) short s4;
typedef __attribute__((ext_vector_type(8))) short s8;

#define GAS __attribute__((address_space(1)))
#define LAS __attribute__((address_space(3)))
typedef GAS u32 gu32;
typedef GAS u64 gu64;
#define RLX_AGENT __ATOMIC_RELAXED, __HIP_MEMORY_SCOPE_AGENT

#define M_ 448
#define F_ 1792
#define DC 1024
#define SK 4096

constexpr u32 SPIN_CAP = 1u << 18;

constexpr u32 WS_EP = 0;
constexpr u32 WS_QG = 4096;
constexpr u32 WS_QL = 65536;
constexpr u32 WS_SC = 262144;
constexpr u32 WS_PV = 1310720;
constexpr u32 WS_ML = 3407872;
constexpr u32 WS_CP = 3416064;
constexpr u32 WS_OG = 4464640;
constexpr u32 WS_END = WS_OG + 2 * 128 * 448 * 8;

#define STAMP(k) do {} while (0)
#define STAMP_DECL
#define STAMP_FLUSH do {} while (0)

#define BAR() do { asm volatile("s_waitcnt lgkmcnt(0)" ::: "memory"); __builtin_amdgcn_s_barrier(); asm volatile("" ::: "memory"); } while (0)

__device__ __forceinline__ float bf2f(u16 v) { return __uint_as_float(((u32)v) << 16); }
__device__ __forceinline__ u16 f2bf(float f) { __hip_bfloat16 b = __float2bfloat16(f); return *reinterpret_cast<u16*>(&b); }
__device__ __forceinline__ float rbf(float f) { return bf2f(f2bf(f)); }
__device__ __forceinline__ float lo_f(u32 v) { return __uint_as_float(v << 16); }
__device__ __forceinline__ float hi_f(u32 v) { return __uint_as_float(v & 0xffff0000u); }
__device__ __forceinline__ u32 pk2(float a, float b) { return (u32)f2bf(a) | ((u32)f2bf(b) << 16); }
__device__ __forceinline__ v4u ld16(const u16* p) { return __builtin_nontemporal_load((const GAS v4u*)p); }
__device__ __forceinline__ v4u ldp16(const u16* p) { return *(const GAS v4u*)p; }
__device__ __forceinline__ u16 ldh(const u16* p) { return *(const GAS u16*)p; }
__device__ __forceinline__ bf8 as_bf8(v4u v) { return __builtin_bit_cast(bf8, v); }

template <int CTRL>
__device__ __forceinline__ float dppf(float v) {
  return __int_as_float(__builtin_amdgcn_update_dpp(0, __float_as_int(v), CTRL, 0xf, 0xf, true));
}
template <int N> __device__ __forceinline__ float ror_add(float v) { return v + dppf<0x120 + N>(v); }
template <int N> __device__ __forceinline__ float ror_max(float v) { return fmaxf(v, dppf<0x120 + N>(v)); }
__device__ __forceinline__ float x16_add(float v) {
  const u32 b = __float_as_uint(v);
  auto p = __builtin_amdgcn_permlane16_swap(b, b, false, false);
  return __uint_as_float(p[0]) + __uint_as_float(p[1]);
}
__device__ __forceinline__ float x32_add(float v) {
  const u32 b = __float_as_uint(v);
  auto p = __builtin_amdgcn_permlane32_swap(b, b, false, false);
  return __uint_as_float(p[0]) + __uint_as_float(p[1]);
}
__device__ __forceinline__ float x16_max(float v) {
  const u32 b = __float_as_uint(v);
  auto p = __builtin_amdgcn_permlane16_swap(b, b, false, false);
  return fmaxf(__uint_as_float(p[0]), __uint_as_float(p[1]));
}
__device__ __forceinline__ float x32_max(float v) {
  const u32 b = __float_as_uint(v);
  auto p = __builtin_amdgcn_permlane32_swap(b, b, false, false);
  return fmaxf(__uint_as_float(p[0]), __uint_as_float(p[1]));
}
__device__ __forceinline__ float wave_sum(float v) {
  v = ror_add<8>(v); v = ror_add<4>(v); v = ror_add<2>(v); v = ror_add<1>(v);
  return x32_add(x16_add(v));
}
__device__ __forceinline__ float wave_max(float v) {
  v = ror_max<8>(v); v = ror_max<4>(v); v = ror_max<2>(v); v = ror_max<1>(v);
  return x32_max(x16_max(v));
}

__device__ __forceinline__ u32 swz(u32 row) { return ((row & 3u) << 2) | ((row >> 2) & 3u); }
__device__ __forceinline__ u32 off_b(u32 row, u32 ch) { return 256u * row + 16u * (ch ^ swz(row)); }

constexpr u32 L_RA = 0;
constexpr u32 L_UV = 65536;
constexpr u32 L_WO = 98304;
constexpr u32 L_WQ = L_WO;
constexpr u32 L_W1 = L_WO + 14336;
constexpr u32 L_W2 = 126976;
constexpr u32 L_XN = 141312;
constexpr u32 L_P = 143104;
constexpr u32 L_QST = L_P;
constexpr u32 L_SRED = L_P;
constexpr u32 L_DP = L_P + 16896;
constexpr u32 L_CR = L_DP + 2048;
constexpr u32 L_VST = L_CR + 512;
constexpr u32 L_VH = L_VST + 512;
constexpr u32 L_FR = L_VH + 64;
constexpr u32 L_FLAG = L_FR + 256;
constexpr u32 L_END = L_FLAG + 128;
static_assert(L_END <= 163840, "LDS");
constexpr u32 F_PROG = L_FLAG;
constexpr u32 F_PULL = L_FLAG + 4;
constexpr u32 F_UK = L_FLAG + 36;
constexpr u32 F_RO = L_FLAG + 40;
constexpr u32 F_SR = L_FLAG + 44;
constexpr u32 F_B = L_FLAG + 48;
constexpr u32 F_QP = L_FLAG + 68;
constexpr u32 F_XN = L_FLAG + 72;
#define ORDER 0
constexpr int P_WQ = 14, P_UK = 46, P_W1 = 60, P_ROWS = 124;
constexpr int P_COL = 188, P_UV = 220, P_W2 = 234, P_WO = 262;
constexpr int P_END = 262;

#define WIN 16
#define WIN2 16
#define WINC 16
constexpr int WMAX = WIN > WIN2 ? (WIN > WINC ? WIN : WINC) : (WIN2 > WINC ? WIN2 : WINC);
#define AUX_COL 2
#define PSLP 2
#define PSTR 64
#define PRE_AUX 0
#define PRE_Q 0
#define PRE 46
#define CPRIO 2
#define SPRIO 3
constexpr u32 LSPIN = 1u << 22;

#define SPIN_FAIL(code) if (++spins > SPIN_CAP) { if (lane == 0) __hip_atomic_store(tmo, (code), RLX_AGENT); break; } __builtin_amdgcn_s_sleep(PSLP); asm volatile("" ::: "memory");

#define POLL1(v, off, code) __builtin_amdgcn_s_setprio(0); _Pragma("unroll 1") for (u32 spins = 0;;) { v = __builtin_amdgcn_raw_buffer_load_b128(rw, (off), 0, 16); if (__all((v[1] == tag) & (v[3] == tag))) break; SPIN_FAIL(code) } __builtin_amdgcn_s_setprio(CPRIO);

#define POLLN(v, N, off, stride, code) { u32 pend = (1u << (N)) - 1u; __builtin_amdgcn_s_setprio(0); _Pragma("unroll 1") for (u32 spins = 0;;) { \
    _Pragma("unroll") for (int s_ = 0; s_ < (N); ++s_) if (pend >> s_ & 1u) v[s_] = __builtin_amdgcn_raw_buffer_load_b128(rw, (off) + s_ * (stride), 0, 16); \
    _Pragma("unroll") for (int s_ = 0; s_ < (N); ++s_) if (pend >> s_ & 1u) { if (__all((v[s_][1] == tag) & (v[s_][3] == tag))) pend &= ~(1u << s_); } \
    if (!pend) break; SPIN_FAIL(code) } __builtin_amdgcn_s_setprio(CPRIO); }

__device__ __forceinline__ void st_gran(char* ws, u32 off, u32 tag, u32 val) {
  __hip_atomic_store((gu64*)(ws + off), ((u64)tag << 32) | val, RLX_AGENT);
}
__device__ __forceinline__ bf8 tr_pair(char* lds, u32 a0, u32 a1) {
  const s4 b0 = __builtin_amdgcn_ds_read_tr16_b64_v4i16((LAS s4*)(lds + a0));
  const s4 b1 = __builtin_amdgcn_ds_read_tr16_b64_v4i16((LAS s4*)(lds + a1));
  const s8 bb = {b0[0], b0[1], b0[2], b0[3], b1[0], b1[1], b1[2], b1[3]};
  return __builtin_bit_cast(bf8, bb);
}
__device__ __forceinline__ v4u lds16(char* lds, u32 a) { return *reinterpret_cast<const v4u*>(lds + a); }
__device__ __forceinline__ v4f lds16f(char* lds, u32 a) { return *reinterpret_cast<const v4f*>(lds + a); }
__device__ __forceinline__ float fdiv(float a, float d, float r) {
  const float q = a * r;
  return fmaf(fmaf(-q, d, a), r, q);
}
__device__ __forceinline__ v4u frag(v4u v, int pa) {
  v4u o;
#pragma unroll
  for (int e = 0; e < 4; ++e) o[e] = (u32)__builtin_amdgcn_ds_bpermute(pa, (int)v[e]);
  return o;
}
__device__ __forceinline__ void gemv448(char* lds, u32 wt, int lane, float& a0, float& a1) {
  const u32 gq = lane >> 4, qq = (lane & 15) >> 2, pp = lane & 3;
  const u32 ao = L_XN + (u32)(lane & 1) * 896u + gq * 16u;
  const u32 bo = wt + (8u * gq + qq) * 32u + pp * 8u;
  v4f acc0 = {0.f, 0.f, 0.f, 0.f}, acc1 = {0.f, 0.f, 0.f, 0.f};
#pragma unroll
  for (int s = 0; s < 7; ++s) {
    acc0 = __builtin_amdgcn_mfma_f32_16x16x32_bf16(as_bf8(lds16(lds, ao + s * 64)), tr_pair(lds, bo + s * 1024, bo + s * 1024 + 128), acc0, 0, 0, 0);
    acc1 = __builtin_amdgcn_mfma_f32_16x16x32_bf16(as_bf8(lds16(lds, ao + (s + 7) * 64)), tr_pair(lds, bo + (s + 7) * 1024, bo + (s + 7) * 1024 + 128), acc1, 0, 0, 0);
  }
  a0 = acc0[0] + acc1[0]; a1 = acc0[1] + acc1[1];
}

__device__ __forceinline__ void fl_write(u32 a, u32 v) { asm volatile("ds_write_b32 %0, %1" :: "v"(a), "v"(v) : "memory"); }
__device__ __forceinline__ void fl_add(u32 a, int lane) { if (lane == 0) asm volatile("ds_add_u32 %0, %1" :: "v"(a), "v"(1u) : "memory"); }
__device__ __forceinline__ int fl_read(u32 a) {
  u32 v; asm volatile("ds_read_b32 %0, %1\n s_waitcnt lgkmcnt(0)" : "=v"(v) : "v"(a) : "memory");
  return (int)__builtin_amdgcn_readfirstlane(v);
}
__device__ __forceinline__ void fl_wait(u32 a, int v) {
#pragma unroll 1
  for (u32 n = 0; n < LSPIN; ++n) { if (fl_read(a) >= v) break; __builtin_amdgcn_s_sleep(0); }
}
__device__ __forceinline__ void sbar(u32 a, int lane) { fl_add(a, lane); fl_wait(a, 8); }
template <int K> __device__ __forceinline__ void drain(u32 fp, int q, int lo) {
  asm volatile("s_waitcnt vmcnt(%0)" :: "n"(K) : "memory");
  fl_write(fp, (u32)(q - K > lo ? q - K : lo));
  if constexpr (K > 0) drain<K - 1>(fp, q, lo);
}
__device__ __forceinline__ void gate(u32 fp, int q, u32 a, int v, int& lo) {
  if (fl_read(a) >= v) return;
  if (lo < q) drain<WMAX - 2>(fp, q, lo);
  lo = q;
  fl_wait(a, v);
}
template <int N, int U, int XM, int SEG, int AUX, int W>
__device__ __forceinline__ void stream(char* lds, u32 lb, const void* base, u32 bytes, u32 vb, u32 sb, u32 ss, u32 ld, int q0, int& lo) {
  const __amdgpu_buffer_rsrc_t rs = __builtin_amdgcn_make_buffer_rsrc((void*)base, 0, bytes, 0x00020000);
#pragma unroll 1
  for (u32 k = 0; k < (u32)(N / U); ++k) {
    const int qk = q0 + (int)(k * U);
    if (SEG == 2) gate(lb + F_PROG, qk, lb + F_PULL + k * 4u, 1, lo);
    const u32 sk = SEG == 1 ? sb + (k < 3u ? k + 1u : k == 3u ? 0u : k) * 256u : sb + k * (U * ss);
#pragma unroll
    for (u32 n = 0; n < (u32)U; ++n) {
      const int q = qk + (int)n;
      asm volatile("s_waitcnt vmcnt(%0)" :: "n"(W - 1) : "memory");
      lo = q - (W - 1) > lo ? q - (W - 1) : lo;
      fl_write(lb + F_PROG, (u32)lo);
      const u32 xm = XM == 1 ? (n & 3u) << 4 : XM == 2 ? ((2u * n) & 15u) << 4 : XM == 3 ? (((n & 1u) << 2) | (((n >> 2) & 1u) << 3)) << 4 : 0u;
      __builtin_amdgcn_raw_ptr_buffer_load_lds(rs, (LAS void*)(lds + ld + k * (U * 1024u) + n * 1024u), 16, vb ^ xm, sk + n * ss, 0, AUX);
    }
  }
}

__global__ __launch_bounds__(576) void mla_decode(const u16* x, const u16* ckv, const u16* wq, const u16* wuk,
                                                  const u16* wuv, const u16* wo, const u16* w1, const u16* b1,
                                                  const u16* w2, const u16* b2, char* ws, u16* out) {
  __shared__ __attribute__((aligned(16))) char lds[L_END];
  const int t = threadIdx.x, lane = t & 63;
  const int w = __builtin_amdgcn_readfirstlane(t >> 6);
  const int i = blockIdx.x, xcd = i & 7, r = i >> 3;
  const int h1i = xcd * 2 + (r >> 4), cs = r & 15;
  const int G = xcd * 4 + (r >> 3), b = G >> 4, g = G & 15, j = r & 7;
  const int b2i = r & 1, hd = xcd * 16 + (r >> 1), ds = hd & 7;
  const int fsl = hd < 112 ? hd : 0;
  STAMP_DECL
  STAMP(0);
  asm volatile("" : "+s"(x), "+s"(ckv), "+s"(wq), "+s"(wuk), "+s"(wuv), "+s"(wo), "+s"(w1), "+s"(b1), "+s"(w2), "+s"(b2), "+s"(ws), "+s"(out));
  gu32* tmo = (gu32*)(ws + WS_EP + 128);
  const __amdgpu_buffer_rsrc_t rw = __builtin_amdgcn_make_buffer_rsrc(ws, 0, WS_END, 0x00020000);
  const u32 gq = lane >> 4, qq = (lane & 15) >> 2, pp = lane & 3;

  const u32 lb = (u32)(uintptr_t)(LAS char*)lds;

  if (w == 8) {
    __builtin_amdgcn_s_setprio(SPRIO);
    if (lane < 32) fl_write(lb + L_FLAG + lane * 4, 0u);
    BAR();
    int lo = 0;
    const u32 hb = (u32)lane >> 5;
    const u32 vk = gq * 2048u + (((u32)lane & 15u) ^ (gq << 2)) * 16u;
    stream<14, 7, 0, 0, 0, WIN>(lds, lb, wq, M_ * 8192u, (u32)(lane >> 1) * 8192u + (u32)(lane & 1) * 16u, (u32)(h1i * 256 + cs * 16) * 2u, 32u * 8192u, L_WQ, 0, lo);
    stream<32, 8, 2, 0, 2, WIN>(lds, lb, wuk, 1024u * 8192u, hb * 8192u + (((u32)lane & 31u) ^ hb) * 16u, (u32)(cs * 64) * 8192u + (u32)h1i * 512u, 16384u, L_UV, P_WQ, lo);
    STAMP(24);
    stream<14, 7, 0, 0, 0, WIN>(lds, lb, w1, M_ * F_ * 2u, (u32)(lane >> 1) * (F_ * 2u) + (u32)(lane & 1) * 16u, (u32)fsl * 32u, 32u * F_ * 2u, L_W1, P_UK, lo);
    stream<64, 8, 1, 1, 0, WIN>(lds, lb, ckv, 2u * SK * DC * 2u, vk, (u32)(b * SK + g * 256 + j * 32) * 2048u, 8192u, L_RA, P_W1, lo);
    STAMP(25);
#define S_COL stream<64, 8, 1, 2, AUX_COL, WINC>(lds, lb, ckv, 2u * SK * DC * 2u, vk, (u32)(b * SK + g * 256) * 2048u + (u32)j * 256u, 8192u, L_RA, P_COL - 64, lo); STAMP(26);
#define S_UV gate(lb + F_PROG, P_UV - 32, lb + F_UK, 4, lo); \
    stream<32, 8, 3, 0, 2, WIN2>(lds, lb, wuv, 1024u * 8192u, hb * 8192u + (((u32)lane & 31u) ^ (hb << 1)) * 16u, (u32)(cs * 64) * 8192u + (u32)h1i * 512u, 16384u, L_UV, P_UV - 32, lo); STAMP(27);
#define S_W2 stream<14, 7, 0, 0, 2, WIN2>(lds, lb, w2, 1792u * 896u, (u32)lane * 16u, (u32)(fsl * 16) * 896u, 1024u, L_W2, P_W2 - 14, lo);
#define S_WO gate(lb + F_PROG, P_WO - 28, lb + F_RO, 2, lo); \
    stream<28, 7, 0, 0, 2, WIN2>(lds, lb, wo, 4096u * 896u, (u32)lane * 16u, (u32)(hd * 32) * 896u, 1024u, L_WO, P_WO - 28, lo); STAMP(28);
    S_COL S_UV S_W2 S_WO
    drain<WMAX - 1>(lb + F_PROG, P_END, lo);
    STAMP(29);
    STAMP_FLUSH;
    return;
  }

  __builtin_amdgcn_s_setprio(CPRIO);
  const u32 epv = __builtin_amdgcn_raw_buffer_load_b32(rw, WS_EP, 0, 0);
  const int pa = (4 * (lane & 15) + (lane >> 4)) * 4;
  float bias = 0.f;
  u32 tag;
  if (w == 0) {
    const int lc = lane < 56 ? lane : 0;
    const v4u xr0 = ldp16(x + lc * 8), xr1 = ldp16(x + M_ + lc * 8);
    STAMP(21);
    BAR();
    float xa[8], xb[8];
#pragma unroll
    for (int p = 0; p < 4; ++p) { xa[2 * p] = lo_f(xr0[p]); xa[2 * p + 1] = hi_f(xr0[p]); xb[2 * p] = lo_f(xr1[p]); xb[2 * p + 1] = hi_f(xr1[p]); }
    const float act = lane < 56 ? 1.f : 0.f;
    float s0 = 0.f, s1 = 0.f;
#pragma unroll
    for (int k = 0; k < 8; ++k) { s0 += xa[k]; s1 += xb[k]; }
    s0 = wave_sum(s0 * act); s1 = wave_sum(s1 * act);
    const float m0 = s0 * (1.f / M_), m1 = s1 * (1.f / M_);
    float v0 = 0.f, v1 = 0.f;
#pragma unroll
    for (int k = 0; k < 8; ++k) { const float d0 = xa[k] - m0, d1 = xb[k] - m1; v0 += d0 * d0; v1 += d1 * d1; }
    v0 = wave_sum(v0 * act); v1 = wave_sum(v1 * act);
    const float mb0 = rbf(m0), mb1 = rbf(m1);
    const float den0 = rbf(sqrtf(rbf(rbf(v0 * (1.f / M_)) + 1e-5f)));
    const float den1 = rbf(sqrtf(rbf(rbf(v1 * (1.f / M_)) + 1e-5f)));
    const float r0 = __builtin_amdgcn_rcpf(den0), r1 = __builtin_amdgcn_rcpf(den1);
    v4u na, nb;
#pragma unroll
    for (int p = 0; p < 4; ++p) {
      na[p] = pk2(fdiv(rbf(xa[2 * p] - mb0), den0, r0), fdiv(rbf(xa[2 * p + 1] - mb0), den0, r0));
      nb[p] = pk2(fdiv(rbf(xb[2 * p] - mb1), den1, r1), fdiv(rbf(xb[2 * p + 1] - mb1), den1, r1));
    }
    if (lane < 56) {
      *reinterpret_cast<v4u*>(lds + L_XN + lane * 16) = na;
      *reinterpret_cast<v4u*>(lds + L_XN + 896 + lane * 16) = nb;
    }
    fl_add(lb + F_XN, lane);
    STAMP(17);
    fl_wait(lb + F_PROG, P_WQ);
    STAMP(18);
    float a0, a1;
    gemv448(lds, L_WQ, lane, a0, a1);
    tag = __builtin_amdgcn_readfirstlane(epv) + 1u;
    const float o0 = dppf<0xB1>(a0), o1 = dppf<0xB1>(a1);
    if (lane < 16) {
      const int bb = lane & 1;
      st_gran(ws, WS_QG + (u32)((h1i * 2 + bb) * 128 + cs * 8 + (lane >> 1)) * 8u, tag, bb ? pk2(o1, a1) : pk2(a0, o0));
    }
    STAMP(1);
    fl_add(lb + F_RO, lane);
  } else if (w < 4) {
    if (w == 1) bias = bf2f(ldh(b1 + fsl * 16 + (lane & 15)));
    BAR();
    if (w == 3) {
      constexpr int NPR = (20096 + 64 * PSTR - 1) / (64 * PSTR);
      u32 d[NPR + 1];
      if (PRE_Q == 0) d[NPR] = __builtin_amdgcn_raw_buffer_load_b32(rw, lane < 8 ? WS_QL + (u32)i * 512u + (u32)lane * 64u : WS_QG + (u32)i * 128u + (u32)(lane & 1) * 64u, 0, PRE_AUX);
      if (PRE > 1) fl_wait(lb + F_PROG, PRE);
      if (PRE_Q == 1) d[NPR] = __builtin_amdgcn_raw_buffer_load_b32(rw, lane < 8 ? WS_QL + (u32)i * 512u + (u32)lane * 64u : WS_QG + (u32)i * 128u + (u32)(lane & 1) * 64u, 0, PRE_AUX);
#pragma unroll
      for (int n = 0; n < NPR; ++n) d[n] = __builtin_amdgcn_raw_buffer_load_b32(rw, WS_SC + (u32)i * 20096u + (u32)lane * PSTR, n * 64 * PSTR, PRE_AUX);
#pragma unroll
      for (int n = 0; n <= NPR; ++n) asm volatile("" :: "v"(d[n]));
    }
    tag = __builtin_amdgcn_readfirstlane(epv) + 1u;
  } else {
    v4u wk[8];
    BAR();
    fl_wait(lb + F_PROG, P_UK);
    {
      const u32 ko = L_UV + (u32)((w - 4) * 16 + (lane & 15)) * 512u;
#pragma unroll
      for (int s = 0; s < 8; ++s) wk[s] = lds16(lds, ko + ((((u32)s * 4u + gq) ^ ((u32)lane & 15u)) * 16u));
    }
    fl_add(lb + F_UK, lane);
    STAMP(19);
    tag = __builtin_amdgcn_readfirstlane(epv) + 1u;
    const u32 qo = WS_QG + (u32)h1i * 2048u + (u32)lane * 16u;
    v4u ga[2];
    POLLN(ga, 2, qo, 1024u, 1u)
    const v4u g0 = ga[0], g1 = ga[1];
    STAMP(20);
    const u32 qs = L_QST + (u32)(w - 4) * 1024u;
    *reinterpret_cast<v2u*>(lds + qs + lane * 8) = v2u{g0[0], g0[2]};
    *reinterpret_cast<v2u*>(lds + qs + 512 + lane * 8) = v2u{g1[0], g1[2]};
    const u32 ao = qs + (u32)(lane & 1) * 512u + gq * 16u;
    v4f acc0 = {0.f, 0.f, 0.f, 0.f}, acc1 = {0.f, 0.f, 0.f, 0.f};
#pragma unroll
    for (int s = 0; s < 4; ++s) {
      acc0 = __builtin_amdgcn_mfma_f32_16x16x32_bf16(as_bf8(lds16(lds, ao + s * 64)), as_bf8(wk[s]), acc0, 0, 0, 0);
      acc1 = __builtin_amdgcn_mfma_f32_16x16x32_bf16(as_bf8(lds16(lds, ao + (s + 4) * 64)), as_bf8(wk[s + 4]), acc1, 0, 0, 0);
    }
    const float a0 = acc0[0] + acc1[0], a1 = acc0[1] + acc1[1];
    const float o0 = dppf<0xB1>(a0), o1 = dppf<0xB1>(a1);
    if (lane < 16) {
      const int bb = lane & 1, wq4 = w - 4;
      const u32 qa = (u32)bb * 65536u + (u32)((cs * 2 + (wq4 >> 1)) * 2 + ((lane >> 2) & 1)) * 1024u + (u32)h1i * 64u
                     + (u32)((wq4 & 1) * 2 + (lane >> 3)) * 16u + (u32)((lane >> 1) & 1) * 8u;
      st_gran(ws, WS_QL + qa, tag, bb ? pk2(o1, a1) : pk2(a0, o0));
    }
    STAMP(2);
    fl_add(lb + F_QP, lane);
  }
  if (w == 1) {
    float a0, a1;
    fl_wait(lb + F_XN, 1);
    fl_wait(lb + F_PROG, P_W1);
    gemv448(lds, L_W1, lane, a0, a1);
    fl_add(lb + F_RO, lane);
    const float hv = rbf(rbf(b2i ? a1 : a0) + bias);
    const float ge = 0.5f * hv * (1.f + tanhf(0.7978845608028654f * (hv + 0.044715f * hv * hv * hv)));
    if (lane < 32) *reinterpret_cast<u16*>(lds + L_VH + lane * 2) = (lane < 16 && hd < 112) ? f2bf(ge) : (u16)0;
  }
  v4u bv[8];
  {
    const int slot = w == 0 ? 3 : w < 4 ? w - 1 : w;
    fl_wait(lb + F_PROG, P_W1 + 8 * (slot + 1));
    const u32 key = (u32)lane & 15u;
    const u32 ro = L_RA + (u32)slot * 8192u + key * 256u;
#pragma unroll
    for (int s = 0; s < 8; ++s) bv[s] = lds16(lds, ro + (u32)(s >> 2) * 4096u + ((((u32)(s & 3) * 4u + gq) ^ swz(key)) * 16u));
    fl_add(lb + F_PULL + (u32)slot * 4u, lane);
  }
  STAMP(3);
  {
    const u32 qo = WS_QL + (u32)b * 65536u + (u32)w * 8192u + (u32)lane * 16u;
    v4u gl[8];
    fl_wait(lb + F_QP, 4);
    POLLN(gl, 8, qo, 1024u, 2u)
    STAMP(4);
    v4f acc0 = {0.f, 0.f, 0.f, 0.f}, acc1 = {0.f, 0.f, 0.f, 0.f};
#pragma unroll
    for (int s = 0; s < 4; ++s) {
      const v4u av = frag(v4u{gl[2 * s][0], gl[2 * s][2], gl[2 * s + 1][0], gl[2 * s + 1][2]}, pa);
      acc0 = __builtin_amdgcn_mfma_f32_16x16x32_bf16(as_bf8(av), as_bf8(bv[s]), acc0, 0, 0, 0);
      acc1 = __builtin_amdgcn_mfma_f32_16x16x32_bf16(as_bf8(av), as_bf8(bv[4 + s]), acc1, 0, 0, 0);
    }
    float* sr = reinterpret_cast<float*>(lds + L_SRED) + (w * 16 + gq * 4) * 33 + (lane & 15);
#pragma unroll
    for (int q = 0; q < 4; ++q) { sr[q * 33] = acc0[q]; sr[q * 33 + 16] = acc1[q]; }
  }
  sbar(lb + F_B, lane);
  STAMP(5);
  {
    const int hq = t >> 5, key = t & 31;
    const float* sr = reinterpret_cast<const float*>(lds + L_SRED) + hq * 33 + key;
    float s = 0.f;
#pragma unroll
    for (int ww = 0; ww < 8; ++ww) s += sr[ww * 16 * 33];
    s *= 0.0625f;
    st_gran(ws, WS_SC + (u32)((G * 16 + hq) * 256 + j * 32 + key) * 8u, tag, __float_as_uint(s));
    fl_add(lb + F_SR, lane);
  }
  {
    float ml[4];
    const u32 so = WS_SC + (u32)((G * 16 + 2 * w) * 256 + 2 * lane) * 8u;
    v4u gs[4];
    POLLN(gs, 4, so, 1024u, 3u)
    STAMP(6);
    fl_wait(lb + F_SR, 8);
#pragma unroll
    for (int hh = 0; hh < 2; ++hh) {
      const float sv[4] = {__uint_as_float(gs[2 * hh][0]), __uint_as_float(gs[2 * hh][2]), __uint_as_float(gs[2 * hh + 1][0]), __uint_as_float(gs[2 * hh + 1][2])};
      const float m = wave_max(fmaxf(fmaxf(sv[0], sv[1]), fmaxf(sv[2], sv[3])));
      const u32 p0 = pk2(__expf(sv[0] - m), __expf(sv[1] - m)), p1 = pk2(__expf(sv[2] - m), __expf(sv[3] - m));
      const float l = wave_sum((lo_f(p0) + hi_f(p0)) + (lo_f(p1) + hi_f(p1)));
      *reinterpret_cast<u32*>(lds + L_P + (2 * w + hh) * 528 + lane * 4) = p0;
      *reinterpret_cast<u32*>(lds + L_P + (2 * w + hh) * 528 + 256 + lane * 4) = p1;
      ml[2 * hh] = m; ml[2 * hh + 1] = l;
    }
    if (j == 0 && lane < 4) {
      const float v = lane == 0 ? ml[0] : lane == 1 ? ml[1] : lane == 2 ? ml[2] : ml[3];
      st_gran(ws, WS_ML + (u32)(((b * 16 + 2 * w + (lane >> 1)) * 16 + g) * 2 + (lane & 1)) * 8u, tag, __float_as_uint(v));
    }
  }
  sbar(lb + F_B + 4, lane);
  STAMP(22);
  fl_wait(lb + F_PROG, P_COL);
  STAMP(7);
  {
    v4f acc0 = {0.f, 0.f, 0.f, 0.f}, acc1 = {0.f, 0.f, 0.f, 0.f};
    const u32 po = L_P + (u32)(lane & 15) * 528u + gq * 16u;
    const u32 k0 = L_RA + off_b(8 * gq + qq, 2 * w + (pp >> 1)) + 8 * (pp & 1);
    const u32 k1 = L_RA + off_b(8 * gq + 4 + qq, 2 * w + (pp >> 1)) + 8 * (pp & 1);
#pragma unroll
    for (int ks = 0; ks < 4; ++ks) {
      acc0 = __builtin_amdgcn_mfma_f32_16x16x32_bf16(as_bf8(lds16(lds, po + ks * 64)), tr_pair(lds, k0 + ks * 8192, k1 + ks * 8192), acc0, 0, 0, 0);
      acc1 = __builtin_amdgcn_mfma_f32_16x16x32_bf16(as_bf8(lds16(lds, po + (ks + 4) * 64)), tr_pair(lds, k0 + (ks + 4) * 8192, k1 + (ks + 4) * 8192), acc1, 0, 0, 0);
    }
    const v4f acc = acc0 + acc1;
    const int hi8 = (lane >> 3) & 1;
    const float s0 = hi8 ? acc[0] : acc[2], s1 = hi8 ? acc[1] : acc[3];
    const float n0 = dppf<0x128>(s0), n1 = dppf<0x128>(s1);
    const u32 g0 = hi8 ? pk2(n0, acc[2]) : pk2(acc[0], n0);
    const u32 g1 = hi8 ? pk2(n1, acc[3]) : pk2(acc[1], n1);
    const int hq = (lane >> 4) * 4 + 2 * hi8;
    const u32 go = WS_PV + (u32)(((b * 16 + hq) * 16 + g) * 512 + j * 64 + w * 8 + (lane & 7)) * 8u;
    st_gran(ws, go, tag, g0);
    st_gran(ws, go + 16u * 512u * 8u, tag, g1);
  }
  STAMP(8);
  {
    const int bq = w >> 2;
    const u32 gg = (u32)(w & 3) * 4u + gq;
    const u32 po = WS_PV + (((u32)(bq * 16 + h1i) * 16u + gg) * 512u + (u32)cs * 32u + (u32)(lane & 15) * 2u) * 8u;
    gu64* mlp = (gu64*)(ws + WS_ML) + (bq * 16 + h1i) * 32 + (lane & 31);
    v4u pv;
    u64 mlv;
#pragma unroll 1
    for (u32 spins = 0;;) {
      mlv = __hip_atomic_load(mlp, RLX_AGENT);
      if (__all((u32)(mlv >> 32) == tag)) break;
      SPIN_FAIL(4u)
    }
    const float mv = __uint_as_float((u32)mlv);
    const float other = dppf<0xB1>(mv);
    const float mg = (lane & 1) ? other : mv, lg = (lane & 1) ? mv : other;
    const float Mx = wave_max(mg);
    const float wg = __expf(mg - Mx);
    const float Ls = wave_sum(wg * lg) * 0.25f;
    const float wn = wg / Ls;
    const float wv = __int_as_float(__builtin_amdgcn_ds_bpermute((int)gg * 8, __float_as_int(wn)));
    POLL1(pv, po, 4u)
    STAMP(9);
    float a[4] = {wv * lo_f(pv[0]), wv * lo_f(pv[2]), wv * hi_f(pv[0]), wv * hi_f(pv[2])};
#pragma unroll
    for (int q = 0; q < 4; ++q) a[q] = x32_add(x16_add(a[q]));
    if (lane < 16) {
      float* dp = reinterpret_cast<float*>(lds + L_DP) + w * 64 + (lane >> 2) * 16 + (lane & 3) * 2;
      dp[0] = a[0]; dp[1] = a[1]; dp[8] = a[2]; dp[9] = a[3];
    }
  }
  sbar(lb + F_B + 8, lane);
  STAMP(23);
  fl_wait(lb + F_PROG, P_UV);
  STAMP(10);
  {
    v4f acc0 = {0.f, 0.f, 0.f, 0.f}, acc1 = {0.f, 0.f, 0.f, 0.f};
    const u32 dpo = L_DP + (u32)((lane >> 2) & 1) * 1024u + gq * 32u;
    const u32 rr = 8u * gq + qq;
    const u32 f = (qq << 1) | ((gq & 1u) << 3);
    const u32 bo0 = L_UV + rr * 512u + ((((u32)w * 4u + (pp >> 1)) ^ f) * 16u) + (pp & 1u) * 8u;
    const u32 bo1 = L_UV + rr * 512u + ((((u32)w * 4u + 2u + (pp >> 1)) ^ f) * 16u) + (pp & 1u) * 8u;
#pragma unroll
    for (int s = 0; s < 2; ++s) {
      v4f c0 = {0.f, 0.f, 0.f, 0.f}, c1 = {0.f, 0.f, 0.f, 0.f};
#pragma unroll
      for (int ww = 0; ww < 4; ++ww) { c0 += lds16f(lds, dpo + ww * 256 + s * 128); c1 += lds16f(lds, dpo + ww * 256 + s * 128 + 16); }
      v4u av = {pk2(c0[0], c0[1]), pk2(c0[2], c0[3]), pk2(c1[0], c1[1]), pk2(c1[2], c1[3])};
      if (lane & 1) {
        av = v4u{pk2(c0[0] - lo_f(av[0]), c0[1] - hi_f(av[0])), pk2(c0[2] - lo_f(av[1]), c0[3] - hi_f(av[1])),
                 pk2(c1[0] - lo_f(av[2]), c1[1] - hi_f(av[2])), pk2(c1[2] - lo_f(av[3]), c1[3] - hi_f(av[3]))};
      }
      acc0 = __builtin_amdgcn_mfma_f32_16x16x32_bf16(as_bf8(av), tr_pair(lds, bo0 + s * 16384, bo0 + s * 16384 + 2048), acc0, 0, 0, 0);
      acc1 = __builtin_amdgcn_mfma_f32_16x16x32_bf16(as_bf8(av), tr_pair(lds, bo1 + s * 16384, bo1 + s * 16384 + 2048), acc1, 0, 0, 0);
    }
    if (lane < 32) {
      const u32 co = WS_CP + ((((u32)(h1i * 2) + gq) * 8u + (u32)w) * 16u + (u32)cs) * 256u + (u32)(lane & 15) * 8u;
      st_gran(ws, co, tag, __float_as_uint(acc0[0] + acc0[1]));
      st_gran(ws, co + 128u, tag, __float_as_uint(acc1[0] + acc1[1]));
    }
  }
  STAMP(11);
  if (w < 4) {
    const u32 co = WS_CP + (u32)((h1i * 2 + b2i) * 8 + ds) * 4096u + (u32)t * 16u;
    v4u cv;
    POLL1(cv, co, 6u)
    const float c0 = x32_add(x16_add(__uint_as_float(cv[0]))), c1 = x32_add(x16_add(__uint_as_float(cv[2])));
    if (lane < 16) *reinterpret_cast<v2u*>(lds + L_CR + w * 128 + lane * 8) = v2u{__float_as_uint(c0), __float_as_uint(c1)};
  }
  STAMP(15);
  sbar(lb + F_B + 12, lane);
  fl_wait(lb + F_PROG, P_WO);
  STAMP(16);
  if (w < 7) {
    const float* cr = reinterpret_cast<const float*>(lds + L_CR) + (lane & 31);
    const float cv = (cr[0] + cr[32]) + (cr[64] + cr[96]);
    *reinterpret_cast<u16*>(lds + L_VST + w * 64 + (lane & 31) * 2) = f2bf(cv);
    const bf8 a0 = as_bf8(lds16(lds, L_VST + w * 64 + gq * 16)), a1 = as_bf8(lds16(lds, L_VH + gq * 16));
    const u32 bo0 = L_WO + (8u * gq + qq) * 896u + (u32)w * 128u + pp * 8u;
    const u32 bo1 = L_W2 + ((8u * gq + qq) & 15u) * 896u + (u32)w * 128u + pp * 8u;
#pragma unroll
    for (int n = 0; n < 4; ++n) {
      v4f acc = {0.f, 0.f, 0.f, 0.f};
      acc = __builtin_amdgcn_mfma_f32_16x16x32_bf16(a0, tr_pair(lds, bo0 + n * 32, bo0 + n * 32 + 3584), acc, 0, 0, 0);
      acc = __builtin_amdgcn_mfma_f32_16x16x32_bf16(a1, tr_pair(lds, bo1 + n * 32, bo1 + n * 32 + 3584), acc, 0, 0, 0);
      if (lane < 16) st_gran(ws, WS_OG + (u32)((b2i * 128 + hd) * M_ + w * 64 + n * 16 + lane) * 8u, tag, __float_as_uint(acc[0]));
    }
  }
  STAMP(12);
  if (i < 112) {
    const int fb = i / 56, fc = i - fb * 56;
    const u32 oo = WS_OG + ((u32)(fb * 128 + (t >> 2)) * M_ + (u32)(fc * 8 + (t & 3) * 2)) * 8u;
    float xr = 0.f;
    if (t < 8) xr = bf2f(ldh(x + fb * M_ + fc * 8 + t)) + 0.f;
    float br = 0.f;
    if (t < 8) br = bf2f(ldh(b2 + fc * 8 + t));
    v4u ov;
    POLL1(ov, oo, 5u)
    float c0 = __uint_as_float(ov[0]), c1 = __uint_as_float(ov[2]);
    c0 = ror_add<4>(c0); c1 = ror_add<4>(c1);
    c0 = ror_add<8>(c0); c1 = ror_add<8>(c1);
    c0 = x32_add(x16_add(c0)); c1 = x32_add(x16_add(c1));
    if (lane < 4) *reinterpret_cast<v2u*>(lds + L_FR + w * 32 + lane * 8) = v2u{__float_as_uint(c0), __float_as_uint(c1)};
    sbar(lb + F_B + 16, lane);
    STAMP(13);
    if (t < 8) {
      float a = 0.f;
#pragma unroll
      for (int q = 0; q < 8; ++q) a += reinterpret_cast<const float*>(lds + L_FR)[q * 8 + t];
      *(GAS u16*)(out + fb * M_ + fc * 8 + t) = f2bf(xr + a + br);
    }
    if (i == 0 && t == 0) __hip_atomic_store((gu32*)(ws + WS_EP), tag, RLX_AGENT);
    STAMP(14);
  }
  STAMP_FLUSH;
}

extern "C" void kernel_launch(void* const* d_in, const int* in_sizes, int n_in,
                              void* d_out, int out_size, void* d_ws, size_t ws_size,
                              hipStream_t stream) {
  const u16* x = (const u16*)d_in[0];
  const u16* ckv = (const u16*)d_in[1];
  const u16* wq = (const u16*)d_in[2];
  const u16* wuk = (const u16*)d_in[3];
  const u16* wuv = (const u16*)d_in[4];
  const u16* wo = (const u16*)d_in[5];
  const u16* w1 = (const u16*)d_in[7];
  const u16* b1 = (const u16*)d_in[8];
  const u16* w2 = (const u16*)d_in[9];
  const u16* b2 = (const u16*)d_in[10];
  mla_decode<<<256, 576, 0, stream>>>(x, ckv, wq, wuk, wuv, wo, w1, b1, w2, b2, (char*)d_ws, (u16*)d_out);
}
```

```cpp
#include <hip/hip_runtime.h>
#include <hip/hip_bf16.h>
#include <stdint.h>

typedef unsigned short u16;
typedef unsigned int u32;
typedef unsigned long long u64;
typedef __attribute__((ext_vector_type(4))) unsigned v4u;
typedef __attribute__((ext_vector_type(2))) unsigned v2u;
typedef __attribute__((ext_vector_type(4))) float v4f;
typedef __attribute__((ext_vector_type(8))) __bf16 bf8;
typedef __attribute__((ext_vector_type(4))) short s4;
typedef __attribute__((ext_vector_type(8))) short s8;

#define GAS __attribute__((address_space(1)))
#define LAS __attribute__((address_space(3)))
typedef GAS u32 gu32;
typedef GAS u64 gu64;
#define RLX_AGENT __ATOMIC_RELAXED, __HIP_MEMORY_SCOPE_AGENT

#define M_ 448
#define F_ 1792
#define DC 1024
#define SK 4096

constexpr u32 SPIN_CAP = 1u << 18;

constexpr u32 WS_EP = 0;
constexpr u32 WS_QL = 65536;
constexpr u32 WS_SC = 262144;
constexpr u32 WS_PV = 1310720;
constexpr u32 WS_ML = 3407872;
constexpr u32 WS_CP = 3416064;
constexpr u32 WS_OG = 4464640;
constexpr u32 WS_QG = WS_OG + 2 * 128 * 448 * 8;
constexpr u32 WS_END = WS_QG + 262144;

#define STAMP(k) do {} while (0)
#define STAMP_DECL
#define STAMP_FLUSH do {} while (0)

#define BAR() do { asm volatile("s_waitcnt lgkmcnt(0)" ::: "memory"); __builtin_amdgcn_s_barrier(); asm volatile("" ::: "memory"); } while (0)

__device__ __forceinline__ float bf2f(u16 v) { return __uint_as_float(((u32)v) << 16); }
__device__ __forceinline__ u16 f2bf(float f) { __hip_bfloat16 b = __float2bfloat16(f); return *reinterpret_cast<u16*>(&b); }
__device__ __forceinline__ float rbf(float f) { return bf2f(f2bf(f)); }
__device__ __forceinline__ float lo_f(u32 v) { return __uint_as_float(v << 16); }
__device__ __forceinline__ float hi_f(u32 v) { return __uint_as_float(v & 0xffff0000u); }
__device__ __forceinline__ u32 pk2(float a, float b) { return (u32)f2bf(a) | ((u32)f2bf(b) << 16); }
__device__ __forceinline__ v4u ld16(const u16* p) { return __builtin_nontemporal_load((const GAS v4u*)p); }
__device__ __forceinline__ v4u ldp16(const u16* p) { return *(const GAS v4u*)p; }
__device__ __forceinline__ u16 ldh(const u16* p) { return *(const GAS u16*)p; }
__device__ __forceinline__ bf8 as_bf8(v4u v) { return __builtin_bit_cast(bf8, v); }

template <int CTRL>
__device__ __forceinline__ float dppf(float v) {
  return __int_as_float(__builtin_amdgcn_update_dpp(0, __float_as_int(v), CTRL, 0xf, 0xf, true));
}
template <int N> __device__ __forceinline__ float ror_add(float v) { return v + dppf<0x120 + N>(v); }
template <int N> __device__ __forceinline__ float ror_max(float v) { return fmaxf(v, dppf<0x120 + N>(v)); }
__device__ __forceinline__ float x16_add(float v) {
  const u32 b = __float_as_uint(v);
  auto p = __builtin_amdgcn_permlane16_swap(b, b, false, false);
  return __uint_as_float(p[0]) + __uint_as_float(p[1]);
}
__device__ __forceinline__ float x32_add(float v) {
  const u32 b = __float_as_uint(v);
  auto p = __builtin_amdgcn_permlane32_swap(b, b, false, false);
  return __uint_as_float(p[0]) + __uint_as_float(p[1]);
}
__device__ __forceinline__ float x16_max(float v) {
  const u32 b = __float_as_uint(v);
  auto p = __builtin_amdgcn_permlane16_swap(b, b, false, false);
  return fmaxf(__uint_as_float(p[0]), __uint_as_float(p[1]));
}
__device__ __forceinline__ float x32_max(float v) {
  const u32 b = __float_as_uint(v);
  auto p = __builtin_amdgcn_permlane32_swap(b, b, false, false);
  return fmaxf(__uint_as_float(p[0]), __uint_as_float(p[1]));
}
__device__ __forceinline__ float wave_sum(float v) {
  v = ror_add<8>(v); v = ror_add<4>(v); v = ror_add<2>(v); v = ror_add<1>(v);
  return x32_add(x16_add(v));
}
__device__ __forceinline__ float wave_max(float v) {
  v = ror_max<8>(v); v = ror_max<4>(v); v = ror_max<2>(v); v = ror_max<1>(v);
  return x32_max(x16_max(v));
}

__device__ __forceinline__ u32 swz(u32 row) { return ((row & 3u) << 2) | ((row >> 2) & 3u); }
__device__ __forceinline__ u32 off_b(u32 row, u32 ch) { return 256u * row + 16u * (ch ^ swz(row)); }

constexpr u32 L_RA = 0;
constexpr u32 L_UV = 65536;
constexpr u32 L_WO = 98304;
constexpr u32 L_WQ = L_WO;
constexpr u32 L_W1 = L_WO + 14336;
constexpr u32 L_W2 = 126976;
constexpr u32 L_XN = 141312;
constexpr u32 L_P = 143104;
constexpr u32 L_QST = L_P;
constexpr u32 L_SRED = L_P;
constexpr u32 L_DP = L_P + 16896;
constexpr u32 L_CR = L_DP + 2048;
constexpr u32 L_VST = L_CR + 512;
constexpr u32 L_VH = L_VST + 512;
constexpr u32 L_FR = L_VH + 64;
constexpr u32 L_BI = L_FR + 256;
constexpr u32 L_FLAG = L_BI + 64;
constexpr u32 L_END = L_FLAG + 128;
static_assert(L_END <= 163840, "LDS");
constexpr u32 F_PROG = L_FLAG;
constexpr u32 F_PULL = L_FLAG + 4;
constexpr u32 F_UK = L_FLAG + 36;
constexpr u32 F_RO = L_FLAG + 40;
constexpr u32 F_SR = L_FLAG + 44;
constexpr u32 F_B = L_FLAG + 48;
constexpr u32 F_QP = L_FLAG + 68;
constexpr u32 F_XN = L_FLAG + 72;
constexpr u32 F_QS = L_FLAG + 76;
#define W1POS 1
constexpr int P_WQ = 14, P_UK = 46, P_ROWS = 110, P_COL = 174, P_W1 = 188, P_UV = 220, P_W2 = 234, P_WO = 262;
constexpr int P_END = 262;

#define WIN 16
#define WIN2 16
#define WINC 16
constexpr int WMAX = WIN > WIN2 ? (WIN > WINC ? WIN : WINC) : (WIN2 > WINC ? WIN2 : WINC);
#define AUX_COL 2
#define PSLP 2
#define QDLY 12
#define QGATE 4
#define PSTR 128
#define PRE_AUX 0
#define PRE_SC 0
#define POUT 0
#define PRE_RD(off) __builtin_amdgcn_raw_buffer_load_b32(rw, (off), 0, PRE_AUX)
#define PRE 110
#define CPRIO 2
#define SPRIO 3
constexpr u32 LSPIN = 1u << 22;

#define SPIN_FAIL(code) if (++spins > SPIN_CAP) { if (lane == 0) __hip_atomic_store(tmo, (code), RLX_AGENT); break; } __builtin_amdgcn_s_sleep(PSLP); asm volatile("" ::: "memory");

#define POLL1(v, off, code) __builtin_amdgcn_s_setprio(0); _Pragma("unroll 1") for (u32 spins = 0;;) { v = __builtin_amdgcn_raw_buffer_load_b128(rw, (off), 0, 16); if (__all((v[1] == tag) & (v[3] == tag))) break; SPIN_FAIL(code) } __builtin_amdgcn_s_setprio(CPRIO);

#define POLLN(v, N, off, stride, code) { u32 pend = (1u << (N)) - 1u; __builtin_amdgcn_s_setprio(0); _Pragma("unroll 1") for (u32 spins = 0;;) { \
    _Pragma("unroll") for (int s_ = 0; s_ < (N); ++s_) if (pend >> s_ & 1u) v[s_] = __builtin_amdgcn_raw_buffer_load_b128(rw, (off) + s_ * (stride), 0, 16); \
    _Pragma("unroll") for (int s_ = 0; s_ < (N); ++s_) if (pend >> s_ & 1u) { if (__all((v[s_][1] == tag) & (v[s_][3] == tag))) pend &= ~(1u << s_); } \
    if (!pend) break; SPIN_FAIL(code) } __builtin_amdgcn_s_setprio(CPRIO); }

__device__ __forceinline__ void st_gran(char* ws, u32 off, u32 tag, u32 val) {
  __hip_atomic_store((gu64*)(ws + off), ((u64)tag << 32) | val, RLX_AGENT);
}
__device__ __forceinline__ bf8 tr_pair(char* lds, u32 a0, u32 a1) {
  const s4 b0 = __builtin_amdgcn_ds_read_tr16_b64_v4i16((LAS s4*)(lds + a0));
  const s4 b1 = __builtin_amdgcn_ds_read_tr16_b64_v4i16((LAS s4*)(lds + a1));
  const s8 bb = {b0[0], b0[1], b0[2], b0[3], b1[0], b1[1], b1[2], b1[3]};
  return __builtin_bit_cast(bf8, bb);
}
__device__ __forceinline__ v4u lds16(char* lds, u32 a) { return *reinterpret_cast<const v4u*>(lds + a); }
__device__ __forceinline__ v4f lds16f(char* lds, u32 a) { return *reinterpret_cast<const v4f*>(lds + a); }
__device__ __forceinline__ float fdiv(float a, float d, float r) {
  const float q = a * r;
  return fmaf(fmaf(-q, d, a), r, q);
}
__device__ __forceinline__ v4u frag(v4u v, int pa) {
  v4u o;
#pragma unroll
  for (int e = 0; e < 4; ++e) o[e] = (u32)__builtin_amdgcn_ds_bpermute(pa, (int)v[e]);
  return o;
}
__device__ __forceinline__ void gemv448(char* lds, u32 wt, int lane, float& a0, float& a1) {
  const u32 gq = lane >> 4, qq = (lane & 15) >> 2, pp = lane & 3;
  const u32 ao = L_XN + (u32)(lane & 1) * 896u + gq * 16u;
  const u32 bo = wt + (8u * gq + qq) * 32u + pp * 8u;
  v4f acc0 = {0.f, 0.f, 0.f, 0.f}, acc1 = {0.f, 0.f, 0.f, 0.f};
#pragma unroll
  for (int s = 0; s < 7; ++s) {
    acc0 = __builtin_amdgcn_mfma_f32_16x16x32_bf16(as_bf8(lds16(lds, ao + s * 64)), tr_pair(lds, bo + s * 1024, bo + s * 1024 + 128), acc0, 0, 0, 0);
    acc1 = __builtin_amdgcn_mfma_f32_16x16x32_bf16(as_bf8(lds16(lds, ao + (s + 7) * 64)), tr_pair(lds, bo + (s + 7) * 1024, bo + (s + 7) * 1024 + 128), acc1, 0, 0, 0);
  }
  a0 = acc0[0] + acc1[0]; a1 = acc0[1] + acc1[1];
}

__device__ __forceinline__ void fl_write(u32 a, u32 v) { asm volatile("ds_write_b32 %0, %1" :: "v"(a), "v"(v) : "memory"); }
__device__ __forceinline__ void fl_add(u32 a, int lane) { if (lane == 0) asm volatile("ds_add_u32 %0, %1" :: "v"(a), "v"(1u) : "memory"); }
__device__ __forceinline__ int fl_read(u32 a) {
  u32 v; asm volatile("ds_read_b32 %0, %1\n s_waitcnt lgkmcnt(0)" : "=v"(v) : "v"(a) : "memory");
  return (int)__builtin_amdgcn_readfirstlane(v);
}
__device__ __forceinline__ void fl_wait(u32 a, int v) {
#pragma unroll 1
  for (u32 n = 0; n < LSPIN; ++n) { if (fl_read(a) >= v) break; __builtin_amdgcn_s_sleep(0); }
}
__device__ __forceinline__ void sbar(u32 a, int lane) { fl_add(a, lane); fl_wait(a, 8); }
template <int K> __device__ __forceinline__ void drain(u32 fp, int q, int lo) {
  asm volatile("s_waitcnt vmcnt(%0)" :: "n"(K) : "memory");
  fl_write(fp, (u32)(q - K > lo ? q - K : lo));
  if constexpr (K > 0) drain<K - 1>(fp, q, lo);
}
__device__ __forceinline__ void gate(u32 fp, int q, u32 a, int v, int& lo) {
  if (fl_read(a) >= v) return;
  if (lo < q) drain<WMAX - 2>(fp, q, lo);
  lo = q;
  fl_wait(a, v);
}
template <int N, int U, int XM, int SEG, int AUX, int W>
__device__ __forceinline__ void stream(char* lds, u32 lb, const void* base, u32 bytes, u32 vb, u32 sb, u32 ss, u32 ld, int q0, int& lo) {
  const __amdgpu_buffer_rsrc_t rs = __builtin_amdgcn_make_buffer_rsrc((void*)base, 0, bytes, 0x00020000);
#pragma unroll 1
  for (u32 k = 0; k < (u32)(N / U); ++k) {
    const int qk = q0 + (int)(k * U);
    if (SEG == 2) gate(lb + F_PROG, qk, lb + F_PULL + k * 4u, 1, lo);
    const u32 sk = SEG == 1 ? sb + (k < 3u ? k + 1u : k == 3u ? 0u : k) * 256u : sb + k * (U * ss);
#pragma unroll
    for (u32 n = 0; n < (u32)U; ++n) {
      const int q = qk + (int)n;
      asm volatile("s_waitcnt vmcnt(%0)" :: "n"(W - 1) : "memory");
      lo = q - (W - 1) > lo ? q - (W - 1) : lo;
      fl_write(lb + F_PROG, (u32)lo);
      const u32 xm = XM == 1 ? (n & 3u) << 4 : XM == 2 ? ((2u * n) & 15u) << 4 : XM == 3 ? (((n & 1u) << 2) | (((n >> 2) & 1u) << 3)) << 4 : 0u;
      __builtin_amdgcn_raw_ptr_buffer_load_lds(rs, (LAS void*)(lds + ld + k * (U * 1024u) + n * 1024u), 16, vb ^ xm, sk + n * ss, 0, AUX);
    }
  }
}

__global__ __launch_bounds__(576) void mla_decode(const u16* x, const u16* ckv, const u16* wq, const u16* wuk,
                                                  const u16* wuv, const u16* wo, const u16* w1, const u16* b1,
                                                  const u16* w2, const u16* b2, char* ws, u16* out) {
  __shared__ __attribute__((aligned(16))) char lds[L_END];
  const int t = threadIdx.x, lane = t & 63;
  const int w = __builtin_amdgcn_readfirstlane(t >> 6);
  const int i = blockIdx.x, xcd = i & 7, r = i >> 3;
  const int h1i = xcd * 2 + (r >> 4), cs = r & 15;
  const int G = xcd * 4 + (r >> 3), b = G >> 4, g = G & 15, j = r & 7;
  const int b2i = r & 1, hd = xcd * 16 + (r >> 1), ds = hd & 7;
  const int fsl = hd < 112 ? hd : 0;
  STAMP_DECL
  STAMP(0);
  asm volatile("" : "+s"(x), "+s"(ckv), "+s"(wq), "+s"(wuk), "+s"(wuv), "+s"(wo), "+s"(w1), "+s"(b1), "+s"(w2), "+s"(b2), "+s"(ws), "+s"(out));
  gu32* tmo = (gu32*)(ws + WS_EP + 128);
  const __amdgpu_buffer_rsrc_t rw = __builtin_amdgcn_make_buffer_rsrc(ws, 0, WS_END, 0x00020000);
  const u32 gq = lane >> 4, qq = (lane & 15) >> 2, pp = lane & 3;

  const u32 lb = (u32)(uintptr_t)(LAS char*)lds;

  if (w == 8) {
    __builtin_amdgcn_s_setprio(SPRIO);
    if (lane < 32) fl_write(lb + L_FLAG + lane * 4, 0u);
    BAR();
    int lo = 0;
    const u32 hb = (u32)lane >> 5;
    const u32 vk = gq * 2048u + (((u32)lane & 15u) ^ (gq << 2)) * 16u;
    stream<14, 7, 0, 0, 0, WIN>(lds, lb, wq, M_ * 8192u, (u32)(lane >> 3) * 8192u + (((u32)lane & 7u) ^ ((((u32)lane >> 3) & 3u) << 1)) * 16u, (u32)(cs & 3) * (112u * 8192u) + (u32)h1i * 512u + (u32)(cs >> 2) * 128u, 8u * 8192u, L_WQ, 0, lo);
    stream<32, 8, 2, 0, 2, WIN>(lds, lb, wuk, 1024u * 8192u, hb * 8192u + (((u32)lane & 31u) ^ hb) * 16u, (u32)(cs * 64) * 8192u + (u32)h1i * 512u, 16384u, L_UV, P_UK - 32, lo);
    STAMP(24);
    stream<64, 8, 1, 1, 0, WIN>(lds, lb, ckv, 2u * SK * DC * 2u, vk, (u32)(b * SK + g * 256 + j * 32) * 2048u, 8192u, L_RA, P_UK, lo);
    STAMP(25);
    stream<64, 8, 1, 2, AUX_COL, WINC>(lds, lb, ckv, 2u * SK * DC * 2u, vk, (u32)(b * SK + g * 256) * 2048u + (u32)j * 256u, 8192u, L_RA, P_ROWS, lo);
    STAMP(26);
    stream<14, 7, 0, 0, 0, WIN>(lds, lb, w1, M_ * F_ * 2u, (u32)(lane >> 1) * (F_ * 2u) + (u32)(lane & 1) * 16u, (u32)fsl * 32u, 32u * F_ * 2u, L_W1, P_W1 - 14, lo);
    gate(lb + F_PROG, P_UV - 32, lb + F_UK, 4, lo);
    stream<32, 8, 3, 0, 2, WIN2>(lds, lb, wuv, 1024u * 8192u, hb * 8192u + (((u32)lane & 31u) ^ (hb << 1)) * 16u, (u32)(cs * 64) * 8192u + (u32)h1i * 512u, 16384u, L_UV, P_UV - 32, lo);
    stream<14, 7, 0, 0, 2, WIN2>(lds, lb, w2, 1792u * 896u, (u32)lane * 16u, (u32)(fsl * 16) * 896u, 1024u, L_W2, P_W2 - 14, lo);
    STAMP(27);
    {
      gate(lb + F_PROG, P_W2, lb + F_RO, 4, lo);
      float a0, a1;
      gemv448(lds, L_W1, lane, a0, a1);
      const float hv = rbf(rbf(b2i ? a1 : a0) + *reinterpret_cast<const float*>(lds + L_BI + (lane & 15) * 4));
      const float ge = 0.5f * hv * (1.f + tanhf(0.7978845608028654f * (hv + 0.044715f * hv * hv * hv)));
      if (lane < 32) *reinterpret_cast<u16*>(lds + L_VH + lane * 2) = (lane < 16 && hd < 112) ? f2bf(ge) : (u16)0;
      asm volatile("s_waitcnt lgkmcnt(0)" ::: "memory");
    }
    STAMP(28);
    stream<28, 7, 0, 0, 2, WIN2>(lds, lb, wo, 4096u * 896u, (u32)lane * 16u, (u32)(hd * 32) * 896u, 1024u, L_WO, P_W2, lo);
    drain<WMAX - 1>(lb + F_PROG, P_END, lo);
    STAMP(29);
    STAMP_FLUSH;
    return;
  }

  __builtin_amdgcn_s_setprio(CPRIO);
  const u32 epv = __builtin_amdgcn_raw_buffer_load_b32(rw, WS_EP, 0, 0);
  const int pa = (4 * (lane & 15) + (lane >> 4)) * 4;
  u32 tag, pre0 = 0;
  if (w == 0) {
    const int lc = lane < 56 ? lane : 0;
    const v4u xr0 = ldp16(x + lc * 8), xr1 = ldp16(x + M_ + lc * 8);
    STAMP(21);
    BAR();
    float xa[8], xb[8];
#pragma unroll
    for (int p = 0; p < 4; ++p) { xa[2 * p] = lo_f(xr0[p]); xa[2 * p + 1] = hi_f(xr0[p]); xb[2 * p] = lo_f(xr1[p]); xb[2 * p + 1] = hi_f(xr1[p]); }
    const float act = lane < 56 ? 1.f : 0.f;
    float s0 = 0.f, s1 = 0.f;
#pragma unroll
    for (int k = 0; k < 8; ++k) { s0 += xa[k]; s1 += xb[k]; }
    s0 = wave_sum(s0 * act); s1 = wave_sum(s1 * act);
    const float m0 = s0 * (1.f / M_), m1 = s1 * (1.f / M_);
    float v0 = 0.f, v1 = 0.f;
#pragma unroll
    for (int k = 0; k < 8; ++k) { const float d0 = xa[k] - m0, d1 = xb[k] - m1; v0 += d0 * d0; v1 += d1 * d1; }
    v0 = wave_sum(v0 * act); v1 = wave_sum(v1 * act);
    const float mb0 = rbf(m0), mb1 = rbf(m1);
    const float den0 = rbf(sqrtf(rbf(rbf(v0 * (1.f / M_)) + 1e-5f)));
    const float den1 = rbf(sqrtf(rbf(rbf(v1 * (1.f / M_)) + 1e-5f)));
    const float r0 = __builtin_amdgcn_rcpf(den0), r1 = __builtin_amdgcn_rcpf(den1);
    v4u na, nb;
#pragma unroll
    for (int p = 0; p < 4; ++p) {
      na[p] = pk2(fdiv(rbf(xa[2 * p] - mb0), den0, r0), fdiv(rbf(xa[2 * p + 1] - mb0), den0, r0));
      nb[p] = pk2(fdiv(rbf(xb[2 * p] - mb1), den1, r1), fdiv(rbf(xb[2 * p + 1] - mb1), den1, r1));
    }
    if (lane < 56) {
      *reinterpret_cast<v4u*>(lds + L_XN + lane * 16) = na;
      *reinterpret_cast<v4u*>(lds + L_XN + 896 + lane * 16) = nb;
    }
    fl_add(lb + F_XN, lane);
    STAMP(17);
  } else if (w < 4) {
    float bias = 0.f;
    if (w == 1) bias = bf2f(ldh(b1 + fsl * 16 + (lane & 15)));
    if (w == 3) pre0 = __builtin_amdgcn_raw_buffer_load_b32(rw, lane < 8 ? WS_QL + (u32)i * 512u + (u32)lane * 64u : WS_QG + (u32)i * 1024u + (u32)(lane & 15) * 64u, 0, PRE_AUX);
    BAR();
    if (w == 1 && lane < 16) *reinterpret_cast<float*>(lds + L_BI + lane * 4) = bias;
    fl_wait(lb + F_XN, 1);
  }
  if (w < 4) {
    fl_wait(lb + F_PROG, P_WQ);
    STAMP(18);
    const u32 ao = L_XN + (u32)(lane & 1) * 896u + (u32)(cs & 3) * 224u;
    const u32 ch = ((((u32)w * 2u + (pp >> 1)) ^ (qq << 1)) * 16u) + (pp & 1u) * 8u;
    const u32 bo = L_WQ + (8u * gq + qq) * 128u + ch;
    v4f acc0 = {0.f, 0.f, 0.f, 0.f}, acc1 = {0.f, 0.f, 0.f, 0.f};
    acc0 = __builtin_amdgcn_mfma_f32_16x16x32_bf16(as_bf8(lds16(lds, ao + gq * 16u)), tr_pair(lds, bo, bo + 512), acc0, 0, 0, 0);
    acc1 = __builtin_amdgcn_mfma_f32_16x16x32_bf16(as_bf8(lds16(lds, ao + gq * 16u + 64)), tr_pair(lds, bo + 4096, bo + 4096 + 512), acc1, 0, 0, 0);
    acc0 = __builtin_amdgcn_mfma_f32_16x16x32_bf16(as_bf8(lds16(lds, ao + gq * 16u + 128)), tr_pair(lds, bo + 8192, bo + 8192 + 512), acc0, 0, 0, 0);
    acc1 = __builtin_amdgcn_mfma_f32_16x16x16bf16_1k(*reinterpret_cast<const s4*>(lds + ao + 192u + gq * 8u),
                                                     __builtin_amdgcn_ds_read_tr16_b64_v4i16((LAS s4*)(lds + L_WQ + (96u + 4u * gq + qq) * 128u + ch)), acc1, 0, 0, 0);
    tag = __builtin_amdgcn_readfirstlane(epv) + 1u;
    if (lane < 32) st_gran(ws, WS_QG + (u32)h1i * 16384u + (u32)(cs & 3) * 4096u + (u32)(cs >> 2) * 1024u + (u32)w * 256u + (u32)lane * 8u, tag,
                           __float_as_uint(gq ? acc0[1] + acc1[1] : acc0[0] + acc1[0]));
    STAMP(1);
    fl_add(lb + F_RO, lane);
    asm volatile("" :: "v"(pre0));
    if (PRE_SC == 0 && w == 3) pre0 = PRE_RD(lane < 32 ? WS_SC + (u32)i * 4096u + (u32)lane * 128u : WS_ML + (u32)(i & 63) * 128u);
  } else {
    v4u wk[8];
    BAR();
    tag = __builtin_amdgcn_readfirstlane(epv) + 1u;
    fl_wait(lb + F_RO, QGATE);
    if (QDLY) __builtin_amdgcn_s_sleep(QDLY);
    STAMP(19);
    const u32 qo = WS_QG + (u32)h1i * 16384u + (u32)(w - 4) * 1024u + (u32)lane * 16u;
    v4u ga[4];
    POLLN(ga, 4, qo, 4096u, 1u)
    {
      const float q0 = (__uint_as_float(ga[0][0]) + __uint_as_float(ga[1][0])) + (__uint_as_float(ga[2][0]) + __uint_as_float(ga[3][0]));
      const float q1 = (__uint_as_float(ga[0][2]) + __uint_as_float(ga[1][2])) + (__uint_as_float(ga[2][2]) + __uint_as_float(ga[3][2]));
      *reinterpret_cast<u32*>(lds + L_QST + (u32)((lane >> 3) & 1) * 512u + (u32)(w - 4) * 128u + gq * 32u + (u32)(lane & 7) * 4u) = pk2(q0, q1);
    }
    fl_add(lb + F_QS, lane);
    STAMP(20);
    fl_wait(lb + F_PROG, P_UK);
    {
      const u32 ko = L_UV + (u32)((w - 4) * 16 + (lane & 15)) * 512u;
#pragma unroll
      for (int s = 0; s < 8; ++s) wk[s] = lds16(lds, ko + ((((u32)s * 4u + gq) ^ ((u32)lane & 15u)) * 16u));
    }
    fl_add(lb + F_UK, lane);
    fl_wait(lb + F_QS, 4);
    const u32 ao = L_QST + (u32)(lane & 1) * 512u + gq * 16u;
    v4f acc0 = {0.f, 0.f, 0.f, 0.f}, acc1 = {0.f, 0.f, 0.f, 0.f};
#pragma unroll
    for (int s = 0; s < 4; ++s) {
      acc0 = __builtin_amdgcn_mfma_f32_16x16x32_bf16(as_bf8(lds16(lds, ao + s * 64)), as_bf8(wk[s]), acc0, 0, 0, 0);
      acc1 = __builtin_amdgcn_mfma_f32_16x16x32_bf16(as_bf8(lds16(lds, ao + (s + 4) * 64)), as_bf8(wk[s + 4]), acc1, 0, 0, 0);
    }
    const float a0 = acc0[0] + acc1[0], a1 = acc0[1] + acc1[1];
    const float o0 = dppf<0xB1>(a0), o1 = dppf<0xB1>(a1);
    if (lane < 16) {
      const int bb = lane & 1, wq4 = w - 4;
      const u32 qa = (u32)bb * 65536u + (u32)((cs * 2 + (wq4 >> 1)) * 2 + ((lane >> 2) & 1)) * 1024u + (u32)h1i * 64u
                     + (u32)((wq4 & 1) * 2 + (lane >> 3)) * 16u + (u32)((lane >> 1) & 1) * 8u;
      st_gran(ws, WS_QL + qa, tag, bb ? pk2(o1, a1) : pk2(a0, o0));
    }
    STAMP(2);
    fl_add(lb + F_QP, lane);
    if (PRE_SC == 1 && w == 7) pre0 = PRE_RD(lane < 32 ? WS_SC + (u32)i * 4096u + (u32)lane * 128u : WS_ML + (u32)(i & 63) * 128u);
  }
  v4u bv[8];
  {
    const int slot = w == 0 ? 3 : w < 4 ? w - 1 : w;
    fl_wait(lb + F_PROG, P_UK + 8 * (slot + 1));
    const u32 key = (u32)lane & 15u;
    const u32 ro = L_RA + (u32)slot * 8192u + key * 256u;
#pragma unroll
    for (int s = 0; s < 8; ++s) bv[s] = lds16(lds, ro + (u32)(s >> 2) * 4096u + ((((u32)(s & 3) * 4u + gq) ^ swz(key)) * 16u));
    fl_add(lb + F_PULL + (u32)slot * 4u, lane);
  }
  STAMP(3);
  {
    const u32 qo = WS_QL + (u32)b * 65536u + (u32)w * 8192u + (u32)lane * 16u;
    v4u gl[8];
    fl_wait(lb + F_QP, 4);
    POLLN(gl, 8, qo, 1024u, 2u)
    asm volatile("" :: "v"(pre0));
    STAMP(4);
    v4f acc0 = {0.f, 0.f, 0.f, 0.f}, acc1 = {0.f, 0.f, 0.f, 0.f};
#pragma unroll
    for (int s = 0; s < 4; ++s) {
      const v4u av = frag(v4u{gl[2 * s][0], gl[2 * s][2], gl[2 * s + 1][0], gl[2 * s + 1][2]}, pa);
      acc0 = __builtin_amdgcn_mfma_f32_16x16x32_bf16(as_bf8(av), as_bf8(bv[s]), acc0, 0, 0, 0);
      acc1 = __builtin_amdgcn_mfma_f32_16x16x32_bf16(as_bf8(av), as_bf8(bv[4 + s]), acc1, 0, 0, 0);
    }
    float* sr = reinterpret_cast<float*>(lds + L_SRED) + (w * 16 + gq * 4) * 33 + (lane & 15);
#pragma unroll
    for (int q = 0; q < 4; ++q) { sr[q * 33] = acc0[q]; sr[q * 33 + 16] = acc1[q]; }
  }
  sbar(lb + F_B, lane);
  STAMP(5);
  {
    const int hq = t >> 5, key = t & 31;
    const float* sr = reinterpret_cast<const float*>(lds + L_SRED) + hq * 33 + key;
    float s = 0.f;
#pragma unroll
    for (int ww = 0; ww < 8; ++ww) s += sr[ww * 16 * 33];
    s *= 0.0625f;
    st_gran(ws, WS_SC + (u32)((G * 16 + hq) * 256 + j * 32 + key) * 8u, tag, __float_as_uint(s));
    fl_add(lb + F_SR, lane);
    if (w == 3) pre0 = PRE_RD(WS_PV + (u32)i * 8192u + (u32)lane * 128u);
  }
  {
    float ml[4];
    const u32 so = WS_SC + (u32)((G * 16 + 2 * w) * 256 + 2 * lane) * 8u;
    v4u gs[4];
    POLLN(gs, 4, so, 1024u, 3u)
    asm volatile("" :: "v"(pre0));
    STAMP(6);
    fl_wait(lb + F_SR, 8);
#pragma unroll
    for (int hh = 0; hh < 2; ++hh) {
      const float sv[4] = {__uint_as_float(gs[2 * hh][0]), __uint_as_float(gs[2 * hh][2]), __uint_as_float(gs[2 * hh + 1][0]), __uint_as_float(gs[2 * hh + 1][2])};
      const float m = wave_max(fmaxf(fmaxf(sv[0], sv[1]), fmaxf(sv[2], sv[3])));
      const u32 p0 = pk2(__expf(sv[0] - m), __expf(sv[1] - m)), p1 = pk2(__expf(sv[2] - m), __expf(sv[3] - m));
      const float l = wave_sum((lo_f(p0) + hi_f(p0)) + (lo_f(p1) + hi_f(p1)));
      *reinterpret_cast<u32*>(lds + L_P + (2 * w + hh) * 528 + lane * 4) = p0;
      *reinterpret_cast<u32*>(lds + L_P + (2 * w + hh) * 528 + 256 + lane * 4) = p1;
      ml[2 * hh] = m; ml[2 * hh + 1] = l;
    }
    if (j == 0 && lane < 4) {
      const float v = lane == 0 ? ml[0] : lane == 1 ? ml[1] : lane == 2 ? ml[2] : ml[3];
      st_gran(ws, WS_ML + (u32)(((b * 16 + 2 * w + (lane >> 1)) * 16 + g) * 2 + (lane & 1)) * 8u, tag, __float_as_uint(v));
    }
  }
  sbar(lb + F_B + 4, lane);
  STAMP(22);
  fl_wait(lb + F_PROG, P_COL);
  STAMP(7);
  {
    v4f acc0 = {0.f, 0.f, 0.f, 0.f}, acc1 = {0.f, 0.f, 0.f, 0.f};
    const u32 po = L_P + (u32)(lane & 15) * 528u + gq * 16u;
    const u32 k0 = L_RA + off_b(8 * gq + qq, 2 * w + (pp >> 1)) + 8 * (pp & 1);
    const u32 k1 = L_RA + off_b(8 * gq + 4 + qq, 2 * w + (pp >> 1)) + 8 * (pp & 1);
#pragma unroll
    for (int ks = 0; ks < 4; ++ks) {
      acc0 = __builtin_amdgcn_mfma_f32_16x16x32_bf16(as_bf8(lds16(lds, po + ks * 64)), tr_pair(lds, k0 + ks * 8192, k1 + ks * 8192), acc0, 0, 0, 0);
      acc1 = __builtin_amdgcn_mfma_f32_16x16x32_bf16(as_bf8(lds16(lds, po + (ks + 4) * 64)), tr_pair(lds, k0 + (ks + 4) * 8192, k1 + (ks + 4) * 8192), acc1, 0, 0, 0);
    }
    const v4f acc = acc0 + acc1;
    const int hi8 = (lane >> 3) & 1;
    const float s0 = hi8 ? acc[0] : acc[2], s1 = hi8 ? acc[1] : acc[3];
    const float n0 = dppf<0x128>(s0), n1 = dppf<0x128>(s1);
    const u32 g0 = hi8 ? pk2(n0, acc[2]) : pk2(acc[0], n0);
    const u32 g1 = hi8 ? pk2(n1, acc[3]) : pk2(acc[1], n1);
    const int hq = (lane >> 4) * 4 + 2 * hi8;
    const u32 go = WS_PV + (u32)(((b * 16 + hq) * 16 + g) * 512 + j * 64 + w * 8 + (lane & 7)) * 8u;
    st_gran(ws, go, tag, g0);
    st_gran(ws, go + 16u * 512u * 8u, tag, g1);
  }
  if (w == 3) pre0 = PRE_RD(lane < 32 ? WS_CP + (u32)i * 4096u + (u32)lane * 128u : WS_OG + (u32)i * 3584u + (u32)(lane < 60 ? lane - 32 : 0) * 128u);
  STAMP(8);
  {
    const int bq = w >> 2;
    const u32 gg = (u32)(w & 3) * 4u + gq;
    const u32 po = WS_PV + (((u32)(bq * 16 + h1i) * 16u + gg) * 512u + (u32)cs * 32u + (u32)(lane & 15) * 2u) * 8u;
    gu64* mlp = (gu64*)(ws + WS_ML) + (bq * 16 + h1i) * 32 + (lane & 31);
    v4u pv;
    u64 mlv;
#pragma unroll 1
    for (u32 spins = 0;;) {
      mlv = __hip_atomic_load(mlp, RLX_AGENT);
      if (__all((u32)(mlv >> 32) == tag)) break;
      SPIN_FAIL(4u)
    }
    const float mv = __uint_as_float((u32)mlv);
    const float other = dppf<0xB1>(mv);
    const float mg = (lane & 1) ? other : mv, lg = (lane & 1) ? mv : other;
    const float Mx = wave_max(mg);
    const float wg = __expf(mg - Mx);
    const float Ls = wave_sum(wg * lg) * 0.25f;
    const float wn = wg / Ls;
    const float wv = __int_as_float(__builtin_amdgcn_ds_bpermute((int)gg * 8, __float_as_int(wn)));
    POLL1(pv, po, 4u)
    asm volatile("" :: "v"(pre0));
    STAMP(9);
    float a[4] = {wv * lo_f(pv[0]), wv * lo_f(pv[2]), wv * hi_f(pv[0]), wv * hi_f(pv[2])};
#pragma unroll
    for (int q = 0; q < 4; ++q) a[q] = x32_add(x16_add(a[q]));
    if (lane < 16) {
      float* dp = reinterpret_cast<float*>(lds + L_DP) + w * 64 + (lane >> 2) * 16 + (lane & 3) * 2;
      dp[0] = a[0]; dp[1] = a[1]; dp[8] = a[2]; dp[9] = a[3];
    }
  }
  sbar(lb + F_B + 8, lane);
  STAMP(23);
  fl_wait(lb + F_PROG, P_UV);
  STAMP(10);
  {
    v4f acc0 = {0.f, 0.f, 0.f, 0.f}, acc1 = {0.f, 0.f, 0.f, 0.f};
    const u32 dpo = L_DP + (u32)((lane >> 2) & 1) * 1024u + gq * 32u;
    const u32 rr = 8u * gq + qq;
    const u32 f = (qq << 1) | ((gq & 1u) << 3);
    const u32 bo0 = L_UV + rr * 512u + ((((u32)w * 4u + (pp >> 1)) ^ f) * 16u) + (pp & 1u) * 8u;
    const u32 bo1 = L_UV + rr * 512u + ((((u32)w * 4u + 2u + (pp >> 1)) ^ f) * 16u) + (pp & 1u) * 8u;
#pragma unroll
    for (int s = 0; s < 2; ++s) {
      v4f c0 = {0.f, 0.f, 0.f, 0.f}, c1 = {0.f, 0.f, 0.f, 0.f};
#pragma unroll
      for (int ww = 0; ww < 4; ++ww) { c0 += lds16f(lds, dpo + ww * 256 + s * 128); c1 += lds16f(lds, dpo + ww * 256 + s * 128 + 16); }
      v4u av = {pk2(c0[0], c0[1]), pk2(c0[2], c0[3]), pk2(c1[0], c1[1]), pk2(c1[2], c1[3])};
      if (lane & 1) {
        av = v4u{pk2(c0[0] - lo_f(av[0]), c0[1] - hi_f(av[0])), pk2(c0[2] - lo_f(av[1]), c0[3] - hi_f(av[1])),
                 pk2(c1[0] - lo_f(av[2]), c1[1] - hi_f(av[2])), pk2(c1[2] - lo_f(av[3]), c1[3] - hi_f(av[3]))};
      }
      acc0 = __builtin_amdgcn_mfma_f32_16x16x32_bf16(as_bf8(av), tr_pair(lds, bo0 + s * 16384, bo0 + s * 16384 + 2048), acc0, 0, 0, 0);
      acc1 = __builtin_amdgcn_mfma_f32_16x16x32_bf16(as_bf8(av), tr_pair(lds, bo1 + s * 16384, bo1 + s * 16384 + 2048), acc1, 0, 0, 0);
    }
    if (lane < 32) {
      const u32 co = WS_CP + ((((u32)(h1i * 2) + gq) * 8u + (u32)w) * 16u + (u32)cs) * 256u + (u32)(lane & 15) * 8u;
      st_gran(ws, co, tag, __float_as_uint(acc0[0] + acc0[1]));
      st_gran(ws, co + 128u, tag, __float_as_uint(acc1[0] + acc1[1]));
    }
  }
  STAMP(11);
  if (w < 4) {
    const u32 co = WS_CP + (u32)((h1i * 2 + b2i) * 8 + ds) * 4096u + (u32)t * 16u;
    v4u cv;
    POLL1(cv, co, 6u)
    const float c0 = x32_add(x16_add(__uint_as_float(cv[0]))), c1 = x32_add(x16_add(__uint_as_float(cv[2])));
    if (lane < 16) *reinterpret_cast<v2u*>(lds + L_CR + w * 128 + lane * 8) = v2u{__float_as_uint(c0), __float_as_uint(c1)};
  }
  STAMP(15);
  sbar(lb + F_B + 12, lane);
  fl_wait(lb + F_PROG, P_WO);
  STAMP(16);
  if (w < 7) {
    const float* cr = reinterpret_cast<const float*>(lds + L_CR) + (lane & 31);
    const float cv = (cr[0] + cr[32]) + (cr[64] + cr[96]);
    *reinterpret_cast<u16*>(lds + L_VST + w * 64 + (lane & 31) * 2) = f2bf(cv);
    const bf8 a0 = as_bf8(lds16(lds, L_VST + w * 64 + gq * 16)), a1 = as_bf8(lds16(lds, L_VH + gq * 16));
    const u32 bo0 = L_WO + (8u * gq + qq) * 896u + (u32)w * 128u + pp * 8u;
    const u32 bo1 = L_W2 + ((8u * gq + qq) & 15u) * 896u + (u32)w * 128u + pp * 8u;
#pragma unroll
    for (int n = 0; n < 4; ++n) {
      v4f acc = {0.f, 0.f, 0.f, 0.f};
      acc = __builtin_amdgcn_mfma_f32_16x16x32_bf16(a0, tr_pair(lds, bo0 + n * 32, bo0 + n * 32 + 3584), acc, 0, 0, 0);
      acc = __builtin_amdgcn_mfma_f32_16x16x32_bf16(a1, tr_pair(lds, bo1 + n * 32, bo1 + n * 32 + 3584), acc, 0, 0, 0);
      if (lane < 16) st_gran(ws, WS_OG + (u32)((b2i * 128 + hd) * M_ + w * 64 + n * 16 + lane) * 8u, tag, __float_as_uint(acc[0]));
    }
  }
  STAMP(12);
  if (i < 112) {
    const int fb = i / 56, fc = i - fb * 56;
    const u32 oo = WS_OG + ((u32)(fb * 128 + (t >> 2)) * M_ + (u32)(fc * 8 + (t & 3) * 2)) * 8u;
    float xr = 0.f;
    if (t < 8) xr = bf2f(ldh(x + fb * M_ + fc * 8 + t)) + 0.f;
    float br = 0.f;
    if (t < 8) br = bf2f(ldh(b2 + fc * 8 + t));
    v4u ov;
    POLL1(ov, oo, 5u)
    float c0 = __uint_as_float(ov[0]), c1 = __uint_as_float(ov[2]);
    c0 = ror_add<4>(c0); c1 = ror_add<4>(c1);
    c0 = ror_add<8>(c0); c1 = ror_add<8>(c1);
    c0 = x32_add(x16_add(c0)); c1 = x32_add(x16_add(c1));
    if (lane < 4) *reinterpret_cast<v2u*>(lds + L_FR + w * 32 + lane * 8) = v2u{__float_as_uint(c0), __float_as_uint(c1)};
    sbar(lb + F_B + 16, lane);
    STAMP(13);
    if (t < 8) {
      float a = 0.f;
#pragma unroll
      for (int q = 0; q < 8; ++q) a += reinterpret_cast<const float*>(lds + L_FR)[q * 8 + t];
      *(GAS u16*)(out + fb * M_ + fc * 8 + t) = f2bf(xr + a + br);
    }
    if (i == 0 && t == 0) __hip_atomic_store((gu32*)(ws + WS_EP), tag, RLX_AGENT);
    STAMP(14);
  }
  STAMP_FLUSH;
}

extern "C" void kernel_launch(void* const* d_in, const int* in_sizes, int n_in,
                              void* d_out, int out_size, void* d_ws, size_t ws_size,
                              hipStream_t stream) {
  const u16* x = (const u16*)d_in[0];
  const u16* ckv = (const u16*)d_in[1];
  const u16* wq = (const u16*)d_in[2];
  const u16* wuk = (const u16*)d_in[3];
  const u16* wuv = (const u16*)d_in[4];
  const u16* wo = (const u16*)d_in[5];
  const u16* w1 = (const u16*)d_in[7];
  const u16* b1 = (const u16*)d_in[8];
  const u16* w2 = (const u16*)d_in[9];
  const u16* b2 = (const u16*)d_in[10];
  mla_decode<<<256, 576, 0, stream>>>(x, ckv, wq, wuk, wuv, wo, w1, b1, w2, b2, (char*)d_ws, (u16*)d_out);
}
```

```cpp
#define MEAS 0
#define MWG 0
#define MWAVE 0
#include <hip/hip_runtime.h>
#include <hip/hip_bf16.h>
#include <stdint.h>

typedef unsigned short u16;
typedef unsigned int u32;
typedef unsigned long long u64;
typedef __attribute__((ext_vector_type(4))) unsigned v4u;
typedef __attribute__((ext_vector_type(2))) unsigned v2u;
typedef __attribute__((ext_vector_type(4))) float v4f;
typedef __attribute__((ext_vector_type(8))) __bf16 bf8;
typedef __attribute__((ext_vector_type(4))) short s4;
typedef __attribute__((ext_vector_type(8))) short s8;

#define GAS __attribute__((address_space(1)))
#define LAS __attribute__((address_space(3)))
typedef GAS u32 gu32;
typedef GAS u64 gu64;
#define RLX_AGENT __ATOMIC_RELAXED, __HIP_MEMORY_SCOPE_AGENT

#define M_ 448
#define F_ 1792
#define DC 1024
#define SK 4096

constexpr u32 SPIN_CAP = 1u << 18;

constexpr u32 WS_EP = 0;
constexpr u32 WS_QL = 65536;
constexpr u32 WS_SC = 262144;
constexpr u32 WS_PV = 1310720;
constexpr u32 WS_ML = 3407872;
constexpr u32 WS_CP = 3416064;
constexpr u32 WS_OG = 4464640;
constexpr u32 WS_QG = WS_OG + 2 * 128 * 448 * 8;
constexpr u32 WS_END = WS_QG + 262144;

#define STAMP(k) do { if ((k) == 0) st0 = __builtin_amdgcn_s_memrealtime(); if ((k) == MEAS) st1 = __builtin_amdgcn_s_memrealtime(); } while (0)
#define STAMP_DECL u64 st0 = 0, st1 = 0;
#define STAMP_FLUSH do { if (blockIdx.x == MWG && w == MWAVE) { const u64 te = __builtin_amdgcn_s_memrealtime() + 2 * (st1 - st0); for (u32 n_ = 0; n_ < 100000u; ++n_) { if (__builtin_amdgcn_s_memrealtime() >= te) break; __builtin_amdgcn_s_sleep(1); } } } while (0)

#define BAR() do { asm volatile("s_waitcnt lgkmcnt(0)" ::: "memory"); __builtin_amdgcn_s_barrier(); asm volatile("" ::: "memory"); } while (0)

__device__ __forceinline__ float bf2f(u16 v) { return __uint_as_float(((u32)v) << 16); }
__device__ __forceinline__ u16 f2bf(float f) { __hip_bfloat16 b = __float2bfloat16(f); return *reinterpret_cast<u16*>(&b); }
__device__ __forceinline__ float rbf(float f) { return bf2f(f2bf(f)); }
__device__ __forceinline__ float lo_f(u32 v) { return __uint_as_float(v << 16); }
__device__ __forceinline__ float hi_f(u32 v) { return __uint_as_float(v & 0xffff0000u); }
__device__ __forceinline__ u32 pk2(float a, float b) { return (u32)f2bf(a) | ((u32)f2bf(b) << 16); }
__device__ __forceinline__ v4u ld16(const u16* p) { return __builtin_nontemporal_load((const GAS v4u*)p); }
__device__ __forceinline__ v4u ldp16(const u16* p) { return *(const GAS v4u*)p; }
__device__ __forceinline__ u16 ldh(const u16* p) { return *(const GAS u16*)p; }
__device__ __forceinline__ bf8 as_bf8(v4u v) { return __builtin_bit_cast(bf8, v); }

template <int CTRL>
__device__ __forceinline__ float dppf(float v) {
  return __int_as_float(__builtin_amdgcn_update_dpp(0, __float_as_int(v), CTRL, 0xf, 0xf, true));
}
template <int N> __device__ __forceinline__ float ror_add(float v) { return v + dppf<0x120 + N>(v); }
template <int N> __device__ __forceinline__ float ror_max(float v) { return fmaxf(v, dppf<0x120 + N>(v)); }
__device__ __forceinline__ float x16_add(float v) {
  const u32 b = __float_as_uint(v);
  auto p = __builtin_amdgcn_permlane16_swap(b, b, false, false);
  return __uint_as_float(p[0]) + __uint_as_float(p[1]);
}
__device__ __forceinline__ float x32_add(float v) {
  const u32 b = __float_as_uint(v);
  auto p = __builtin_amdgcn_permlane32_swap(b, b, false, false);
  return __uint_as_float(p[0]) + __uint_as_float(p[1]);
}
__device__ __forceinline__ float x16_max(float v) {
  const u32 b = __float_as_uint(v);
  auto p = __builtin_amdgcn_permlane16_swap(b, b, false, false);
  return fmaxf(__uint_as_float(p[0]), __uint_as_float(p[1]));
}
__device__ __forceinline__ float x32_max(float v) {
  const u32 b = __float_as_uint(v);
  auto p = __builtin_amdgcn_permlane32_swap(b, b, false, false);
  return fmaxf(__uint_as_float(p[0]), __uint_as_float(p[1]));
}
__device__ __forceinline__ float wave_sum(float v) {
  v = ror_add<8>(v); v = ror_add<4>(v); v = ror_add<2>(v); v = ror_add<1>(v);
  return x32_add(x16_add(v));
}
__device__ __forceinline__ float wave_max(float v) {
  v = ror_max<8>(v); v = ror_max<4>(v); v = ror_max<2>(v); v = ror_max<1>(v);
  return x32_max(x16_max(v));
}

__device__ __forceinline__ u32 swz(u32 row) { return ((row & 3u) << 2) | ((row >> 2) & 3u); }
__device__ __forceinline__ u32 off_b(u32 row, u32 ch) { return 256u * row + 16u * (ch ^ swz(row)); }

constexpr u32 L_RA = 0;
constexpr u32 L_UV = 65536;
constexpr u32 L_WO = 98304;
constexpr u32 L_WQ = L_WO;
constexpr u32 L_W1 = L_WO + 14336;
constexpr u32 L_W2 = 126976;
constexpr u32 L_XN = 141312;
constexpr u32 L_P = 143104;
constexpr u32 L_QST = L_P;
constexpr u32 L_SRED = L_P;
constexpr u32 L_DP = L_P + 16896;
constexpr u32 L_CR = L_DP + 2048;
constexpr u32 L_VST = L_CR + 512;
constexpr u32 L_VH = L_VST + 512;
constexpr u32 L_FR = L_VH + 64;
constexpr u32 L_BI = L_FR + 256;
constexpr u32 L_FLAG = L_BI + 64;
constexpr u32 L_END = L_FLAG + 128;
static_assert(L_END <= 163840, "LDS");
constexpr u32 F_PROG = L_FLAG;
constexpr u32 F_PULL = L_FLAG + 4;
constexpr u32 F_UK = L_FLAG + 36;
constexpr u32 F_RO = L_FLAG + 40;
constexpr u32 F_SR = L_FLAG + 44;
constexpr u32 F_B = L_FLAG + 48;
constexpr u32 F_QP = L_FLAG + 68;
constexpr u32 F_XN = L_FLAG + 72;
constexpr u32 F_QS = L_FLAG + 76;
#define W1POS 1
constexpr int P_WQ = 14, P_UK = 46, P_ROWS = 110, P_COL = 174, P_W1 = 188, P_UV = 220, P_W2 = 234, P_WO = 262;
constexpr int P_END = 262;

#define WIN 16
#define WIN2 16
#define WINC 16
constexpr int WMAX = WIN > WIN2 ? (WIN > WINC ? WIN : WINC) : (WIN2 > WINC ? WIN2 : WINC);
#define AUX_COL 2
#define PSLP 2
#define QDLY 12
#define QGATE 4
#define PSTR 128
#define PRE_AUX 0
#define PRE_SC 0
#define POUT 0
#define PRE_RD(off) __builtin_amdgcn_raw_buffer_load_b32(rw, (off), 0, PRE_AUX)
#define PRE 110
#define CPRIO 2
#define SPRIO 3
constexpr u32 LSPIN = 1u << 22;

#define SPIN_FAIL(code) if (++spins > SPIN_CAP) { if (lane == 0) __hip_atomic_store(tmo, (code), RLX_AGENT); break; } __builtin_amdgcn_s_sleep(PSLP); asm volatile("" ::: "memory");

#define POLL1(v, off, code) __builtin_amdgcn_s_setprio(0); _Pragma("unroll 1") for (u32 spins = 0;;) { v = __builtin_amdgcn_raw_buffer_load_b128(rw, (off), 0, 16); if (__all((v[1] == tag) & (v[3] == tag))) break; SPIN_FAIL(code) } __builtin_amdgcn_s_setprio(CPRIO);

#define POLLN(v, N, off, stride, code) { u32 pend = (1u << (N)) - 1u; __builtin_amdgcn_s_setprio(0); _Pragma("unroll 1") for (u32 spins = 0;;) { \
    _Pragma("unroll") for (int s_ = 0; s_ < (N); ++s_) if (pend >> s_ & 1u) v[s_] = __builtin_amdgcn_raw_buffer_load_b128(rw, (off) + s_ * (stride), 0, 16); \
    _Pragma("unroll") for (int s_ = 0; s_ < (N); ++s_) if (pend >> s_ & 1u) { if (__all((v[s_][1] == tag) & (v[s_][3] == tag))) pend &= ~(1u << s_); } \
    if (!pend) break; SPIN_FAIL(code) } __builtin_amdgcn_s_setprio(CPRIO); }

__device__ __forceinline__ void st_gran(char* ws, u32 off, u32 tag, u32 val) {
  __hip_atomic_store((gu64*)(ws + off), ((u64)tag << 32) | val, RLX_AGENT);
}
__device__ __forceinline__ bf8 tr_pair(char* lds, u32 a0, u32 a1) {
  const s4 b0 = __builtin_amdgcn_ds_read_tr16_b64_v4i16((LAS s4*)(lds + a0));
  const s4 b1 = __builtin_amdgcn_ds_read_tr16_b64_v4i16((LAS s4*)(lds + a1));
  const s8 bb = {b0[0], b0[1], b0[2], b0[3], b1[0], b1[1], b1[2], b1[3]};
  return __builtin_bit_cast(bf8, bb);
}
__device__ __forceinline__ v4u lds16(char* lds, u32 a) { return *reinterpret_cast<const v4u*>(lds + a); }
__device__ __forceinline__ v4f lds16f(char* lds, u32 a) { return *reinterpret_cast<const v4f*>(lds + a); }
__device__ __forceinline__ float fdiv(float a, float d, float r) {
  const float q = a * r;
  return fmaf(fmaf(-q, d, a), r, q);
}
__device__ __forceinline__ v4u frag(v4u v, int pa) {
  v4u o;
#pragma unroll
  for (int e = 0; e < 4; ++e) o[e] = (u32)__builtin_amdgcn_ds_bpermute(pa, (int)v[e]);
  return o;
}
__device__ __forceinline__ void gemv448(char* lds, u32 wt, int lane, float& a0, float& a1) {
  const u32 gq = lane >> 4, qq = (lane & 15) >> 2, pp = lane & 3;
  const u32 ao = L_XN + (u32)(lane & 1) * 896u + gq * 16u;
  const u32 bo = wt + (8u * gq + qq) * 32u + pp * 8u;
  v4f acc0 = {0.f, 0.f, 0.f, 0.f}, acc1 = {0.f, 0.f, 0.f, 0.f};
#pragma unroll
  for (int s = 0; s < 7; ++s) {
    acc0 = __builtin_amdgcn_mfma_f32_16x16x32_bf16(as_bf8(lds16(lds, ao + s * 64)), tr_pair(lds, bo + s * 1024, bo + s * 1024 + 128), acc0, 0, 0, 0);
    acc1 = __builtin_amdgcn_mfma_f32_16x16x32_bf16(as_bf8(lds16(lds, ao + (s + 7) * 64)), tr_pair(lds, bo + (s + 7) * 1024, bo + (s + 7) * 1024 + 128), acc1, 0, 0, 0);
  }
  a0 = acc0[0] + acc1[0]; a1 = acc0[1] + acc1[1];
}

__device__ __forceinline__ void fl_write(u32 a, u32 v) { asm volatile("ds_write_b32 %0, %1" :: "v"(a), "v"(v) : "memory"); }
__device__ __forceinline__ void fl_add(u32 a, int lane) { if (lane == 0) asm volatile("ds_add_u32 %0, %1" :: "v"(a), "v"(1u) : "memory"); }
__device__ __forceinline__ int fl_read(u32 a) {
  u32 v; asm volatile("ds_read_b32 %0, %1\n s_waitcnt lgkmcnt(0)" : "=v"(v) : "v"(a) : "memory");
  return (int)__builtin_amdgcn_readfirstlane(v);
}
__device__ __forceinline__ void fl_wait(u32 a, int v) {
#pragma unroll 1
  for (u32 n = 0; n < LSPIN; ++n) { if (fl_read(a) >= v) break; __builtin_amdgcn_s_sleep(0); }
}
__device__ __forceinline__ void sbar(u32 a, int lane) { fl_add(a, lane); fl_wait(a, 8); }
template <int K> __device__ __forceinline__ void drain(u32 fp, int q, int lo) {
  asm volatile("s_waitcnt vmcnt(%0)" :: "n"(K) : "memory");
  fl_write(fp, (u32)(q - K > lo ? q - K : lo));
  if constexpr (K > 0) drain<K - 1>(fp, q, lo);
}
__device__ __forceinline__ void gate(u32 fp, int q, u32 a, int v, int& lo) {
  if (fl_read(a) >= v) return;
  if (lo < q) drain<WMAX - 2>(fp, q, lo);
  lo = q;
  fl_wait(a, v);
}
template <int N, int U, int XM, int SEG, int AUX, int W>
__device__ __forceinline__ void stream(char* lds, u32 lb, const void* base, u32 bytes, u32 vb, u32 sb, u32 ss, u32 ld, int q0, int& lo) {
  const __amdgpu_buffer_rsrc_t rs = __builtin_amdgcn_make_buffer_rsrc((void*)base, 0, bytes, 0x00020000);
#pragma unroll 1
  for (u32 k = 0; k < (u32)(N / U); ++k) {
    const int qk = q0 + (int)(k * U);
    if (SEG == 2) gate(lb + F_PROG, qk, lb + F_PULL + k * 4u, 1, lo);
    const u32 sk = SEG == 1 ? sb + (k < 3u ? k + 1u : k == 3u ? 0u : k) * 256u : sb + k * (U * ss);
#pragma unroll
    for (u32 n = 0; n < (u32)U; ++n) {
      const int q = qk + (int)n;
      asm volatile("s_waitcnt vmcnt(%0)" :: "n"(W - 1) : "memory");
      lo = q - (W - 1) > lo ? q - (W - 1) : lo;
      fl_write(lb + F_PROG, (u32)lo);
      const u32 xm = XM == 1 ? (n & 3u) << 4 : XM == 2 ? ((2u * n) & 15u) << 4 : XM == 3 ? (((n & 1u) << 2) | (((n >> 2) & 1u) << 3)) << 4 : 0u;
      __builtin_amdgcn_raw_ptr_buffer_load_lds(rs, (LAS void*)(lds + ld + k * (U * 1024u) + n * 1024u), 16, vb ^ xm, sk + n * ss, 0, AUX);
    }
  }
}

__global__ __launch_bounds__(576) void mla_decode(const u16* x, const u16* ckv, const u16* wq, const u16* wuk,
                                                  const u16* wuv, const u16* wo, const u16* w1, const u16* b1,
                                                  const u16* w2, const u16* b2, char* ws, u16* out) {
  __shared__ __attribute__((aligned(16))) char lds[L_END];
  const int t = threadIdx.x, lane = t & 63;
  const int w = __builtin_amdgcn_readfirstlane(t >> 6);
  const int i = blockIdx.x, xcd = i & 7, r = i >> 3;
  const int h1i = xcd * 2 + (r >> 4), cs = r & 15;
  const int G = xcd * 4 + (r >> 3), b = G >> 4, g = G & 15, j = r & 7;
  const int b2i = r & 1, hd = xcd * 16 + (r >> 1), ds = hd & 7;
  const int fsl = hd < 112 ? hd : 0;
  STAMP_DECL
  STAMP(0);
  asm volatile("" : "+s"(x), "+s"(ckv), "+s"(wq), "+s"(wuk), "+s"(wuv), "+s"(wo), "+s"(w1), "+s"(b1), "+s"(w2), "+s"(b2), "+s"(ws), "+s"(out));
  gu32* tmo = (gu32*)(ws + WS_EP + 128);
  const __amdgpu_buffer_rsrc_t rw = __builtin_amdgcn_make_buffer_rsrc(ws, 0, WS_END, 0x00020000);
  const u32 gq = lane >> 4, qq = (lane & 15) >> 2, pp = lane & 3;

  const u32 lb = (u32)(uintptr_t)(LAS char*)lds;

  if (w == 8) {
    __builtin_amdgcn_s_setprio(SPRIO);
    if (lane < 32) fl_write(lb + L_FLAG + lane * 4, 0u);
    BAR();
    int lo = 0;
    const u32 hb = (u32)lane >> 5;
    const u32 vk = gq * 2048u + (((u32)lane & 15u) ^ (gq << 2)) * 16u;
    stream<14, 7, 0, 0, 0, WIN>(lds, lb, wq, M_ * 8192u, (u32)(lane >> 3) * 8192u + (((u32)lane & 7u) ^ ((((u32)lane >> 3) & 3u) << 1)) * 16u, (u32)(cs & 3) * (112u * 8192u) + (u32)h1i * 512u + (u32)(cs >> 2) * 128u, 8u * 8192u, L_WQ, 0, lo);
    stream<32, 8, 2, 0, 2, WIN>(lds, lb, wuk, 1024u * 8192u, hb * 8192u + (((u32)lane & 31u) ^ hb) * 16u, (u32)(cs * 64) * 8192u + (u32)h1i * 512u, 16384u, L_UV, P_UK - 32, lo);
    STAMP(24);
    stream<64, 8, 1, 1, 0, WIN>(lds, lb, ckv, 2u * SK * DC * 2u, vk, (u32)(b * SK + g * 256 + j * 32) * 2048u, 8192u, L_RA, P_UK, lo);
    STAMP(25);
    stream<64, 8, 1, 2, AUX_COL, WINC>(lds, lb, ckv, 2u * SK * DC * 2u, vk, (u32)(b * SK + g * 256) * 2048u + (u32)j * 256u, 8192u, L_RA, P_ROWS, lo);
    STAMP(26);
    stream<14, 7, 0, 0, 0, WIN>(lds, lb, w1, M_ * F_ * 2u, (u32)(lane >> 1) * (F_ * 2u) + (u32)(lane & 1) * 16u, (u32)fsl * 32u, 32u * F_ * 2u, L_W1, P_W1 - 14, lo);
    gate(lb + F_PROG, P_UV - 32, lb + F_UK, 4, lo);
    stream<32, 8, 3, 0, 2, WIN2>(lds, lb, wuv, 1024u * 8192u, hb * 8192u + (((u32)lane & 31u) ^ (hb << 1)) * 16u, (u32)(cs * 64) * 8192u + (u32)h1i * 512u, 16384u, L_UV, P_UV - 32, lo);
    stream<14, 7, 0, 0, 2, WIN2>(lds, lb, w2, 1792u * 896u, (u32)lane * 16u, (u32)(fsl * 16) * 896u, 1024u, L_W2, P_W2 - 14, lo);
    STAMP(27);
    {
      gate(lb + F_PROG, P_W2, lb + F_RO, 4, lo);
      float a0, a1;
      gemv448(lds, L_W1, lane, a0, a1);
      const float hv = rbf(rbf(b2i ? a1 : a0) + *reinterpret_cast<const float*>(lds + L_BI + (lane & 15) * 4));
      const float ge = 0.5f * hv * (1.f + tanhf(0.7978845608028654f * (hv + 0.044715f * hv * hv * hv)));
      if (lane < 32) *reinterpret_cast<u16*>(lds + L_VH + lane * 2) = (lane < 16 && hd < 112) ? f2bf(ge) : (u16)0;
      asm volatile("s_waitcnt lgkmcnt(0)" ::: "memory");
    }
    STAMP(28);
    stream<28, 7, 0, 0, 2, WIN2>(lds, lb, wo, 4096u * 896u, (u32)lane * 16u, (u32)(hd * 32) * 896u, 1024u, L_WO, P_W2, lo);
    drain<WMAX - 1>(lb + F_PROG, P_END, lo);
    STAMP(29);
    STAMP_FLUSH;
    return;
  }

  __builtin_amdgcn_s_setprio(CPRIO);
  const u32 epv = __builtin_amdgcn_raw_buffer_load_b32(rw, WS_EP, 0, 0);
  const int pa = (4 * (lane & 15) + (lane >> 4)) * 4;
  u32 tag, pre0 = 0;
  if (w == 0) {
    const int lc = lane < 56 ? lane : 0;
    const v4u xr0 = ldp16(x + lc * 8), xr1 = ldp16(x + M_ + lc * 8);
    STAMP(21);
    BAR();
    float xa[8], xb[8];
#pragma unroll
    for (int p = 0; p < 4; ++p) { xa[2 * p] = lo_f(xr0[p]); xa[2 * p + 1] = hi_f(xr0[p]); xb[2 * p] = lo_f(xr1[p]); xb[2 * p + 1] = hi_f(xr1[p]); }
    const float act = lane < 56 ? 1.f : 0.f;
    float s0 = 0.f, s1 = 0.f;
#pragma unroll
    for (int k = 0; k < 8; ++k) { s0 += xa[k]; s1 += xb[k]; }
    s0 = wave_sum(s0 * act); s1 = wave_sum(s1 * act);
    const float m0 = s0 * (1.f / M_), m1 = s1 * (1.f / M_);
    float v0 = 0.f, v1 = 0.f;
#pragma unroll
    for (int k = 0; k < 8; ++k) { const float d0 = xa[k] - m0, d1 = xb[k] - m1; v0 += d0 * d0; v1 += d1 * d1; }
    v0 = wave_sum(v0 * act); v1 = wave_sum(v1 * act);
    const float mb0 = rbf(m0), mb1 = rbf(m1);
    const float den0 = rbf(sqrtf(rbf(rbf(v0 * (1.f / M_)) + 1e-5f)));
    const float den1 = rbf(sqrtf(rbf(rbf(v1 * (1.f / M_)) + 1e-5f)));
    const float r0 = __builtin_amdgcn_rcpf(den0), r1 = __builtin_amdgcn_rcpf(den1);
    v4u na, nb;
#pragma unroll
    for (int p = 0; p < 4; ++p) {
      na[p] = pk2(fdiv(rbf(xa[2 * p] - mb0), den0, r0), fdiv(rbf(xa[2 * p + 1] - mb0), den0, r0));
      nb[p] = pk2(fdiv(rbf(xb[2 * p] - mb1), den1, r1), fdiv(rbf(xb[2 * p + 1] - mb1), den1, r1));
    }
    if (lane < 56) {
      *reinterpret_cast<v4u*>(lds + L_XN + lane * 16) = na;
      *reinterpret_cast<v4u*>(lds + L_XN + 896 + lane * 16) = nb;
    }
    fl_add(lb + F_XN, lane);
    STAMP(17);
  } else if (w < 4) {
    float bias = 0.f;
    if (w == 1) bias = bf2f(ldh(b1 + fsl * 16 + (lane & 15)));
    if (w == 3) pre0 = __builtin_amdgcn_raw_buffer_load_b32(rw, lane < 8 ? WS_QL + (u32)i * 512u + (u32)lane * 64u : WS_QG + (u32)i * 1024u + (u32)(lane & 15) * 64u, 0, PRE_AUX);
    BAR();
    if (w == 1 && lane < 16) *reinterpret_cast<float*>(lds + L_BI + lane * 4) = bias;
    fl_wait(lb + F_XN, 1);
  }
  if (w < 4) {
    fl_wait(lb + F_PROG, P_WQ);
    STAMP(18);
    const u32 ao = L_XN + (u32)(lane & 1) * 896u + (u32)(cs & 3) * 224u;
    const u32 ch = ((((u32)w * 2u + (pp >> 1)) ^ (qq << 1)) * 16u) + (pp & 1u) * 8u;
    const u32 bo = L_WQ + (8u * gq + qq) * 128u + ch;
    v4f acc0 = {0.f, 0.f, 0.f, 0.f}, acc1 = {0.f, 0.f, 0.f, 0.f};
    acc0 = __builtin_amdgcn_mfma_f32_16x16x32_bf16(as_bf8(lds16(lds, ao + gq * 16u)), tr_pair(lds, bo, bo + 512), acc0, 0, 0, 0);
    acc1 = __builtin_amdgcn_mfma_f32_16x16x32_bf16(as_bf8(lds16(lds, ao + gq * 16u + 64)), tr_pair(lds, bo + 4096, bo + 4096 + 512), acc1, 0, 0, 0);
    acc0 = __builtin_amdgcn_mfma_f32_16x16x32_bf16(as_bf8(lds16(lds, ao + gq * 16u + 128)), tr_pair(lds, bo + 8192, bo + 8192 + 512), acc0, 0, 0, 0);
    acc1 = __builtin_amdgcn_mfma_f32_16x16x16bf16_1k(*reinterpret_cast<const s4*>(lds + ao + 192u + gq * 8u),
                                                     __builtin_amdgcn_ds_read_tr16_b64_v4i16((LAS s4*)(lds + L_WQ + (96u + 4u * gq + qq) * 128u + ch)), acc1, 0, 0, 0);
    tag = __builtin_amdgcn_readfirstlane(epv) + 1u;
    if (lane < 32) st_gran(ws, WS_QG + (u32)h1i * 16384u + (u32)(cs & 3) * 4096u + (u32)(cs >> 2) * 1024u + (u32)w * 256u + (u32)lane * 8u, tag,
                           __float_as_uint(gq ? acc0[1] + acc1[1] : acc0[0] + acc1[0]));
    STAMP(1);
    fl_add(lb + F_RO, lane);
    asm volatile("" :: "v"(pre0));
    if (PRE_SC == 0 && w == 3) pre0 = PRE_RD(lane < 32 ? WS_SC + (u32)i * 4096u + (u32)lane * 128u : WS_ML + (u32)(i & 63) * 128u);
  } else {
    v4u wk[8];
    BAR();
    tag = __builtin_amdgcn_readfirstlane(epv) + 1u;
    fl_wait(lb + F_RO, QGATE);
    if (QDLY) __builtin_amdgcn_s_sleep(QDLY);
    STAMP(19);
    const u32 qo = WS_QG + (u32)h1i * 16384u + (u32)(w - 4) * 1024u + (u32)lane * 16u;
    v4u ga[4];
    POLLN(ga, 4, qo, 4096u, 1u)
    {
      const float q0 = (__uint_as_float(ga[0][0]) + __uint_as_float(ga[1][0])) + (__uint_as_float(ga[2][0]) + __uint_as_float(ga[3][0]));
      const float q1 = (__uint_as_float(ga[0][2]) + __uint_as_float(ga[1][2])) + (__uint_as_float(ga[2][2]) + __uint_as_float(ga[3][2]));
      *reinterpret_cast<u32*>(lds + L_QST + (u32)((lane >> 3) & 1) * 512u + (u32)(w - 4) * 128u + gq * 32u + (u32)(lane & 7) * 4u) = pk2(q0, q1);
    }
    fl_add(lb + F_QS, lane);
    STAMP(20);
    fl_wait(lb + F_PROG, P_UK);
    {
      const u32 ko = L_UV + (u32)((w - 4) * 16 + (lane & 15)) * 512u;
#pragma unroll
      for (int s = 0; s < 8; ++s) wk[s] = lds16(lds, ko + ((((u32)s * 4u + gq) ^ ((u32)lane & 15u)) * 16u));
    }
    fl_add(lb + F_UK, lane);
    fl_wait(lb + F_QS, 4);
    const u32 ao = L_QST + (u32)(lane & 1) * 512u + gq * 16u;
    v4f acc0 = {0.f, 0.f, 0.f, 0.f}, acc1 = {0.f, 0.f, 0.f, 0.f};
#pragma unroll
    for (int s = 0; s < 4; ++s) {
      acc0 = __builtin_amdgcn_mfma_f32_16x16x32_bf16(as_bf8(lds16(lds, ao + s * 64)), as_bf8(wk[s]), acc0, 0, 0, 0);
      acc1 = __builtin_amdgcn_mfma_f32_16x16x32_bf16(as_bf8(lds16(lds, ao + (s + 4) * 64)), as_bf8(wk[s + 4]), acc1, 0, 0, 0);
    }
    const float a0 = acc0[0] + acc1[0], a1 = acc0[1] + acc1[1];
    const float o0 = dppf<0xB1>(a0), o1 = dppf<0xB1>(a1);
    if (lane < 16) {
      const int bb = lane & 1, wq4 = w - 4;
      const u32 qa = (u32)bb * 65536u + (u32)((cs * 2 + (wq4 >> 1)) * 2 + ((lane >> 2) & 1)) * 1024u + (u32)h1i * 64u
                     + (u32)((wq4 & 1) * 2 + (lane >> 3)) * 16u + (u32)((lane >> 1) & 1) * 8u;
      st_gran(ws, WS_QL + qa, tag, bb ? pk2(o1, a1) : pk2(a0, o0));
    }
    STAMP(2);
    fl_add(lb + F_QP, lane);
    if (PRE_SC == 1 && w == 7) pre0 = PRE_RD(lane < 32 ? WS_SC + (u32)i * 4096u + (u32)lane * 128u : WS_ML + (u32)(i & 63) * 128u);
  }
  v4u bv[8];
  {
    const int slot = w == 0 ? 3 : w < 4 ? w - 1 : w;
    fl_wait(lb + F_PROG, P_UK + 8 * (slot + 1));
    const u32 key = (u32)lane & 15u;
    const u32 ro = L_RA + (u32)slot * 8192u + key * 256u;
#pragma unroll
    for (int s = 0; s < 8; ++s) bv[s] = lds16(lds, ro + (u32)(s >> 2) * 4096u + ((((u32)(s & 3) * 4u + gq) ^ swz(key)) * 16u));
    fl_add(lb + F_PULL + (u32)slot * 4u, lane);
  }
  STAMP(3);
  {
    const u32 qo = WS_QL + (u32)b * 65536u + (u32)w * 8192u + (u32)lane * 16u;
    v4u gl[8];
    fl_wait(lb + F_QP, 4);
    POLLN(gl, 8, qo, 1024u, 2u)
    asm volatile("" :: "v"(pre0));
    STAMP(4);
    v4f acc0 = {0.f, 0.f, 0.f, 0.f}, acc1 = {0.f, 0.f, 0.f, 0.f};
#pragma unroll
    for (int s = 0; s < 4; ++s) {
      const v4u av = frag(v4u{gl[2 * s][0], gl[2 * s][2], gl[2 * s + 1][0], gl[2 * s + 1][2]}, pa);
      acc0 = __builtin_amdgcn_mfma_f32_16x16x32_bf16(as_bf8(av), as_bf8(bv[s]), acc0, 0, 0, 0);
      acc1 = __builtin_amdgcn_mfma_f32_16x16x32_bf16(as_bf8(av), as_bf8(bv[4 + s]), acc1, 0, 0, 0);
    }
    float* sr = reinterpret_cast<float*>(lds + L_SRED) + (w * 16 + gq * 4) * 33 + (lane & 15);
#pragma unroll
    for (int q = 0; q < 4; ++q) { sr[q * 33] = acc0[q]; sr[q * 33 + 16] = acc1[q]; }
  }
  sbar(lb + F_B, lane);
  STAMP(5);
  {
    const int hq = t >> 5, key = t & 31;
    const float* sr = reinterpret_cast<const float*>(lds + L_SRED) + hq * 33 + key;
    float s = 0.f;
#pragma unroll
    for (int ww = 0; ww < 8; ++ww) s += sr[ww * 16 * 33];
    s *= 0.0625f;
    st_gran(ws, WS_SC + (u32)((G * 16 + hq) * 256 + j * 32 + key) * 8u, tag, __float_as_uint(s));
    fl_add(lb + F_SR, lane);
    if (w == 3) pre0 = PRE_RD(WS_PV + (u32)i * 8192u + (u32)lane * 128u);
  }
  {
    float ml[4];
    const u32 so = WS_SC + (u32)((G * 16 + 2 * w) * 256 + 2 * lane) * 8u;
    v4u gs[4];
    POLLN(gs, 4, so, 1024u, 3u)
    asm volatile("" :: "v"(pre0));
    STAMP(6);
    fl_wait(lb + F_SR, 8);
#pragma unroll
    for (int hh = 0; hh < 2; ++hh) {
      const float sv[4] = {__uint_as_float(gs[2 * hh][0]), __uint_as_float(gs[2 * hh][2]), __uint_as_float(gs[2 * hh + 1][0]), __uint_as_float(gs[2 * hh + 1][2])};
      const float m = wave_max(fmaxf(fmaxf(sv[0], sv[1]), fmaxf(sv[2], sv[3])));
      const u32 p0 = pk2(__expf(sv[0] - m), __expf(sv[1] - m)), p1 = pk2(__expf(sv[2] - m), __expf(sv[3] - m));
      const float l = wave_sum((lo_f(p0) + hi_f(p0)) + (lo_f(p1) + hi_f(p1)));
      *reinterpret_cast<u32*>(lds + L_P + (2 * w + hh) * 528 + lane * 4) = p0;
      *reinterpret_cast<u32*>(lds + L_P + (2 * w + hh) * 528 + 256 + lane * 4) = p1;
      ml[2 * hh] = m; ml[2 * hh + 1] = l;
    }
    if (j == 0 && lane < 4) {
      const float v = lane == 0 ? ml[0] : lane == 1 ? ml[1] : lane == 2 ? ml[2] : ml[3];
      st_gran(ws, WS_ML + (u32)(((b * 16 + 2 * w + (lane >> 1)) * 16 + g) * 2 + (lane & 1)) * 8u, tag, __float_as_uint(v));
    }
  }
  sbar(lb + F_B + 4, lane);
  STAMP(22);
  fl_wait(lb + F_PROG, P_COL);
  STAMP(7);
  {
    v4f acc0 = {0.f, 0.f, 0.f, 0.f}, acc1 = {0.f, 0.f, 0.f, 0.f};
    const u32 po = L_P + (u32)(lane & 15) * 528u + gq * 16u;
    const u32 k0 = L_RA + off_b(8 * gq + qq, 2 * w + (pp >> 1)) + 8 * (pp & 1);
    const u32 k1 = L_RA + off_b(8 * gq + 4 + qq, 2 * w + (pp >> 1)) + 8 * (pp & 1);
#pragma unroll
    for (int ks = 0; ks < 4; ++ks) {
      acc0 = __builtin_amdgcn_mfma_f32_16x16x32_bf16(as_bf8(lds16(lds, po + ks * 64)), tr_pair(lds, k0 + ks * 8192, k1 + ks * 8192), acc0, 0, 0, 0);
      acc1 = __builtin_amdgcn_mfma_f32_16x16x32_bf16(as_bf8(lds16(lds, po + (ks + 4) * 64)), tr_pair(lds, k0 + (ks + 4) * 8192, k1 + (ks + 4) * 8192), acc1, 0, 0, 0);
    }
    const v4f acc = acc0 + acc1;
    const int hi8 = (lane >> 3) & 1;
    const float s0 = hi8 ? acc[0] : acc[2], s1 = hi8 ? acc[1] : acc[3];
    const float n0 = dppf<0x128>(s0), n1 = dppf<0x128>(s1);
    const u32 g0 = hi8 ? pk2(n0, acc[2]) : pk2(acc[0], n0);
    const u32 g1 = hi8 ? pk2(n1, acc[3]) : pk2(acc[1], n1);
    const int hq = (lane >> 4) * 4 + 2 * hi8;
    const u32 go = WS_PV + (u32)(((b * 16 + hq) * 16 + g) * 512 + j * 64 + w * 8 + (lane & 7)) * 8u;
    st_gran(ws, go, tag, g0);
    st_gran(ws, go + 16u * 512u * 8u, tag, g1);
  }
  if (w == 3) pre0 = PRE_RD(lane < 32 ? WS_CP + (u32)i * 4096u + (u32)lane * 128u : WS_OG + (u32)i * 3584u + (u32)(lane < 60 ? lane - 32 : 0) * 128u);
  STAMP(8);
  {
    const int bq = w >> 2;
    const u32 gg = (u32)(w & 3) * 4u + gq;
    const u32 po = WS_PV + (((u32)(bq * 16 + h1i) * 16u + gg) * 512u + (u32)cs * 32u + (u32)(lane & 15) * 2u) * 8u;
    gu64* mlp = (gu64*)(ws + WS_ML) + (bq * 16 + h1i) * 32 + (lane & 31);
    v4u pv;
    u64 mlv;
#pragma unroll 1
    for (u32 spins = 0;;) {
      mlv = __hip_atomic_load(mlp, RLX_AGENT);
      if (__all((u32)(mlv >> 32) == tag)) break;
      SPIN_FAIL(4u)
    }
    const float mv = __uint_as_float((u32)mlv);
    const float other = dppf<0xB1>(mv);
    const float mg = (lane & 1) ? other : mv, lg = (lane & 1) ? mv : other;
    const float Mx = wave_max(mg);
    const float wg = __expf(mg - Mx);
    const float Ls = wave_sum(wg * lg) * 0.25f;
    const float wn = wg / Ls;
    const float wv = __int_as_float(__builtin_amdgcn_ds_bpermute((int)gg * 8, __float_as_int(wn)));
    POLL1(pv, po, 4u)
    asm volatile("" :: "v"(pre0));
    STAMP(9);
    float a[4] = {wv * lo_f(pv[0]), wv * lo_f(pv[2]), wv * hi_f(pv[0]), wv * hi_f(pv[2])};
#pragma unroll
    for (int q = 0; q < 4; ++q) a[q] = x32_add(x16_add(a[q]));
    if (lane < 16) {
      float* dp = reinterpret_cast<float*>(lds + L_DP) + w * 64 + (lane >> 2) * 16 + (lane & 3) * 2;
      dp[0] = a[0]; dp[1] = a[1]; dp[8] = a[2]; dp[9] = a[3];
    }
  }
  sbar(lb + F_B + 8, lane);
  STAMP(23);
  fl_wait(lb + F_PROG, P_UV);
  STAMP(10);
  {
    v4f acc0 = {0.f, 0.f, 0.f, 0.f}, acc1 = {0.f, 0.f, 0.f, 0.f};
    const u32 dpo = L_DP + (u32)((lane >> 2) & 1) * 1024u + gq * 32u;
    const u32 rr = 8u * gq + qq;
    const u32 f = (qq << 1) | ((gq & 1u) << 3);
    const u32 bo0 = L_UV + rr * 512u + ((((u32)w * 4u + (pp >> 1)) ^ f) * 16u) + (pp & 1u) * 8u;
    const u32 bo1 = L_UV + rr * 512u + ((((u32)w * 4u + 2u + (pp >> 1)) ^ f) * 16u) + (pp & 1u) * 8u;
#pragma unroll
    for (int s = 0; s < 2; ++s) {
      v4f c0 = {0.f, 0.f, 0.f, 0.f}, c1 = {0.f, 0.f, 0.f, 0.f};
#pragma unroll
      for (int ww = 0; ww < 4; ++ww) { c0 += lds16f(lds, dpo + ww * 256 + s * 128); c1 += lds16f(lds, dpo + ww * 256 + s * 128 + 16); }
      v4u av = {pk2(c0[0], c0[1]), pk2(c0[2], c0[3]), pk2(c1[0], c1[1]), pk2(c1[2], c1[3])};
      if (lane & 1) {
        av = v4u{pk2(c0[0] - lo_f(av[0]), c0[1] - hi_f(av[0])), pk2(c0[2] - lo_f(av[1]), c0[3] - hi_f(av[1])),
                 pk2(c1[0] - lo_f(av[2]), c1[1] - hi_f(av[2])), pk2(c1[2] - lo_f(av[3]), c1[3] - hi_f(av[3]))};
      }
      acc0 = __builtin_amdgcn_mfma_f32_16x16x32_bf16(as_bf8(av), tr_pair(lds, bo0 + s * 16384, bo0 + s * 16384 + 2048), acc0, 0, 0, 0);
      acc1 = __builtin_amdgcn_mfma_f32_16x16x32_bf16(as_bf8(av), tr_pair(lds, bo1 + s * 16384, bo1 + s * 16384 + 2048), acc1, 0, 0, 0);
    }
    if (lane < 32) {
      const u32 co = WS_CP + ((((u32)(h1i * 2) + gq) * 8u + (u32)w) * 16u + (u32)cs) * 256u + (u32)(lane & 15) * 8u;
      st_gran(ws, co, tag, __float_as_uint(acc0[0] + acc0[1]));
      st_gran(ws, co + 128u, tag, __float_as_uint(acc1[0] + acc1[1]));
    }
  }
  STAMP(11);
  if (w < 4) {
    const u32 co = WS_CP + (u32)((h1i * 2 + b2i) * 8 + ds) * 4096u + (u32)t * 16u;
    v4u cv;
    POLL1(cv, co, 6u)
    const float c0 = x32_add(x16_add(__uint_as_float(cv[0]))), c1 = x32_add(x16_add(__uint_as_float(cv[2])));
    if (lane < 16) *reinterpret_cast<v2u*>(lds + L_CR + w * 128 + lane * 8) = v2u{__float_as_uint(c0), __float_as_uint(c1)};
  }
  STAMP(15);
  sbar(lb + F_B + 12, lane);
  fl_wait(lb + F_PROG, P_WO);
  STAMP(16);
  if (w < 7) {
    const float* cr = reinterpret_cast<const float*>(lds + L_CR) + (lane & 31);
    const float cv = (cr[0] + cr[32]) + (cr[64] + cr[96]);
    *reinterpret_cast<u16*>(lds + L_VST + w * 64 + (lane & 31) * 2) = f2bf(cv);
    const bf8 a0 = as_bf8(lds16(lds, L_VST + w * 64 + gq * 16)), a1 = as_bf8(lds16(lds, L_VH + gq * 16));
    const u32 bo0 = L_WO + (8u * gq + qq) * 896u + (u32)w * 128u + pp * 8u;
    const u32 bo1 = L_W2 + ((8u * gq + qq) & 15u) * 896u + (u32)w * 128u + pp * 8u;
#pragma unroll
    for (int n = 0; n < 4; ++n) {
      v4f acc = {0.f, 0.f, 0.f, 0.f};
      acc = __builtin_amdgcn_mfma_f32_16x16x32_bf16(a0, tr_pair(lds, bo0 + n * 32, bo0 + n * 32 + 3584), acc, 0, 0, 0);
      acc = __builtin_amdgcn_mfma_f32_16x16x32_bf16(a1, tr_pair(lds, bo1 + n * 32, bo1 + n * 32 + 3584), acc, 0, 0, 0);
      if (lane < 16) st_gran(ws, WS_OG + (u32)((b2i * 128 + hd) * M_ + w * 64 + n * 16 + lane) * 8u, tag, __float_as_uint(acc[0]));
    }
  }
  STAMP(12);
  if (i < 112) {
    const int fb = i / 56, fc = i - fb * 56;
    const u32 oo = WS_OG + ((u32)(fb * 128 + (t >> 2)) * M_ + (u32)(fc * 8 + (t & 3) * 2)) * 8u;
    float xr = 0.f;
    if (t < 8) xr = bf2f(ldh(x + fb * M_ + fc * 8 + t)) + 0.f;
    float br = 0.f;
    if (t < 8) br = bf2f(ldh(b2 + fc * 8 + t));
    v4u ov;
    POLL1(ov, oo, 5u)
    float c0 = __uint_as_float(ov[0]), c1 = __uint_as_float(ov[2]);
    c0 = ror_add<4>(c0); c1 = ror_add<4>(c1);
    c0 = ror_add<8>(c0); c1 = ror_add<8>(c1);
    c0 = x32_add(x16_add(c0)); c1 = x32_add(x16_add(c1));
    if (lane < 4) *reinterpret_cast<v2u*>(lds + L_FR + w * 32 + lane * 8) = v2u{__float_as_uint(c0), __float_as_uint(c1)};
    sbar(lb + F_B + 16, lane);
    STAMP(13);
    if (t < 8) {
      float a = 0.f;
#pragma unroll
      for (int q = 0; q < 8; ++q) a += reinterpret_cast<const float*>(lds + L_FR)[q * 8 + t];
      *(GAS u16*)(out + fb * M_ + fc * 8 + t) = f2bf(xr + a + br);
    }
    if (i == 0 && t == 0) __hip_atomic_store((gu32*)(ws + WS_EP), tag, RLX_AGENT);
    STAMP(14);
  }
  STAMP_FLUSH;
}

extern "C" void kernel_launch(void* const* d_in, const int* in_sizes, int n_in,
                              void* d_out, int out_size, void* d_ws, size_t ws_size,
                              hipStream_t stream) {
  const u16* x = (const u16*)d_in[0];
  const u16* ckv = (const u16*)d_in[1];
  const u16* wq = (const u16*)d_in[2];
  const u16* wuk = (const u16*)d_in[3];
  const u16* wuv = (const u16*)d_in[4];
  const u16* wo = (const u16*)d_in[5];
  const u16* w1 = (const u16*)d_in[7];
  const u16* b1 = (const u16*)d_in[8];
  const u16* w2 = (const u16*)d_in[9];
  const u16* b2 = (const u16*)d_in[10];
  mla_decode<<<256, 576, 0, stream>>>(x, ckv, wq, wuk, wuv, wo, w1, b1, w2, b2, (char*)d_ws, (u16*)d_out);
}
```

```cpp
#include <hip/hip_runtime.h>
#include <hip/hip_bf16.h>
#include <stdint.h>

typedef unsigned short u16;
typedef unsigned int u32;
typedef unsigned long long u64;
typedef __attribute__((ext_vector_type(4))) unsigned v4u;
typedef __attribute__((ext_vector_type(2))) unsigned v2u;
typedef __attribute__((ext_vector_type(4))) float v4f;
typedef __attribute__((ext_vector_type(8))) __bf16 bf8;
typedef __attribute__((ext_vector_type(4))) short s4;
typedef __attribute__((ext_vector_type(8))) short s8;

#define GAS __attribute__((address_space(1)))
#define LAS __attribute__((address_space(3)))
typedef GAS u32 gu32;
typedef GAS u64 gu64;
#define RLX_AGENT __ATOMIC_RELAXED, __HIP_MEMORY_SCOPE_AGENT

#define M_ 448
#define F_ 1792
#define DC 1024
#define SK 4096

constexpr u32 SPIN_CAP = 1u << 18;

constexpr u32 WS_EP = 0;
constexpr u32 WS_QL = 65536;
constexpr u32 WS_SC = 262144;
constexpr u32 WS_PV = 1310720;
constexpr u32 WS_ML = 3407872;
constexpr u32 WS_CP = 3416064;
constexpr u32 WS_OG = 4464640;
constexpr u32 WS_QG = WS_OG + 2 * 128 * 448 * 8;
constexpr u32 WS_END = WS_QG + 262144;

#define STAMP(k) do {} while (0)
#define STAMP_DECL
#define STAMP_FLUSH do {} while (0)

#define BAR() do { asm volatile("s_waitcnt lgkmcnt(0)" ::: "memory"); __builtin_amdgcn_s_barrier(); asm volatile("" ::: "memory"); } while (0)

__device__ __forceinline__ float bf2f(u16 v) { return __uint_as_float(((u32)v) << 16); }
__device__ __forceinline__ u16 f2bf(float f) { __hip_bfloat16 b = __float2bfloat16(f); return *reinterpret_cast<u16*>(&b); }
__device__ __forceinline__ float rbf(float f) { return bf2f(f2bf(f)); }
__device__ __forceinline__ float lo_f(u32 v) { return __uint_as_float(v << 16); }
__device__ __forceinline__ float hi_f(u32 v) { return __uint_as_float(v & 0xffff0000u); }
__device__ __forceinline__ u32 pk2(float a, float b) { return (u32)f2bf(a) | ((u32)f2bf(b) << 16); }
__device__ __forceinline__ v4u ld16(const u16* p) { return __builtin_nontemporal_load((const GAS v4u*)p); }
__device__ __forceinline__ v4u ldp16(const u16* p) { return *(const GAS v4u*)p; }
__device__ __forceinline__ u16 ldh(const u16* p) { return *(const GAS u16*)p; }
__device__ __forceinline__ bf8 as_bf8(v4u v) { return __builtin_bit_cast(bf8, v); }

template <int CTRL>
__device__ __forceinline__ float dppf(float v) {
  return __int_as_float(__builtin_amdgcn_update_dpp(0, __float_as_int(v), CTRL, 0xf, 0xf, true));
}
template <int N> __device__ __forceinline__ float ror_add(float v) { return v + dppf<0x120 + N>(v); }
template <int N> __device__ __forceinline__ float ror_max(float v) { return fmaxf(v, dppf<0x120 + N>(v)); }
__device__ __forceinline__ float x16_add(float v) {
  const u32 b = __float_as_uint(v);
  auto p = __builtin_amdgcn_permlane16_swap(b, b, false, false);
  return __uint_as_float(p[0]) + __uint_as_float(p[1]);
}
__device__ __forceinline__ float x32_add(float v) {
  const u32 b = __float_as_uint(v);
  auto p = __builtin_amdgcn_permlane32_swap(b, b, false, false);
  return __uint_as_float(p[0]) + __uint_as_float(p[1]);
}
__device__ __forceinline__ float x16_max(float v) {
  const u32 b = __float_as_uint(v);
  auto p = __builtin_amdgcn_permlane16_swap(b, b, false, false);
  return fmaxf(__uint_as_float(p[0]), __uint_as_float(p[1]));
}
__device__ __forceinline__ float x32_max(float v) {
  const u32 b = __float_as_uint(v);
  auto p = __builtin_amdgcn_permlane32_swap(b, b, false, false);
  return fmaxf(__uint_as_float(p[0]), __uint_as_float(p[1]));
}
__device__ __forceinline__ float wave_sum(float v) {
  v = ror_add<8>(v); v = ror_add<4>(v); v = ror_add<2>(v); v = ror_add<1>(v);
  return x32_add(x16_add(v));
}
__device__ __forceinline__ float wave_max(float v) {
  v = ror_max<8>(v); v = ror_max<4>(v); v = ror_max<2>(v); v = ror_max<1>(v);
  return x32_max(x16_max(v));
}

__device__ __forceinline__ u32 swz(u32 row) { return ((row & 3u) << 2) | ((row >> 2) & 3u); }
__device__ __forceinline__ u32 off_b(u32 row, u32 ch) { return 256u * row + 16u * (ch ^ swz(row)); }

constexpr u32 L_RA = 0;
constexpr u32 L_UV = 65536;
constexpr u32 L_WO = 98304;
constexpr u32 L_WQ = L_WO;
constexpr u32 L_W1 = L_WO + 14336;
constexpr u32 L_W2 = 126976;
constexpr u32 L_XN = 134144;
constexpr u32 L_P = L_XN + 1792;
constexpr u32 L_QST = L_P;
constexpr u32 L_SRED = L_P;
constexpr u32 L_DP = L_P + 16896;
constexpr u32 L_CR = L_DP + 2048;
constexpr u32 L_VST = L_CR + 1024;
constexpr u32 L_VH = L_VST + 1024;
constexpr u32 L_FR = L_VH + 128;
constexpr u32 L_BI = L_FR + 256;
constexpr u32 L_FLAG = L_BI + 64;
constexpr u32 L_END = L_FLAG + 128;
static_assert(L_END <= 163840, "LDS");
constexpr u32 F_PROG = L_FLAG;
constexpr u32 F_PULL = L_FLAG + 4;
constexpr u32 F_UK = L_FLAG + 36;
constexpr u32 F_RO = L_FLAG + 40;
constexpr u32 F_SR = L_FLAG + 44;
constexpr u32 F_B = L_FLAG + 48;
constexpr u32 F_QP = L_FLAG + 68;
constexpr u32 F_XN = L_FLAG + 72;
constexpr u32 F_QS = L_FLAG + 76;
constexpr u32 F_E1 = L_FLAG + 80;
constexpr u32 F_E2 = L_FLAG + 84;
constexpr u32 F_E3 = L_FLAG + 88;
#define GE1 8
#define GE2 8
#define GE3 8
#define LORD 0
constexpr int P_WQ = 14, P_UK = 46, P_ROWS = 110, P_COL = 174, P_UV = 206, P_W1 = 220, P_W2 = 227, P_WO = 241;
constexpr int P_END = 241;

#define WIN 16
#define WIN2 16
#define WINC 16
constexpr int WMAX = WIN > WIN2 ? (WIN > WINC ? WIN : WINC) : (WIN2 > WINC ? WIN2 : WINC);
#define AUX_COL 2
#define PSLP 2
#define QDLY 12
#define BDLY 16
#define CDLY 14
#define DDLY 12
#define FDLY 16
#define EDLY 0
#define QGATE 4
#define PSTR 128
#define PRE_AUX 0
#define PRE_SC 0
#define POUT 0
#define PRE_RD(off) __builtin_amdgcn_raw_buffer_load_b32(rw, (off), 0, PRE_AUX)
#define PRE 110
#define CPRIO 2
#define SPRIO 3
constexpr u32 LSPIN = 1u << 22;

#define SPIN_FAIL(code) if (++spins > SPIN_CAP) { if (lane == 0) __hip_atomic_store(tmo, (code), RLX_AGENT); break; } __builtin_amdgcn_s_sleep(PSLP); asm volatile("" ::: "memory");

#define POLL1(v, off, code) __builtin_amdgcn_s_setprio(0); _Pragma("unroll 1") for (u32 spins = 0;;) { v = __builtin_amdgcn_raw_buffer_load_b128(rw, (off), 0, 16); if (__all((v[1] == tag) & (v[3] == tag))) break; SPIN_FAIL(code) } __builtin_amdgcn_s_setprio(CPRIO);

#define POLLN(v, N, off, stride, code, ev) { u32 pend = (1u << (N)) - 1u; __builtin_amdgcn_s_setprio(0); _Pragma("unroll 1") for (u32 spins = 0;;) { \
    _Pragma("unroll") for (int s_ = 0; s_ < (N); ++s_) if (pend >> s_ & 1u) v[s_] = __builtin_amdgcn_raw_buffer_load_b128(rw, (off) + s_ * (stride), 0, 16); \
    if ((ev) && spins == 0) fl_add(lb + (ev), lane); \
    _Pragma("unroll") for (int s_ = 0; s_ < (N); ++s_) if (pend >> s_ & 1u) { if (__all((v[s_][1] == tag) & (v[s_][3] == tag))) pend &= ~(1u << s_); } \
    if (!pend) break; SPIN_FAIL(code) } __builtin_amdgcn_s_setprio(CPRIO); }

__device__ __forceinline__ void st_gran(char* ws, u32 off, u32 tag, u32 val) {
  __hip_atomic_store((gu64*)(ws + off), ((u64)tag << 32) | val, RLX_AGENT);
}
#define ST2(off, a, b) __builtin_amdgcn_raw_buffer_store_b128(v4u{(a), tag, (b), tag}, rw, (off), 0, 16)
template <int CTRL> __device__ __forceinline__ u32 dppu(u32 v) { return (u32)__builtin_amdgcn_update_dpp(0, (int)v, CTRL, 0xf, 0xf, true); }
__device__ __forceinline__ bf8 tr_pair(char* lds, u32 a0, u32 a1) {
  const s4 b0 = __builtin_amdgcn_ds_read_tr16_b64_v4i16((LAS s4*)(lds + a0));
  const s4 b1 = __builtin_amdgcn_ds_read_tr16_b64_v4i16((LAS s4*)(lds + a1));
  const s8 bb = {b0[0], b0[1], b0[2], b0[3], b1[0], b1[1], b1[2], b1[3]};
  return __builtin_bit_cast(bf8, bb);
}
__device__ __forceinline__ v4u lds16(char* lds, u32 a) { return *reinterpret_cast<const v4u*>(lds + a); }
__device__ __forceinline__ v4f lds16f(char* lds, u32 a) { return *reinterpret_cast<const v4f*>(lds + a); }
__device__ __forceinline__ float fdiv(float a, float d, float r) {
  const float q = a * r;
  return fmaf(fmaf(-q, d, a), r, q);
}
__device__ __forceinline__ v4u frag(v4u v, int pa) {
  v4u o;
#pragma unroll
  for (int e = 0; e < 4; ++e) o[e] = (u32)__builtin_amdgcn_ds_bpermute(pa, (int)v[e]);
  return o;
}
__device__ __forceinline__ void gemv448(char* lds, u32 wt, int lane, float& a0, float& a1) {
  const u32 gq = lane >> 4, qq = (lane & 15) >> 2, pp = lane & 3;
  const u32 ao = L_XN + (u32)(lane & 1) * 896u + gq * 16u;
  const u32 bo = wt + (8u * gq + qq) * 32u + pp * 8u;
  v4f acc0 = {0.f, 0.f, 0.f, 0.f}, acc1 = {0.f, 0.f, 0.f, 0.f};
#pragma unroll
  for (int s = 0; s < 7; ++s) {
    acc0 = __builtin_amdgcn_mfma_f32_16x16x32_bf16(as_bf8(lds16(lds, ao + s * 64)), tr_pair(lds, bo + s * 1024, bo + s * 1024 + 128), acc0, 0, 0, 0);
    acc1 = __builtin_amdgcn_mfma_f32_16x16x32_bf16(as_bf8(lds16(lds, ao + (s + 7) * 64)), tr_pair(lds, bo + (s + 7) * 1024, bo + (s + 7) * 1024 + 128), acc1, 0, 0, 0);
  }
  a0 = acc0[0] + acc1[0]; a1 = acc0[1] + acc1[1];
}

__device__ __forceinline__ void fl_write(u32 a, u32 v) { asm volatile("ds_write_b32 %0, %1" :: "v"(a), "v"(v) : "memory"); }
__device__ __forceinline__ void fl_add(u32 a, int lane) { if (lane == 0) asm volatile("ds_add_u32 %0, %1" :: "v"(a), "v"(1u) : "memory"); }
__device__ __forceinline__ int fl_read(u32 a) {
  u32 v; asm volatile("ds_read_b32 %0, %1\n s_waitcnt lgkmcnt(0)" : "=v"(v) : "v"(a) : "memory");
  return (int)__builtin_amdgcn_readfirstlane(v);
}
__device__ __forceinline__ void fl_wait(u32 a, int v) {
#pragma unroll 1
  for (u32 n = 0; n < LSPIN; ++n) { if (fl_read(a) >= v) break; __builtin_amdgcn_s_sleep(0); }
}
__device__ __forceinline__ void sbar(u32 a, int lane) { fl_add(a, lane); fl_wait(a, 8); }
template <int K> __device__ __forceinline__ void drain(u32 fp, int q, int lo) {
  asm volatile("s_waitcnt vmcnt(%0)" :: "n"(K) : "memory");
  fl_write(fp, (u32)(q - K > lo ? q - K : lo));
  if constexpr (K > 0) drain<K - 1>(fp, q, lo);
}
__device__ __forceinline__ void gate(u32 fp, int q, u32 a, int v, int& lo) {
  if (fl_read(a) >= v) return;
  if (lo < q) drain<WMAX - 2>(fp, q, lo);
  lo = q;
  fl_wait(a, v);
}
template <int N, int U, int XM, int SEG, int AUX, int W>
__device__ __forceinline__ void stream(char* lds, u32 lb, const void* base, u32 bytes, u32 vb, u32 sb, u32 ss, u32 ld, int q0, int& lo) {
  const __amdgpu_buffer_rsrc_t rs = __builtin_amdgcn_make_buffer_rsrc((void*)base, 0, bytes, 0x00020000);
#pragma unroll 1
  for (u32 k = 0; k < (u32)(N / U); ++k) {
    const int qk = q0 + (int)(k * U);
    if (SEG == 2) gate(lb + F_PROG, qk, lb + F_PULL + k * 4u, 1, lo);
    const u32 sk = SEG == 1 ? sb + (k < 3u ? k + 1u : k == 3u ? 0u : k) * 256u : sb + k * (U * ss);
#pragma unroll
    for (u32 n = 0; n < (u32)U; ++n) {
      const int q = qk + (int)n;
      asm volatile("s_waitcnt vmcnt(%0)" :: "n"(W - 1) : "memory");
      lo = q - (W - 1) > lo ? q - (W - 1) : lo;
      fl_write(lb + F_PROG, (u32)lo);
      const u32 xm = XM == 1 ? (n & 3u) << 4 : XM == 2 ? ((2u * n) & 15u) << 4 : XM == 3 ? (((n & 1u) << 2) | (((n >> 2) & 1u) << 3)) << 4 : 0u;
      if (XM == 4) {
        const u32 c = vb + (k * U + n) * 64u, rr = (c * 2341u) >> 16;
        __builtin_amdgcn_raw_ptr_buffer_load_lds(rs, (LAS void*)(lds + ld + k * (U * 1024u) + n * 1024u), 16, c * 16u + rr * 448u, sb, 0, AUX);
      } else
      __builtin_amdgcn_raw_ptr_buffer_load_lds(rs, (LAS void*)(lds + ld + k * (U * 1024u) + n * 1024u), 16, vb ^ xm, sk + n * ss, 0, AUX);
    }
  }
}

__global__ __launch_bounds__(576) void mla_decode(const u16* x, const u16* ckv, const u16* wq, const u16* wuk,
                                                  const u16* wuv, const u16* wo, const u16* w1, const u16* b1,
                                                  const u16* w2, const u16* b2, char* ws, u16* out) {
  __shared__ __attribute__((aligned(16))) char lds[L_END];
  const int t = threadIdx.x, lane = t & 63;
  const int w = __builtin_amdgcn_readfirstlane(t >> 6);
  const int i = blockIdx.x, xcd = i & 7, r = i >> 3;
  const int h1i = xcd * 2 + (r >> 4), cs = r & 15;
  const int G = xcd * 4 + (r >> 3), b = G >> 4, g = G & 15, j = r & 7;
  const int ch = r & 1, hd = xcd * 16 + (r >> 1), ds = hd & 7;
  const int fsl = hd < 112 ? hd : 0;
  STAMP_DECL
  STAMP(0);
  asm volatile("" : "+s"(x), "+s"(ckv), "+s"(wq), "+s"(wuk), "+s"(wuv), "+s"(wo), "+s"(w1), "+s"(b1), "+s"(w2), "+s"(b2), "+s"(ws), "+s"(out));
  gu32* tmo = (gu32*)(ws + WS_EP + 128);
  const __amdgpu_buffer_rsrc_t rw = __builtin_amdgcn_make_buffer_rsrc(ws, 0, WS_END, 0x00020000);
  const u32 gq = lane >> 4, qq = (lane & 15) >> 2, pp = lane & 3;

  const u32 lb = (u32)(uintptr_t)(LAS char*)lds;

  if (w == 8) {
    __builtin_amdgcn_s_setprio(SPRIO);
    if (lane < 32) fl_write(lb + L_FLAG + lane * 4, 0u);
    BAR();
    int lo = 0;
    const u32 hb = (u32)lane >> 5;
    const u32 vk = gq * 2048u + (((u32)lane & 15u) ^ (gq << 2)) * 16u;
    stream<14, 7, 0, 0, 0, WIN>(lds, lb, wq, M_ * 8192u, (u32)(lane >> 3) * 8192u + (((u32)lane & 7u) ^ ((((u32)lane >> 3) & 3u) << 1)) * 16u, (u32)(cs & 3) * (112u * 8192u) + (u32)h1i * 512u + (u32)(cs >> 2) * 128u, 8u * 8192u, L_WQ, 0, lo);
    stream<32, 8, 2, 0, 2, WIN>(lds, lb, wuk, 1024u * 8192u, hb * 8192u + (((u32)lane & 31u) ^ hb) * 16u, (u32)(cs * 64) * 8192u + (u32)h1i * 512u, 16384u, L_UV, P_UK - 32, lo);
    STAMP(24);
    stream<64, 8, 1, 1, 0, WIN>(lds, lb, ckv, 2u * SK * DC * 2u, vk, (u32)(b * SK + g * 256 + j * 32) * 2048u, 8192u, L_RA, P_UK, lo);
    STAMP(25);
    if (GE1) gate(lb + F_PROG, P_ROWS, lb + F_E1, GE1, lo);
    stream<64, 8, 1, 2, AUX_COL, WINC>(lds, lb, ckv, 2u * SK * DC * 2u, vk, (u32)(b * SK + g * 256) * 2048u + (u32)j * 256u, 8192u, L_RA, P_ROWS, lo);
    STAMP(26);
    if (GE2) gate(lb + F_PROG, P_COL, lb + F_E2, GE2, lo);
#define S_W1 stream<14, 7, 0, 0, 0, WIN2>(lds, lb, w1, M_ * F_ * 2u, (u32)(lane >> 1) * (F_ * 2u) + (u32)(lane & 1) * 16u, (u32)fsl * 32u, 32u * F_ * 2u, L_W1, P_W1 - 14, lo);
    if (LORD == 1) { S_W1 }
    gate(lb + F_PROG, P_UV - 32, lb + F_UK, 4, lo);
    stream<32, 8, 3, 0, 2, WIN2>(lds, lb, wuv, 1024u * 8192u, hb * 8192u + (((u32)lane & 31u) ^ (hb << 1)) * 16u, (u32)(cs * 64) * 8192u + (u32)h1i * 512u, 16384u, L_UV, P_UV - 32, lo);
    if (LORD == 0) { S_W1 }
    STAMP(27);
    if (GE3) gate(lb + F_PROG, LORD == 2 ? P_UV : 220, lb + F_E3, GE3, lo);
    if (LORD == 2) { S_W1 }
    stream<7, 7, 4, 0, 2, WIN2>(lds, lb, w2, 1792u * 896u, (u32)lane, (u32)(fsl * 16) * 896u + (u32)ch * 448u, 0u, L_W2, P_W2 - 7, lo);
    gate(lb + F_PROG, P_W2, lb + F_RO, 4, lo);
    stream<14, 7, 4, 0, 2, WIN2>(lds, lb, wo, 4096u * 896u, (u32)lane, (u32)(hd * 32) * 896u + (u32)ch * 448u, 0u, L_WO, P_W2, lo);
    STAMP(28);
    {
      asm volatile("s_waitcnt vmcnt(%0)" :: "n"(P_END - P_W1) : "memory");
      float a0, a1;
      gemv448(lds, L_W1, lane, a0, a1);
      const float hv = rbf(rbf(lane >= 32 ? a1 : a0) + *reinterpret_cast<const float*>(lds + L_BI + (lane & 15) * 4));
      const float ge = 0.5f * hv * (1.f + tanhf(0.7978845608028654f * (hv + 0.044715f * hv * hv * hv)));
      *reinterpret_cast<u16*>(lds + L_VH + lane * 2) = ((lane & 16) == 0 && hd < 112) ? f2bf(ge) : (u16)0;
      asm volatile("s_waitcnt lgkmcnt(0)" ::: "memory");
    }
    drain<WMAX - 1>(lb + F_PROG, P_END, lo);
    STAMP(29);
    STAMP_FLUSH;
    return;
  }

  __builtin_amdgcn_s_setprio(CPRIO);
  const u32 epv = __builtin_amdgcn_raw_buffer_load_b32(rw, WS_EP, 0, 0);
  const int pa = (4 * (lane & 15) + (lane >> 4)) * 4;
  u32 tag, pre0 = 0;
  if (w == 0) {
    const int lc = lane < 56 ? lane : 0;
    const v4u xr0 = ldp16(x + lc * 8), xr1 = ldp16(x + M_ + lc * 8);
    STAMP(21);
    BAR();
    float xa[8], xb[8];
#pragma unroll
    for (int p = 0; p < 4; ++p) { xa[2 * p] = lo_f(xr0[p]); xa[2 * p + 1] = hi_f(xr0[p]); xb[2 * p] = lo_f(xr1[p]); xb[2 * p + 1] = hi_f(xr1[p]); }
    const float act = lane < 56 ? 1.f : 0.f;
    float s0 = 0.f, s1 = 0.f;
#pragma unroll
    for (int k = 0; k < 8; ++k) { s0 += xa[k]; s1 += xb[k]; }
    s0 = wave_sum(s0 * act); s1 = wave_sum(s1 * act);
    const float m0 = s0 * (1.f / M_), m1 = s1 * (1.f / M_);
    float v0 = 0.f, v1 = 0.f;
#pragma unroll
    for (int k = 0; k < 8; ++k) { const float d0 = xa[k] - m0, d1 = xb[k] - m1; v0 += d0 * d0; v1 += d1 * d1; }
    v0 = wave_sum(v0 * act); v1 = wave_sum(v1 * act);
    const float mb0 = rbf(m0), mb1 = rbf(m1);
    const float den0 = rbf(sqrtf(rbf(rbf(v0 * (1.f / M_)) + 1e-5f)));
    const float den1 = rbf(sqrtf(rbf(rbf(v1 * (1.f / M_)) + 1e-5f)));
    const float r0 = __builtin_amdgcn_rcpf(den0), r1 = __builtin_amdgcn_rcpf(den1);
    v4u na, nb;
#pragma unroll
    for (int p = 0; p < 4; ++p) {
      na[p] = pk2(fdiv(rbf(xa[2 * p] - mb0), den0, r0), fdiv(rbf(xa[2 * p + 1] - mb0), den0, r0));
      nb[p] = pk2(fdiv(rbf(xb[2 * p] - mb1), den1, r1), fdiv(rbf(xb[2 * p + 1] - mb1), den1, r1));
    }
    if (lane < 56) {
      *reinterpret_cast<v4u*>(lds + L_XN + lane * 16) = na;
      *reinterpret_cast<v4u*>(lds + L_XN + 896 + lane * 16) = nb;
    }
    fl_add(lb + F_XN, lane);
    STAMP(17);
  } else if (w < 4) {
    float bias = 0.f;
    if (w == 1) bias = bf2f(ldh(b1 + fsl * 16 + (lane & 15)));
    if (w == 3) pre0 = __builtin_amdgcn_raw_buffer_load_b32(rw, lane < 8 ? WS_QL + (u32)i * 512u + (u32)lane * 64u : WS_QG + (u32)i * 1024u + (u32)(lane & 15) * 64u, 0, PRE_AUX);
    BAR();
    if (w == 1 && lane < 16) *reinterpret_cast<float*>(lds + L_BI + lane * 4) = bias;
    fl_wait(lb + F_XN, 1);
  }
  if (w < 4) {
    fl_wait(lb + F_PROG, P_WQ);
    STAMP(18);
    const u32 ao = L_XN + (u32)(lane & 1) * 896u + (u32)(cs & 3) * 224u;
    const u32 ch = ((((u32)w * 2u + (pp >> 1)) ^ (qq << 1)) * 16u) + (pp & 1u) * 8u;
    const u32 bo = L_WQ + (8u * gq + qq) * 128u + ch;
    v4f acc0 = {0.f, 0.f, 0.f, 0.f}, acc1 = {0.f, 0.f, 0.f, 0.f};
    acc0 = __builtin_amdgcn_mfma_f32_16x16x32_bf16(as_bf8(lds16(lds, ao + gq * 16u)), tr_pair(lds, bo, bo + 512), acc0, 0, 0, 0);
    acc1 = __builtin_amdgcn_mfma_f32_16x16x32_bf16(as_bf8(lds16(lds, ao + gq * 16u + 64)), tr_pair(lds, bo + 4096, bo + 4096 + 512), acc1, 0, 0, 0);
    acc0 = __builtin_amdgcn_mfma_f32_16x16x32_bf16(as_bf8(lds16(lds, ao + gq * 16u + 128)), tr_pair(lds, bo + 8192, bo + 8192 + 512), acc0, 0, 0, 0);
    acc1 = __builtin_amdgcn_mfma_f32_16x16x16bf16_1k(*reinterpret_cast<const s4*>(lds + ao + 192u + gq * 8u),
                                                     __builtin_amdgcn_ds_read_tr16_b64_v4i16((LAS s4*)(lds + L_WQ + (96u + 4u * gq + qq) * 128u + ch)), acc1, 0, 0, 0);
    tag = __builtin_amdgcn_readfirstlane(epv) + 1u;
    {
      const u32 qv = __float_as_uint(gq ? acc0[1] + acc1[1] : acc0[0] + acc1[0]), qn = dppu<0xB1>(qv);
      if ((lane & 33) == 0) ST2(WS_QG + (u32)h1i * 16384u + (u32)(cs & 3) * 4096u + (u32)(cs >> 2) * 1024u + (u32)w * 256u + (u32)lane * 8u, qv, qn);
    }
    STAMP(1);
    fl_add(lb + F_RO, lane);
    asm volatile("" :: "v"(pre0));
    if (PRE_SC == 0 && w == 3) pre0 = PRE_RD(lane < 32 ? WS_SC + (u32)i * 4096u + (u32)lane * 128u : WS_ML + (u32)(i & 63) * 128u);
  } else {
    v4u wk[8];
    BAR();
    tag = __builtin_amdgcn_readfirstlane(epv) + 1u;
    fl_wait(lb + F_RO, QGATE);
    if (QDLY) __builtin_amdgcn_s_sleep(QDLY);
    STAMP(19);
    const u32 qo = WS_QG + (u32)h1i * 16384u + (u32)(w - 4) * 1024u + (u32)lane * 16u;
    v4u ga[4];
    POLLN(ga, 4, qo, 4096u, 1u, 0u)
    {
      const float q0 = (__uint_as_float(ga[0][0]) + __uint_as_float(ga[1][0])) + (__uint_as_float(ga[2][0]) + __uint_as_float(ga[3][0]));
      const float q1 = (__uint_as_float(ga[0][2]) + __uint_as_float(ga[1][2])) + (__uint_as_float(ga[2][2]) + __uint_as_float(ga[3][2]));
      *reinterpret_cast<u32*>(lds + L_QST + (u32)((lane >> 3) & 1) * 512u + (u32)(w - 4) * 128u + gq * 32u + (u32)(lane & 7) * 4u) = pk2(q0, q1);
    }
    fl_add(lb + F_QS, lane);
    STAMP(20);
    fl_wait(lb + F_PROG, P_UK);
    {
      const u32 ko = L_UV + (u32)((w - 4) * 16 + (lane & 15)) * 512u;
#pragma unroll
      for (int s = 0; s < 8; ++s) wk[s] = lds16(lds, ko + ((((u32)s * 4u + gq) ^ ((u32)lane & 15u)) * 16u));
    }
    fl_add(lb + F_UK, lane);
    fl_wait(lb + F_QS, 4);
    const u32 ao = L_QST + (u32)(lane & 1) * 512u + gq * 16u;
    v4f acc0 = {0.f, 0.f, 0.f, 0.f}, acc1 = {0.f, 0.f, 0.f, 0.f};
#pragma unroll
    for (int s = 0; s < 4; ++s) {
      acc0 = __builtin_amdgcn_mfma_f32_16x16x32_bf16(as_bf8(lds16(lds, ao + s * 64)), as_bf8(wk[s]), acc0, 0, 0, 0);
      acc1 = __builtin_amdgcn_mfma_f32_16x16x32_bf16(as_bf8(lds16(lds, ao + (s + 4) * 64)), as_bf8(wk[s + 4]), acc1, 0, 0, 0);
    }
    const float a0 = acc0[0] + acc1[0], a1 = acc0[1] + acc1[1];
    const float o0 = dppf<0xB1>(a0), o1 = dppf<0xB1>(a1);
    {
      const int bb = lane & 1, wq4 = w - 4;
      const u32 qa = (u32)bb * 65536u + (u32)((cs * 2 + (wq4 >> 1)) * 2 + ((lane >> 2) & 1)) * 1024u + (u32)h1i * 64u
                     + (u32)((wq4 & 1) * 2 + ((lane >> 3) & 1)) * 16u;
      const u32 qv = bb ? pk2(o1, a1) : pk2(a0, o0), qn = dppu<0x4E>(qv);
      if ((lane & 50) == 0) ST2(WS_QL + qa, qv, qn);
    }
    STAMP(2);
    if (BDLY) __builtin_amdgcn_s_sleep(BDLY);
    fl_add(lb + F_QP, lane);
    if (PRE_SC == 1 && w == 7) pre0 = PRE_RD(lane < 32 ? WS_SC + (u32)i * 4096u + (u32)lane * 128u : WS_ML + (u32)(i & 63) * 128u);
  }
  v4u bv[8];
  const int slot = w == 0 ? 3 : w < 4 ? w - 1 : w;
  {
    fl_wait(lb + F_PROG, P_UK + 8 * (slot + 1));
    const u32 key = (u32)lane & 15u;
    const u32 ro = L_RA + (u32)slot * 8192u + key * 256u;
#pragma unroll
    for (int s = 0; s < 8; ++s) bv[s] = lds16(lds, ro + (u32)(s >> 2) * 4096u + ((((u32)(s & 3) * 4u + gq) ^ swz(key)) * 16u));
    fl_add(lb + F_PULL + (u32)slot * 4u, lane);
  }
  STAMP(3);
  {
    const u32 qo = WS_QL + (u32)b * 65536u + (u32)w * 8192u + (u32)lane * 16u;
    v4u gl[8];
    fl_wait(lb + F_QP, 4);
    POLLN(gl, 8, qo, 1024u, 2u, F_E1)
    asm volatile("" :: "v"(pre0));
    STAMP(4);
    v4f acc0 = {0.f, 0.f, 0.f, 0.f}, acc1 = {0.f, 0.f, 0.f, 0.f};
#pragma unroll
    for (int s = 0; s < 4; ++s) {
      const v4u av = frag(v4u{gl[2 * s][0], gl[2 * s][2], gl[2 * s + 1][0], gl[2 * s + 1][2]}, pa);
      acc0 = __builtin_amdgcn_mfma_f32_16x16x32_bf16(as_bf8(av), as_bf8(bv[s]), acc0, 0, 0, 0);
      acc1 = __builtin_amdgcn_mfma_f32_16x16x32_bf16(as_bf8(av), as_bf8(bv[4 + s]), acc1, 0, 0, 0);
    }
    float* sr = reinterpret_cast<float*>(lds + L_SRED) + (w * 16 + gq * 4) * 33 + (lane & 15);
#pragma unroll
    for (int q = 0; q < 4; ++q) { sr[q * 33] = acc0[q]; sr[q * 33 + 16] = acc1[q]; }
  }
  sbar(lb + F_B, lane);
  STAMP(5);
  {
    const int hq = t >> 5, key = t & 31;
    const float* sr = reinterpret_cast<const float*>(lds + L_SRED) + hq * 33 + key;
    float s = 0.f;
#pragma unroll
    for (int ww = 0; ww < 8; ++ww) s += sr[ww * 16 * 33];
    s *= 0.0625f;
    const u32 sn = dppu<0xB1>(__float_as_uint(s));
    if ((lane & 1) == 0) ST2(WS_SC + (u32)((G * 16 + hq) * 256 + j * 32 + key) * 8u, __float_as_uint(s), sn);
    fl_add(lb + F_SR, lane);
    if (w == 3) pre0 = PRE_RD(WS_PV + (u32)i * 8192u + (u32)lane * 128u);
  }
  {
    float ml[4];
    const u32 so = WS_SC + (u32)((G * 16 + 2 * w) * 256 + 2 * lane) * 8u;
    v4u gs[4];
    if (CDLY) __builtin_amdgcn_s_sleep(CDLY);
    POLLN(gs, 4, so, 1024u, 3u, F_E2)
    asm volatile("" :: "v"(pre0));
    STAMP(6);
    fl_wait(lb + F_SR, 8);
#pragma unroll
    for (int hh = 0; hh < 2; ++hh) {
      const float sv[4] = {__uint_as_float(gs[2 * hh][0]), __uint_as_float(gs[2 * hh][2]), __uint_as_float(gs[2 * hh + 1][0]), __uint_as_float(gs[2 * hh + 1][2])};
      const float m = wave_max(fmaxf(fmaxf(sv[0], sv[1]), fmaxf(sv[2], sv[3])));
      const u32 p0 = pk2(__expf(sv[0] - m), __expf(sv[1] - m)), p1 = pk2(__expf(sv[2] - m), __expf(sv[3] - m));
      const float l = wave_sum((lo_f(p0) + hi_f(p0)) + (lo_f(p1) + hi_f(p1)));
      *reinterpret_cast<u32*>(lds + L_P + (2 * w + hh) * 528 + lane * 4) = p0;
      *reinterpret_cast<u32*>(lds + L_P + (2 * w + hh) * 528 + 256 + lane * 4) = p1;
      ml[2 * hh] = m; ml[2 * hh + 1] = l;
    }
    if (j == 0 && lane < 2)
      ST2(WS_ML + (u32)((b * 16 + 2 * w + lane) * 16 + g) * 16u, __float_as_uint(lane ? ml[2] : ml[0]), __float_as_uint(lane ? ml[3] : ml[1]));
  }
  sbar(lb + F_B + 4, lane);
  STAMP(22);
  fl_wait(lb + F_PROG, P_COL);
  STAMP(7);
  {
    v4f acc0 = {0.f, 0.f, 0.f, 0.f}, acc1 = {0.f, 0.f, 0.f, 0.f};
    const u32 po = L_P + (u32)(lane & 15) * 528u + gq * 16u;
    const u32 k0 = L_RA + off_b(8 * gq + qq, 2 * w + (pp >> 1)) + 8 * (pp & 1);
    const u32 k1 = L_RA + off_b(8 * gq + 4 + qq, 2 * w + (pp >> 1)) + 8 * (pp & 1);
#pragma unroll
    for (int ks = 0; ks < 4; ++ks) {
      acc0 = __builtin_amdgcn_mfma_f32_16x16x32_bf16(as_bf8(lds16(lds, po + ks * 64)), tr_pair(lds, k0 + ks * 8192, k1 + ks * 8192), acc0, 0, 0, 0);
      acc1 = __builtin_amdgcn_mfma_f32_16x16x32_bf16(as_bf8(lds16(lds, po + (ks + 4) * 64)), tr_pair(lds, k0 + (ks + 4) * 8192, k1 + (ks + 4) * 8192), acc1, 0, 0, 0);
    }
    const v4f acc = acc0 + acc1;
    const int hi8 = (lane >> 3) & 1;
    const float s0 = hi8 ? acc[0] : acc[2], s1 = hi8 ? acc[1] : acc[3];
    const float n0 = dppf<0x128>(s0), n1 = dppf<0x128>(s1);
    const u32 g0 = hi8 ? pk2(n0, acc[2]) : pk2(acc[0], n0);
    const u32 g1 = hi8 ? pk2(n1, acc[3]) : pk2(acc[1], n1);
    const int hq = (lane >> 4) * 4 + 2 * hi8;
    const int od = lane & 1;
    const u32 go = WS_PV + (u32)(((b * 16 + hq + od) * 16 + g) * 512 + j * 64 + w * 8 + (lane & 6)) * 8u;
    const u32 gn = dppu<0xB1>(od ? g0 : g1);
    ST2(go, od ? gn : g0, od ? g1 : gn);
  }
  if (w == 3) pre0 = PRE_RD(lane < 32 ? WS_CP + (u32)i * 4096u + (u32)lane * 128u : WS_OG + (u32)i * 3584u + (u32)(lane < 60 ? lane - 32 : 0) * 128u);
  STAMP(8);
  {
    const int bq = w >> 2;
    const u32 gg = (u32)(w & 3) * 4u + gq;
    const u32 po = WS_PV + (((u32)(bq * 16 + h1i) * 16u + gg) * 512u + (u32)cs * 32u + (u32)(lane & 15) * 2u) * 8u;
    gu64* mlp = (gu64*)(ws + WS_ML) + (bq * 16 + h1i) * 32 + (lane & 31);
    v4u pv;
    u64 mlv;
    if (DDLY) __builtin_amdgcn_s_sleep(DDLY);
    __builtin_amdgcn_s_setprio(0);
#pragma unroll 1
    for (u32 spins = 0, pend = 3u;;) {
      if (pend & 1u) mlv = __hip_atomic_load(mlp, RLX_AGENT);
      if (pend & 2u) pv = __builtin_amdgcn_raw_buffer_load_b128(rw, po, 0, 16);
      if (spins == 0) fl_add(lb + F_E3, lane);
      if (__all((u32)(mlv >> 32) == tag)) pend &= ~1u;
      if (__all((pv[1] == tag) & (pv[3] == tag))) pend &= ~2u;
      if (!pend) break;
      SPIN_FAIL(4u)
    }
    __builtin_amdgcn_s_setprio(CPRIO);
    const float mv = __uint_as_float((u32)mlv);
    const float other = dppf<0xB1>(mv);
    const float mg = (lane & 1) ? other : mv, lg = (lane & 1) ? mv : other;
    const float Mx = wave_max(mg);
    const float wg = __expf(mg - Mx);
    const float Ls = wave_sum(wg * lg) * 0.25f;
    const float wn = wg / Ls;
    const float wv = __int_as_float(__builtin_amdgcn_ds_bpermute((int)gg * 8, __float_as_int(wn)));
    asm volatile("" :: "v"(pre0));
    STAMP(9);
    float a[4] = {wv * lo_f(pv[0]), wv * lo_f(pv[2]), wv * hi_f(pv[0]), wv * hi_f(pv[2])};
#pragma unroll
    for (int q = 0; q < 4; ++q) a[q] = x32_add(x16_add(a[q]));
    if (lane < 16) {
      float* dp = reinterpret_cast<float*>(lds + L_DP) + w * 64 + (lane >> 2) * 16 + (lane & 3) * 2;
      dp[0] = a[0]; dp[1] = a[1]; dp[8] = a[2]; dp[9] = a[3];
    }
  }
  sbar(lb + F_B + 8, lane);
  STAMP(23);
  fl_wait(lb + F_PROG, P_UV);
  STAMP(10);
  {
    v4f acc0 = {0.f, 0.f, 0.f, 0.f}, acc1 = {0.f, 0.f, 0.f, 0.f};
    const u32 dpo = L_DP + (u32)((lane >> 2) & 1) * 1024u + gq * 32u;
    const u32 rr = 8u * gq + qq;
    const u32 f = (qq << 1) | ((gq & 1u) << 3);
    const u32 bo0 = L_UV + rr * 512u + ((((u32)w * 4u + (pp >> 1)) ^ f) * 16u) + (pp & 1u) * 8u;
    const u32 bo1 = L_UV + rr * 512u + ((((u32)w * 4u + 2u + (pp >> 1)) ^ f) * 16u) + (pp & 1u) * 8u;
#pragma unroll
    for (int s = 0; s < 2; ++s) {
      v4f c0 = {0.f, 0.f, 0.f, 0.f}, c1 = {0.f, 0.f, 0.f, 0.f};
#pragma unroll
      for (int ww = 0; ww < 4; ++ww) { c0 += lds16f(lds, dpo + ww * 256 + s * 128); c1 += lds16f(lds, dpo + ww * 256 + s * 128 + 16); }
      v4u av = {pk2(c0[0], c0[1]), pk2(c0[2], c0[3]), pk2(c1[0], c1[1]), pk2(c1[2], c1[3])};
      if (lane & 1) {
        av = v4u{pk2(c0[0] - lo_f(av[0]), c0[1] - hi_f(av[0])), pk2(c0[2] - lo_f(av[1]), c0[3] - hi_f(av[1])),
                 pk2(c1[0] - lo_f(av[2]), c1[1] - hi_f(av[2])), pk2(c1[2] - lo_f(av[3]), c1[3] - hi_f(av[3]))};
      }
      acc0 = __builtin_amdgcn_mfma_f32_16x16x32_bf16(as_bf8(av), tr_pair(lds, bo0 + s * 16384, bo0 + s * 16384 + 2048), acc0, 0, 0, 0);
      acc1 = __builtin_amdgcn_mfma_f32_16x16x32_bf16(as_bf8(av), tr_pair(lds, bo1 + s * 16384, bo1 + s * 16384 + 2048), acc1, 0, 0, 0);
    }
    {
      const int od = lane & 1;
      const u32 co = WS_CP + ((((u32)(h1i * 2) + (gq & 1u)) * 8u + (u32)w) * 16u + (u32)cs) * 256u + (u32)od * 128u + (u32)(lane & 14) * 8u;
      const u32 ca = __float_as_uint(acc0[0] + acc0[1]), cb = __float_as_uint(acc1[0] + acc1[1]);
      const u32 cn = dppu<0xB1>(od ? ca : cb);
      if (lane < 32) ST2(co, od ? cn : ca, od ? cb : cn);
    }
  }
  STAMP(11);
  {
    const u32 co = WS_CP + (u32)((h1i * 2 + (w >> 2)) * 8 + ds) * 4096u + (u32)(t & 255) * 16u;
    v4u cv;
    if (FDLY) __builtin_amdgcn_s_sleep(FDLY);
    POLL1(cv, co, 6u)
    const float c0 = x32_add(x16_add(__uint_as_float(cv[0]))), c1 = x32_add(x16_add(__uint_as_float(cv[2])));
    if (lane < 16) *reinterpret_cast<v2u*>(lds + L_CR + w * 128 + lane * 8) = v2u{__float_as_uint(c0), __float_as_uint(c1)};
  }
  STAMP(15);
  sbar(lb + F_B + 12, lane);
  fl_wait(lb + F_PROG, P_END);
  STAMP(16);
  if (w < 7) {
    const float* cr = reinterpret_cast<const float*>(lds + L_CR) + (lane >> 5) * 128 + (lane & 31);
    const float cv = (cr[0] + cr[32]) + (cr[64] + cr[96]);
    *reinterpret_cast<u16*>(lds + L_VST + w * 128 + lane * 2) = f2bf(cv);
    const bf8 a0 = as_bf8(lds16(lds, L_VST + w * 128 + (lane & 1) * 64 + gq * 16)), a1 = as_bf8(lds16(lds, L_VH + (lane & 1) * 64 + gq * 16));
    const u32 bo0 = L_WO + (8u * gq + qq) * 448u + (u32)w * 64u + pp * 8u;
    const u32 bo1 = L_W2 + ((8u * gq + qq) & 15u) * 448u + (u32)w * 64u + pp * 8u;
    float ov = 0.f;
#pragma unroll
    for (int n = 0; n < 2; ++n) {
      v4f acc = {0.f, 0.f, 0.f, 0.f};
      acc = __builtin_amdgcn_mfma_f32_16x16x32_bf16(a0, tr_pair(lds, bo0 + n * 32, bo0 + n * 32 + 1792), acc, 0, 0, 0);
      acc = __builtin_amdgcn_mfma_f32_16x16x32_bf16(a1, tr_pair(lds, bo1 + n * 32, bo1 + n * 32 + 1792), acc, 0, 0, 0);
      if ((gq & 1u) == (u32)n) ov = gq >> 1 ? acc[1] : acc[0];
    }
    const u32 on = dppu<0xB1>(__float_as_uint(ov));
    if ((lane & 1) == 0) ST2(WS_OG + (u32)(((lane >> 5) * 128 + hd) * M_ + ch * 224 + w * 32 + (lane & 31)) * 8u, __float_as_uint(ov), on);
  }
  STAMP(12);
  if (i < 112) {
    const int fb = i / 56, fc = i - fb * 56;
    const u32 oo = WS_OG + ((u32)(fb * 128 + (t >> 2)) * M_ + (u32)(fc * 8 + (t & 3) * 2)) * 8u;
    float xr = 0.f;
    if (t < 8) xr = bf2f(ldh(x + fb * M_ + fc * 8 + t)) + 0.f;
    float br = 0.f;
    if (t < 8) br = bf2f(ldh(b2 + fc * 8 + t));
    v4u ov;
    if (EDLY) __builtin_amdgcn_s_sleep(EDLY);
    POLL1(ov, oo, 5u)
    float c0 = __uint_as_float(ov[0]), c1 = __uint_as_float(ov[2]);
    c0 = ror_add<4>(c0); c1 = ror_add<4>(c1);
    c0 = ror_add<8>(c0); c1 = ror_add<8>(c1);
    c0 = x32_add(x16_add(c0)); c1 = x32_add(x16_add(c1));
    if (lane < 4) *reinterpret_cast<v2u*>(lds + L_FR + w * 32 + lane * 8) = v2u{__float_as_uint(c0), __float_as_uint(c1)};
    sbar(lb + F_B + 16, lane);
    STAMP(13);
    if (t < 8) {
      float a = 0.f;
#pragma unroll
      for (int q = 0; q < 8; ++q) a += reinterpret_cast<const float*>(lds + L_FR)[q * 8 + t];
      *(GAS u16*)(out + fb * M_ + fc * 8 + t) = f2bf(xr + a + br);
    }
    if (i == 0 && t == 0) __hip_atomic_store((gu32*)(ws + WS_EP), tag, RLX_AGENT);
    STAMP(14);
  }
  STAMP_FLUSH;
}

extern "C" void kernel_launch(void* const* d_in, const int* in_sizes, int n_in,
                              void* d_out, int out_size, void* d_ws, size_t ws_size,
                              hipStream_t stream) {
  const u16* x = (const u16*)d_in[0];
  const u16* ckv = (const u16*)d_in[1];
  const u16* wq = (const u16*)d_in[2];
  const u16* wuk = (const u16*)d_in[3];
  const u16* wuv = (const u16*)d_in[4];
  const u16* wo = (const u16*)d_in[5];
  const u16* w1 = (const u16*)d_in[7];
  const u16* b1 = (const u16*)d_in[8];
  const u16* w2 = (const u16*)d_in[9];
  const u16* b2 = (const u16*)d_in[10];
  mla_decode<<<256, 576, 0, stream>>>(x, ckv, wq, wuk, wuv, wo, w1, b1, w2, b2, (char*)d_ws, (u16*)d_out);
}
```

```cpp
#include <hip/hip_runtime.h>
#include <hip/hip_bf16.h>
#include <stdint.h>

typedef unsigned short u16;
typedef unsigned int u32;
typedef unsigned long long u64;
typedef __attribute__((ext_vector_type(4))) unsigned v4u;
typedef __attribute__((ext_vector_type(2))) unsigned v2u;
typedef __attribute__((ext_vector_type(4))) float v4f;
typedef __attribute__((ext_vector_type(8))) __bf16 bf8;
typedef __attribute__((ext_vector_type(4))) short s4;
typedef __attribute__((ext_vector_type(8))) short s8;

#define GAS __attribute__((address_space(1)))
#define LAS __attribute__((address_space(3)))
typedef GAS u32 gu32;
typedef GAS u64 gu64;
#define RLX_AGENT __ATOMIC_RELAXED, __HIP_MEMORY_SCOPE_AGENT

#define M_ 448
#define F_ 1792
#define DC 1024
#define SK 4096

constexpr u32 SPIN_CAP = 1u << 18;

constexpr u32 WS_EP = 0;
constexpr u32 WS_QL = 65536;
constexpr u32 WS_SC = 262144;
constexpr u32 WS_PV = 1310720;
constexpr u32 WS_ML = 3407872;
constexpr u32 WS_CP = 3416064;
constexpr u32 WS_OG = 4464640;
constexpr u32 WS_QG = WS_OG + 2 * 128 * 448 * 8;
constexpr u32 WS_END = WS_QG + 262144;

#define STAMP(k) do {} while (0)
#define STAMP_DECL
#define STAMP_FLUSH do {} while (0)

#define BAR() do { asm volatile("s_waitcnt lgkmcnt(0)" ::: "memory"); __builtin_amdgcn_s_barrier(); asm volatile("" ::: "memory"); } while (0)

__device__ __forceinline__ float bf2f(u16 v) { return __uint_as_float(((u32)v) << 16); }
__device__ __forceinline__ u16 f2bf(float f) { __hip_bfloat16 b = __float2bfloat16(f); return *reinterpret_cast<u16*>(&b); }
__device__ __forceinline__ float rbf(float f) { return bf2f(f2bf(f)); }
__device__ __forceinline__ float lo_f(u32 v) { return __uint_as_float(v << 16); }
__device__ __forceinline__ float hi_f(u32 v) { return __uint_as_float(v & 0xffff0000u); }
__device__ __forceinline__ u32 pk2(float a, float b) { return (u32)f2bf(a) | ((u32)f2bf(b) << 16); }
__device__ __forceinline__ v4u ld16(const u16* p) { return __builtin_nontemporal_load((const GAS v4u*)p); }
__device__ __forceinline__ v4u ldp16(const u16* p) { return *(const GAS v4u*)p; }
__device__ __forceinline__ u16 ldh(const u16* p) { return *(const GAS u16*)p; }
__device__ __forceinline__ bf8 as_bf8(v4u v) { return __builtin_bit_cast(bf8, v); }

template <int CTRL>
__device__ __forceinline__ float dppf(float v) {
  return __int_as_float(__builtin_amdgcn_update_dpp(0, __float_as_int(v), CTRL, 0xf, 0xf, true));
}
template <int N> __device__ __forceinline__ float ror_add(float v) { return v + dppf<0x120 + N>(v); }
template <int N> __device__ __forceinline__ float ror_max(float v) { return fmaxf(v, dppf<0x120 + N>(v)); }
__device__ __forceinline__ float x16_add(float v) {
  const u32 b = __float_as_uint(v);
  auto p = __builtin_amdgcn_permlane16_swap(b, b, false, false);
  return __uint_as_float(p[0]) + __uint_as_float(p[1]);
}
__device__ __forceinline__ float x32_add(float v) {
  const u32 b = __float_as_uint(v);
  auto p = __builtin_amdgcn_permlane32_swap(b, b, false, false);
  return __uint_as_float(p[0]) + __uint_as_float(p[1]);
}
__device__ __forceinline__ float x16_max(float v) {
  const u32 b = __float_as_uint(v);
  auto p = __builtin_amdgcn_permlane16_swap(b, b, false, false);
  return fmaxf(__uint_as_float(p[0]), __uint_as_float(p[1]));
}
__device__ __forceinline__ float x32_max(float v) {
  const u32 b = __float_as_uint(v);
  auto p = __builtin_amdgcn_permlane32_swap(b, b, false, false);
  return fmaxf(__uint_as_float(p[0]), __uint_as_float(p[1]));
}
__device__ __forceinline__ float wave_sum(float v) {
  v = ror_add<8>(v); v = ror_add<4>(v); v = ror_add<2>(v); v = ror_add<1>(v);
  return x32_add(x16_add(v));
}
__device__ __forceinline__ float wave_max(float v) {
  v = ror_max<8>(v); v = ror_max<4>(v); v = ror_max<2>(v); v = ror_max<1>(v);
  return x32_max(x16_max(v));
}

__device__ __forceinline__ u32 swz(u32 row) { return ((row & 3u) << 2) | ((row >> 2) & 3u); }
__device__ __forceinline__ u32 off_b(u32 row, u32 ch) { return 256u * row + 16u * (ch ^ swz(row)); }

constexpr u32 L_RA = 0;
constexpr u32 L_UV = 65536;
constexpr u32 L_WO = 98304;
constexpr u32 L_WQ = L_WO;
constexpr u32 L_W1 = L_WO + 14336;
constexpr u32 L_W2 = 126976;
constexpr u32 L_XN = 134144;
constexpr u32 L_P = L_XN + 1792;
constexpr u32 L_QST = L_P;
constexpr u32 L_SRED = L_P;
constexpr u32 L_AT = L_P + 16896;
constexpr u32 L_WG = L_AT + 4608;
constexpr u32 L_VST = L_WG + 128;
constexpr u32 L_VH = L_VST + 128;
constexpr u32 L_FR = L_VH + 128;
constexpr u32 L_BI = L_FR + 256;
constexpr u32 L_FLAG = L_BI + 64;
constexpr u32 L_END = L_FLAG + 128;
static_assert(L_END <= 163840, "LDS");
constexpr u32 F_PROG = L_FLAG;
constexpr u32 F_COL = L_FLAG + 4;
constexpr u32 F_UK = L_FLAG + 36;
constexpr u32 F_RO = L_FLAG + 40;
constexpr u32 F_SR = L_FLAG + 44;
constexpr u32 F_B = L_FLAG + 48;
constexpr u32 F_QP = L_FLAG + 68;
constexpr u32 F_XN = L_FLAG + 72;
constexpr u32 F_QS = L_FLAG + 76;
constexpr u32 F_E1 = L_FLAG + 80;
constexpr u32 F_E2 = L_FLAG + 84;
constexpr u32 F_E3 = L_FLAG + 88;
#define SELFPRE 15
#define MLW 5
#define TLBW 0
#define EARLYQ 1
#define GPOS 1
#define GE1 0
#define GE2 8
#define GE3 0
#define LORD 0
constexpr int P_WQ = 14, P_UK = 46, P_ROWS = 110, P_UV = 142, P_W1 = 156, P_W2 = 163, P_WO = 177;
constexpr int P_END = 177;

#define WIN 16
#define WIN2 16
#define WINC 16
constexpr int WMAX = WIN > WIN2 ? (WIN > WINC ? WIN : WINC) : (WIN2 > WINC ? WIN2 : WINC);
#define AUX_COL 2
#define PSLP 2
#define QDLY 12
#define BDLY 16
#define CDLY 14
#define DDLY 12
#define FDLY 16
#define EDLY 0
#define QGATE 4
#define PSTR 128
#define PRE_AUX 0
#define PRE_SC 0
#define POUT 0
#define PRE_RD(off) __builtin_amdgcn_raw_buffer_load_b32(rw, (off), 0, PRE_AUX)
#define PRE 110
#define CPRIO 2
#define SPRIO 3
constexpr u32 LSPIN = 1u << 22;

#define SPIN_FAIL(code) if (++spins > SPIN_CAP) { if (lane == 0) __hip_atomic_store(tmo, (code), RLX_AGENT); break; } __builtin_amdgcn_s_sleep(PSLP); asm volatile("" ::: "memory");

#define POLL1(v, off, code) __builtin_amdgcn_s_setprio(0); _Pragma("unroll 1") for (u32 spins = 0;;) { v = __builtin_amdgcn_raw_buffer_load_b128(rw, (off), 0, 16); if (__all((v[1] == tag) & (v[3] == tag))) break; SPIN_FAIL(code) } __builtin_amdgcn_s_setprio(CPRIO);

#define POLLN(v, N, off, stride, code, ev) { u32 pend = (1u << (N)) - 1u; __builtin_amdgcn_s_setprio(0); _Pragma("unroll 1") for (u32 spins = 0;;) { \
    _Pragma("unroll") for (int s_ = 0; s_ < (N); ++s_) if (pend >> s_ & 1u) v[s_] = __builtin_amdgcn_raw_buffer_load_b128(rw, (off) + s_ * (stride), 0, 16); \
    if ((ev) && spins == 0) fl_add(lb + (ev), lane); \
    _Pragma("unroll") for (int s_ = 0; s_ < (N); ++s_) if (pend >> s_ & 1u) { if (__all((v[s_][1] == tag) & (v[s_][3] == tag))) pend &= ~(1u << s_); } \
    if (!pend) break; SPIN_FAIL(code) } __builtin_amdgcn_s_setprio(CPRIO); }

#define PIN(v) asm volatile("" : "+v"(v))
#define PISSUE(v, N, off, stride) _Pragma("unroll") for (int s_ = 0; s_ < (N); ++s_) v[s_] = __builtin_amdgcn_raw_buffer_load_b128(rw, (off) + ((s_ * (stride)) & 4095u), (s_ * (stride)) & ~4095u, 16);
#define PBAD(v) (((v)[1] ^ tag) | ((v)[3] ^ tag))
#define PRETRY(v, N, off, stride, code) { u32 pend = 0; \
    _Pragma("unroll") for (int s_ = 0; s_ < (N); ++s_) if (!__all(PBAD(v[s_]) == 0u)) pend |= 1u << s_; \
    __builtin_amdgcn_s_setprio(0); _Pragma("unroll 1") for (u32 spins = 0; pend;) { SPIN_FAIL(code) \
    _Pragma("unroll") for (int s_ = 0; s_ < (N); ++s_) if (pend >> s_ & 1u) v[s_] = __builtin_amdgcn_raw_buffer_load_b128(rw, (off) + ((s_ * (stride)) & 4095u), (s_ * (stride)) & ~4095u, 16); \
    _Pragma("unroll") for (int s_ = 0; s_ < (N); ++s_) if (pend >> s_ & 1u) { if (__all(PBAD(v[s_]) == 0u)) pend &= ~(1u << s_); } \
    } __builtin_amdgcn_s_setprio(CPRIO); }
#define PCHECK(v, N, off, stride, code) { u32 bad_ = 0; _Pragma("unroll") for (int s_ = 0; s_ < (N); ++s_) bad_ |= PBAD(v[s_]); \
    if (__builtin_expect(!__all(bad_ == 0u), 0)) PRETRY(v, N, off, stride, code) }

#define DPPMAX(n) "s_nop 1\n v_max_f32_dpp %0, %0, %0 row_ror:" #n " row_mask:0xf bank_mask:0xf\n"
__device__ __forceinline__ void wave_max2(float& a, float& b, float& jn) {
  asm volatile("s_nop 1\n v_permlane32_swap_b32 %0, %1\n v_max_f32 %0, %0, %1\n v_mov_b32 %1, %0\n"
               "s_nop 1\n v_permlane16_swap_b32 %0, %1\n v_max_f32 %0, %0, %1\n"
               DPPMAX(8) DPPMAX(4) DPPMAX(2) DPPMAX(1)
               "v_mov_b32 %2, %0\n v_mov_b32 %1, %0\n s_nop 1\n v_permlane32_swap_b32 %0, %1\n s_nop 1"
               : "+v"(a), "+v"(b), "=&v"(jn));
}
__device__ __forceinline__ float row_max(float a) {
  asm volatile(DPPMAX(8) DPPMAX(4) DPPMAX(2) DPPMAX(1) "s_nop 1" : "+v"(a));
  return a;
}
__device__ __forceinline__ float j32_add(float a, float b) {
  auto p = __builtin_amdgcn_permlane32_swap(__float_as_uint(a), __float_as_uint(b), false, false);
  return __uint_as_float(p[0]) + __uint_as_float(p[1]);
}
__device__ __forceinline__ float j16_add(float a, float b) {
  auto p = __builtin_amdgcn_permlane16_swap(__float_as_uint(a), __float_as_uint(b), false, false);
  return __uint_as_float(p[0]) + __uint_as_float(p[1]);
}
typedef __attribute__((ext_vector_type(16))) float v16f;
#define COLNT " nt"
__device__ __forceinline__ v4u mk_rsrc(const void* p, u32 bytes) {
  const u64 a = (u64)(uintptr_t)p;
  return v4u{(u32)a, (u32)(a >> 32) & 0xffffu, bytes, 0x00020000u};
}
#define COL_REQ(V) "s_nop 0\n buffer_load_dwordx4 " V ", %2, %0 offen" COLNT " lds\n s_add_u32 m0, m0, 0x400\n s_add_u32 %0, %0, 0x2000\n"
__device__ __forceinline__ void col_reads(v4u rk4, u32 vk, u32 ck, u32 ld) {
  u32 t0, t1;
  asm volatile(
      "s_mov_b32 %1, m0\n s_mov_b32 m0, %8\n s_mov_b32 %0, %7\n"
      COL_REQ("%3") COL_REQ("%4") COL_REQ("%5") COL_REQ("%6") COL_REQ("%3") COL_REQ("%4") COL_REQ("%5") COL_REQ("%6")
      "s_mov_b32 m0, %1\n"
      : "=&s"(t0), "=&s"(t1)
      : "s"(rk4), "v"(vk), "v"(vk ^ 16u), "v"(vk ^ 32u), "v"(vk ^ 48u), "s"(ck), "s"(ld)
      : "memory", "scc");
}

__device__ __forceinline__ void st_gran(char* ws, u32 off, u32 tag, u32 val) {
  __hip_atomic_store((gu64*)(ws + off), ((u64)tag << 32) | val, RLX_AGENT);
}
#define ST2(off, a, b) __builtin_amdgcn_raw_buffer_store_b128(v4u{(a), tag, (b), tag}, rw, (off), 0, 16)
template <int CTRL> __device__ __forceinline__ u32 dppu(u32 v) { return (u32)__builtin_amdgcn_update_dpp(0, (int)v, CTRL, 0xf, 0xf, true); }
__device__ __forceinline__ bf8 tr_pair(char* lds, u32 a0, u32 a1) {
  const s4 b0 = __builtin_amdgcn_ds_read_tr16_b64_v4i16((LAS s4*)(lds + a0));
  const s4 b1 = __builtin_amdgcn_ds_read_tr16_b64_v4i16((LAS s4*)(lds + a1));
  const s8 bb = {b0[0], b0[1], b0[2], b0[3], b1[0], b1[1], b1[2], b1[3]};
  return __builtin_bit_cast(bf8, bb);
}
__device__ __forceinline__ v4u lds16(char* lds, u32 a) { return *reinterpret_cast<const v4u*>(lds + a); }
__device__ __forceinline__ v4f lds16f(char* lds, u32 a) { return *reinterpret_cast<const v4f*>(lds + a); }
__device__ __forceinline__ float fdiv(float a, float d, float r) {
  const float q = a * r;
  return fmaf(fmaf(-q, d, a), r, q);
}
__device__ __forceinline__ v4u frag(v4u v, int pa) {
  v4u o;
#pragma unroll
  for (int e = 0; e < 4; ++e) o[e] = (u32)__builtin_amdgcn_ds_bpermute(pa, (int)v[e]);
  return o;
}
__device__ __forceinline__ void gemv448(char* lds, u32 wt, int lane, float& a0, float& a1) {
  const u32 gq = lane >> 4, qq = (lane & 15) >> 2, pp = lane & 3;
  const u32 ao = L_XN + (u32)(lane & 1) * 896u + gq * 16u;
  const u32 bo = wt + (8u * gq + qq) * 32u + pp * 8u;
  v4f acc0 = {0.f, 0.f, 0.f, 0.f}, acc1 = {0.f, 0.f, 0.f, 0.f};
#pragma unroll
  for (int s = 0; s < 7; ++s) {
    acc0 = __builtin_amdgcn_mfma_f32_16x16x32_bf16(as_bf8(lds16(lds, ao + s * 64)), tr_pair(lds, bo + s * 1024, bo + s * 1024 + 128), acc0, 0, 0, 0);
    acc1 = __builtin_amdgcn_mfma_f32_16x16x32_bf16(as_bf8(lds16(lds, ao + (s + 7) * 64)), tr_pair(lds, bo + (s + 7) * 1024, bo + (s + 7) * 1024 + 128), acc1, 0, 0, 0);
  }
  a0 = acc0[0] + acc1[0]; a1 = acc0[1] + acc1[1];
}

__device__ __forceinline__ void fl_write(u32 a, u32 v) { asm volatile("ds_write_b32 %0, %1" :: "v"(a), "v"(v) : "memory"); }
__device__ __forceinline__ void fl_add(u32 a, int lane) { if (lane == 0) asm volatile("ds_add_u32 %0, %1" :: "v"(a), "v"(1u) : "memory"); }
__device__ __forceinline__ int fl_read(u32 a) {
  u32 v; asm volatile("ds_read_b32 %0, %1\n s_waitcnt lgkmcnt(0)" : "=v"(v) : "v"(a) : "memory");
  return (int)__builtin_amdgcn_readfirstlane(v);
}
__device__ __forceinline__ void fl_wait(u32 a, int v) {
#pragma unroll 1
  for (u32 n = 0; n < LSPIN; ++n) { if (fl_read(a) >= v) break; __builtin_amdgcn_s_sleep(0); }
}
__device__ __forceinline__ void sbar(u32 a, int lane) { fl_add(a, lane); fl_wait(a, 8); }
template <int K> __device__ __forceinline__ void drain(u32 fp, int q, int lo) {
  asm volatile("s_waitcnt vmcnt(%0)" :: "n"(K) : "memory");
  fl_write(fp, (u32)(q - K > lo ? q - K : lo));
  if constexpr (K > 0) drain<K - 1>(fp, q, lo);
}
__device__ __forceinline__ void gate(u32 fp, int q, u32 a, int v, int& lo) {
  if (fl_read(a) >= v) return;
  if (lo < q) drain<WMAX - 2>(fp, q, lo);
  lo = q;
  fl_wait(a, v);
}
template <int N, int U, int XM, int SEG, int AUX, int W>
__device__ __forceinline__ void stream(char* lds, u32 lb, const void* base, u32 bytes, u32 vb, u32 sb, u32 ss, u32 ld, int q0, int& lo) {
  const __amdgpu_buffer_rsrc_t rs = __builtin_amdgcn_make_buffer_rsrc((void*)base, 0, bytes, 0x00020000);
#pragma unroll 1
  for (u32 k = 0; k < (u32)(N / U); ++k) {
    const int qk = q0 + (int)(k * U);
    const u32 sk = SEG == 1 ? sb + (k < 3u ? k + 1u : k == 3u ? 0u : k) * 256u : sb + k * (U * ss);
#pragma unroll
    for (u32 n = 0; n < (u32)U; ++n) {
      const int q = qk + (int)n;
      asm volatile("s_waitcnt vmcnt(%0)" :: "n"(W - 1) : "memory");
      lo = q - (W - 1) > lo ? q - (W - 1) : lo;
      fl_write(lb + F_PROG, (u32)lo);
      const u32 xm = XM == 1 ? (n & 3u) << 4 : XM == 2 ? ((2u * n) & 15u) << 4 : XM == 3 ? (((n >> 2) & 1u) << 3) << 4 : 0u;
      if (XM == 4) {
        const u32 c = vb + (k * U + n) * 64u, rr = (c * 2341u) >> 16;
        __builtin_amdgcn_raw_ptr_buffer_load_lds(rs, (LAS void*)(lds + ld + k * (U * 1024u) + n * 1024u), 16, c * 16u + rr * 448u, sb, 0, AUX);
      } else
      __builtin_amdgcn_raw_ptr_buffer_load_lds(rs, (LAS void*)(lds + ld + k * (U * 1024u) + n * 1024u), 16, vb ^ xm, sk + n * ss, 0, AUX);
    }
  }
}

__global__ __launch_bounds__(576) void mla_decode(const u16* x, const u16* ckv, const u16* wq, const u16* wuk,
                                                  const u16* wuv, const u16* wo, const u16* w1, const u16* b1,
                                                  const u16* w2, const u16* b2, char* ws, u16* out) {
  __shared__ __attribute__((aligned(16))) char lds[L_END];
  const int t = threadIdx.x, lane = t & 63;
  const int w = __builtin_amdgcn_readfirstlane(t >> 6);
  const int i = blockIdx.x, xcd = i & 7, r = i >> 3;
  const int h1i = xcd * 2 + (r >> 4), cs = r & 15;
  const int G = xcd * 4 + (r >> 3), b = G >> 4, g = G & 15, j = r & 7;
  const int ch = r & 1, hd = xcd * 16 + (r >> 1), ds = hd & 7;
  const int fsl = hd < 112 ? hd : 0;
  STAMP_DECL
  STAMP(0);
  asm volatile("" : "+s"(x), "+s"(ckv), "+s"(wq), "+s"(wuk), "+s"(wuv), "+s"(wo), "+s"(w1), "+s"(b1), "+s"(w2), "+s"(b2), "+s"(ws), "+s"(out));
  gu32* tmo = (gu32*)(ws + WS_EP + 128);
  const __amdgpu_buffer_rsrc_t rw = __builtin_amdgcn_make_buffer_rsrc(ws, 0, WS_END, 0x00020000);
  const u32 gq = lane >> 4, qq = (lane & 15) >> 2, pp = lane & 3;

  const u32 lb = (u32)(uintptr_t)(LAS char*)lds;

  if (w == 8) {
    __builtin_amdgcn_s_setprio(SPRIO);
    if (lane < 32) fl_write(lb + L_FLAG + lane * 4, 0u);
    if (!EARLYQ) BAR();
    int lo = 0;
    const u32 hb = (u32)lane >> 5;
    const u32 vk = gq * 2048u + (((u32)lane & 15u) ^ (gq << 2)) * 16u;
    stream<14, 7, 0, 0, 0, WIN>(lds, lb, wq, M_ * 8192u, (u32)(lane >> 3) * 8192u + (((u32)lane & 7u) ^ ((((u32)lane >> 3) & 3u) << 1)) * 16u, (u32)(cs & 3) * (112u * 8192u) + (u32)h1i * 512u + (u32)(cs >> 2) * 128u, 8u * 8192u, L_WQ, 0, lo);
    if (EARLYQ) BAR();
    stream<32, 8, 2, 0, 2, WIN>(lds, lb, wuk, 1024u * 8192u, hb * 8192u + (((u32)lane & 31u) ^ hb) * 16u, (u32)(cs * 64) * 8192u + (u32)h1i * 512u, 16384u, L_UV, P_UK - 32, lo);
    STAMP(24);
    stream<64, 8, 1, 1, 0, WIN>(lds, lb, ckv, 2u * SK * DC * 2u, vk, (u32)(b * SK + g * 256 + j * 32) * 2048u, 8192u, L_RA, P_UK, lo);
    STAMP(25);
    if (GE1) gate(lb + F_PROG, P_ROWS, lb + F_E1, GE1, lo);
    STAMP(30);
    gate(lb + F_PROG, P_UV - 32, lb + F_UK, 4, lo);
    if (GPOS == 0) gate(lb + F_PROG, P_ROWS, lb + F_E2, 8, lo);
    stream<32, 8, 3, 0, 2, WIN2>(lds, lb, wuv, 1024u * 8192u, hb * 8192u + (((u32)lane & 31u) ^ (hb << 2)) * 16u, (u32)(cs * 64) * 8192u + (u32)h1i * 512u, 16384u, L_UV, P_UV - 32, lo);
    STAMP(26);
    if (GPOS == 1) gate(lb + F_PROG, P_UV, lb + F_E2, 8, lo);
    STAMP(31);
    stream<14, 7, 0, 0, 0, WIN2>(lds, lb, w1, M_ * F_ * 2u, (u32)(lane >> 1) * (F_ * 2u) + (u32)(lane & 1) * 16u, (u32)fsl * 32u, 32u * F_ * 2u, L_W1, P_W1 - 14, lo);
    STAMP(27);
    if (GPOS == 2) gate(lb + F_PROG, P_W1, lb + F_E2, 8, lo);
    if (GE3) gate(lb + F_PROG, P_W1, lb + F_E3, GE3, lo);
    stream<7, 7, 4, 0, 2, WIN2>(lds, lb, w2, 1792u * 896u, (u32)lane, (u32)(fsl * 16) * 896u + (u32)ch * 448u, 0u, L_W2, P_W2 - 7, lo);
    gate(lb + F_PROG, P_W2, lb + F_RO, 4, lo);
    stream<14, 7, 4, 0, 2, WIN2>(lds, lb, wo, 4096u * 896u, (u32)lane, (u32)(hd * 32) * 896u + (u32)ch * 448u, 0u, L_WO, P_W2, lo);
    STAMP(28);
    {
      asm volatile("s_waitcnt vmcnt(%0)" :: "n"(P_END - P_W1) : "memory");
      float a0, a1;
      gemv448(lds, L_W1, lane, a0, a1);
      const float hv = rbf(rbf(lane >= 32 ? a1 : a0) + *reinterpret_cast<const float*>(lds + L_BI + (lane & 15) * 4));
      const float ge = 0.5f * hv * (1.f + tanhf(0.7978845608028654f * (hv + 0.044715f * hv * hv * hv)));
      *reinterpret_cast<u16*>(lds + L_VH + lane * 2) = ((lane & 16) == 0 && hd < 112) ? f2bf(ge) : (u16)0;
      asm volatile("s_waitcnt lgkmcnt(0)" ::: "memory");
    }
    drain<WMAX - 1>(lb + F_PROG, P_END, lo);
    STAMP(29);
    STAMP_FLUSH;
    return;
  }

  __builtin_amdgcn_s_setprio(CPRIO);
  const u32 epv = __builtin_amdgcn_raw_buffer_load_b32(rw, WS_EP, 0, 0);
  const int pa = (4 * (lane & 15) + (lane >> 4)) * 4;
  u32 tag, pre0 = 0;
  if (w == 0) {
    const int lc = lane < 56 ? lane : 0;
    const v4u xr0 = ldp16(x + lc * 8), xr1 = ldp16(x + M_ + lc * 8);
    STAMP(21);
    BAR();
    float xa[8], xb[8];
#pragma unroll
    for (int p = 0; p < 4; ++p) { xa[2 * p] = lo_f(xr0[p]); xa[2 * p + 1] = hi_f(xr0[p]); xb[2 * p] = lo_f(xr1[p]); xb[2 * p + 1] = hi_f(xr1[p]); }
    const float act = lane < 56 ? 1.f : 0.f;
    float s0 = 0.f, s1 = 0.f;
#pragma unroll
    for (int k = 0; k < 8; ++k) { s0 += xa[k]; s1 += xb[k]; }
    s0 = wave_sum(s0 * act); s1 = wave_sum(s1 * act);
    const float m0 = s0 * (1.f / M_), m1 = s1 * (1.f / M_);
    float v0 = 0.f, v1 = 0.f;
#pragma unroll
    for (int k = 0; k < 8; ++k) { const float d0 = xa[k] - m0, d1 = xb[k] - m1; v0 += d0 * d0; v1 += d1 * d1; }
    v0 = wave_sum(v0 * act); v1 = wave_sum(v1 * act);
    const float mb0 = rbf(m0), mb1 = rbf(m1);
    const float den0 = rbf(sqrtf(rbf(rbf(v0 * (1.f / M_)) + 1e-5f)));
    const float den1 = rbf(sqrtf(rbf(rbf(v1 * (1.f / M_)) + 1e-5f)));
    const float r0 = __builtin_amdgcn_rcpf(den0), r1 = __builtin_amdgcn_rcpf(den1);
    v4u na, nb;
#pragma unroll
    for (int p = 0; p < 4; ++p) {
      na[p] = pk2(fdiv(rbf(xa[2 * p] - mb0), den0, r0), fdiv(rbf(xa[2 * p + 1] - mb0), den0, r0));
      nb[p] = pk2(fdiv(rbf(xb[2 * p] - mb1), den1, r1), fdiv(rbf(xb[2 * p + 1] - mb1), den1, r1));
    }
    if (lane < 56) {
      *reinterpret_cast<v4u*>(lds + L_XN + lane * 16) = na;
      *reinterpret_cast<v4u*>(lds + L_XN + 896 + lane * 16) = nb;
    }
    fl_add(lb + F_XN, lane);
    STAMP(17);
  } else if (w < 4) {
    float bias = 0.f;
    if (w == 1) bias = bf2f(ldh(b1 + fsl * 16 + (lane & 15)));
    if (w == 3) pre0 = __builtin_amdgcn_raw_buffer_load_b32(rw, lane < 8 ? WS_QL + (u32)(lane >> 2) * 65536u + (u32)(cs * 4 + (lane & 3)) * 1024u + (u32)h1i * 64u : WS_QG + (u32)h1i * 16384u + (u32)(cs & 3) * 4096u + (u32)(cs >> 2) * 1024u + (u32)(lane & 7) * 128u, 0, PRE_AUX);
    BAR();
    if (w == 1 && lane < 16) *reinterpret_cast<float*>(lds + L_BI + lane * 4) = bias;
    fl_wait(lb + F_XN, 1);
  }
  if (w < 4) {
    fl_wait(lb + F_PROG, P_WQ);
    STAMP(18);
    const u32 ao = L_XN + (u32)(lane & 1) * 896u + (u32)(cs & 3) * 224u;
    const u32 ch = ((((u32)w * 2u + (pp >> 1)) ^ (qq << 1)) * 16u) + (pp & 1u) * 8u;
    const u32 bo = L_WQ + (8u * gq + qq) * 128u + ch;
    v4f acc0 = {0.f, 0.f, 0.f, 0.f}, acc1 = {0.f, 0.f, 0.f, 0.f};
    acc0 = __builtin_amdgcn_mfma_f32_16x16x32_bf16(as_bf8(lds16(lds, ao + gq * 16u)), tr_pair(lds, bo, bo + 512), acc0, 0, 0, 0);
    acc1 = __builtin_amdgcn_mfma_f32_16x16x32_bf16(as_bf8(lds16(lds, ao + gq * 16u + 64)), tr_pair(lds, bo + 4096, bo + 4096 + 512), acc1, 0, 0, 0);
    acc0 = __builtin_amdgcn_mfma_f32_16x16x32_bf16(as_bf8(lds16(lds, ao + gq * 16u + 128)), tr_pair(lds, bo + 8192, bo + 8192 + 512), acc0, 0, 0, 0);
    acc1 = __builtin_amdgcn_mfma_f32_16x16x16bf16_1k(*reinterpret_cast<const s4*>(lds + ao + 192u + gq * 8u),
                                                     __builtin_amdgcn_ds_read_tr16_b64_v4i16((LAS s4*)(lds + L_WQ + (96u + 4u * gq + qq) * 128u + ch)), acc1, 0, 0, 0);
    tag = __builtin_amdgcn_readfirstlane(epv) + 1u;
    {
      const u32 qv = __float_as_uint(gq ? acc0[1] + acc1[1] : acc0[0] + acc1[0]), qn = dppu<0xB1>(qv);
      if ((lane & 33) == 0) ST2(WS_QG + (u32)h1i * 16384u + (u32)(cs & 3) * 4096u + (u32)(cs >> 2) * 1024u + (u32)w * 256u + (u32)lane * 8u, qv, qn);
    }
    STAMP(1);
    fl_add(lb + F_RO, lane);
    asm volatile("" :: "v"(pre0));
    if (w == 3) pre0 = PRE_RD(lane < 32 ? WS_SC + (u32)((G * 16 + (lane >> 1)) * 256 + j * 32) * 8u + (u32)(lane & 1) * 128u : WS_ML + (u32)((b * 16 + 2 * j + (lane & 1)) * 16 + (g & 8)) * 16u);
  } else {
    v4u wk[8];
    BAR();
    tag = __builtin_amdgcn_readfirstlane(epv) + 1u;
    u32 qo = WS_QG + (u32)h1i * 16384u + (u32)(w - 4) * 1024u + (u32)lane * 16u;
    u32 qw = L_QST + (u32)((lane >> 3) & 1) * 512u + (u32)(w - 4) * 128u + gq * 32u + (u32)(lane & 7) * 4u;
    u32 ao = L_QST + (u32)(lane & 1) * 512u + gq * 16u;
    const int bb = lane & 1, wq4 = w - 4;
    u32 qa = WS_QL + (u32)bb * 65536u + (u32)((cs * 2 + (wq4 >> 1)) * 2 + ((lane >> 2) & 1)) * 1024u + (u32)h1i * 64u
             + (u32)((wq4 & 1) * 2 + ((lane >> 3) & 1)) * 16u;
    PIN(qo); PIN(qw); PIN(ao); PIN(qa);
    fl_wait(lb + F_RO, QGATE);
    if (QDLY) __builtin_amdgcn_s_sleep(QDLY);
    STAMP(19);
    v4u ga[4];
    PISSUE(ga, 4, qo, 4096u)
    fl_wait(lb + F_PROG, P_UK);
    {
      const u32 ko = L_UV + (u32)((w - 4) * 16 + (lane & 15)) * 512u;
#pragma unroll
      for (int s = 0; s < 8; ++s) wk[s] = lds16(lds, ko + ((((u32)s * 4u + gq) ^ ((u32)lane & 15u)) * 16u));
    }
    fl_add(lb + F_UK, lane);
    PCHECK(ga, 4, qo, 4096u, 1u)
    {
      const float q0 = (__uint_as_float(ga[0][0]) + __uint_as_float(ga[1][0])) + (__uint_as_float(ga[2][0]) + __uint_as_float(ga[3][0]));
      const float q1 = (__uint_as_float(ga[0][2]) + __uint_as_float(ga[1][2])) + (__uint_as_float(ga[2][2]) + __uint_as_float(ga[3][2]));
      *reinterpret_cast<u32*>(lds + qw) = pk2(q0, q1);
    }
    fl_add(lb + F_QS, lane);
    STAMP(20);
    fl_wait(lb + F_QS, 4);
    v4f acc0 = {0.f, 0.f, 0.f, 0.f}, acc1 = {0.f, 0.f, 0.f, 0.f};
#pragma unroll
    for (int s = 0; s < 4; ++s) {
      acc0 = __builtin_amdgcn_mfma_f32_16x16x32_bf16(as_bf8(lds16(lds, ao + s * 64)), as_bf8(wk[s]), acc0, 0, 0, 0);
      acc1 = __builtin_amdgcn_mfma_f32_16x16x32_bf16(as_bf8(lds16(lds, ao + (s + 4) * 64)), as_bf8(wk[s + 4]), acc1, 0, 0, 0);
    }
    const float a0 = acc0[0] + acc1[0], a1 = acc0[1] + acc1[1];
    const float own = bb ? a1 : a0, oth = dppf<0xB1>(bb ? a0 : a1);
    {
      const u32 qv = bb ? pk2(oth, own) : pk2(own, oth), qn = dppu<0x4E>(qv);
      if ((lane & 50) == 0) ST2(qa, qv, qn);
    }
    STAMP(2);
    if (BDLY) __builtin_amdgcn_s_sleep(BDLY);
    fl_add(lb + F_QP, lane);
  }
  v4u bv[8];
  const int slot = w == 0 ? 3 : w < 4 ? w - 1 : w;
  {
    fl_wait(lb + F_PROG, P_UK + 8 * (slot + 1));
    const u32 key = (u32)lane & 15u;
    const u32 ro = L_RA + (u32)slot * 8192u + key * 256u;
#pragma unroll
    for (int s = 0; s < 8; ++s) bv[s] = lds16(lds, ro + (u32)(s >> 2) * 4096u + ((((u32)(s & 3) * 4u + gq) ^ swz(key)) * 16u));
  }
  STAMP(3);
  u32 so = WS_SC + (u32)((G * 16 + 2 * w) * 256 + 2 * lane) * 8u;
  {
    u32 qo = WS_QL + (u32)b * 65536u + (u32)w * 8192u + (u32)lane * 16u;
    u32 srw = L_SRED + (u32)((w * 16 + (int)gq * 4) * 33 + (lane & 15)) * 4u;
    u32 srr = L_SRED + (u32)((t >> 5) * 33 + (t & 31)) * 4u;
    u32 sco = WS_SC + (u32)((G * 16 + (t >> 5)) * 256 + j * 32 + (t & 31)) * 8u;
    int pav = pa;
    PIN(qo); PIN(srw); PIN(srr); PIN(sco); PIN(so); PIN(pav);
    v4u gl[8];
    fl_wait(lb + F_QP, 4);
    PISSUE(gl, 8, qo, 1024u)
    fl_add(lb + F_E1, lane);
    v4f acc0, acc1;
#pragma unroll 1
    for (int pass = 0;; ++pass) {
      u32 bad = 0;
      acc0 = v4f{0.f, 0.f, 0.f, 0.f}; acc1 = v4f{0.f, 0.f, 0.f, 0.f};
#pragma unroll
      for (int s = 0; s < 4; ++s) {
        bad |= PBAD(gl[2 * s]) | PBAD(gl[2 * s + 1]);
        const v4u av = frag(v4u{gl[2 * s][0], gl[2 * s][2], gl[2 * s + 1][0], gl[2 * s + 1][2]}, pav);
        acc0 = __builtin_amdgcn_mfma_f32_16x16x32_bf16(as_bf8(av), as_bf8(bv[s]), acc0, 0, 0, 0);
        acc1 = __builtin_amdgcn_mfma_f32_16x16x32_bf16(as_bf8(av), as_bf8(bv[4 + s]), acc1, 0, 0, 0);
      }
      if (__builtin_expect(pass || __all(bad == 0u), 1)) break;
      PRETRY(gl, 8, qo, 1024u, 2u)
    }
    asm volatile("" :: "v"(pre0));
    STAMP(4);
    float* sr = reinterpret_cast<float*>(lds + srw);
#pragma unroll
    for (int q = 0; q < 4; ++q) { sr[q * 33] = acc0[q]; sr[q * 33 + 16] = acc1[q]; }
    sbar(lb + F_B, lane);
    STAMP(5);
    const float* sp = reinterpret_cast<const float*>(lds + srr);
    float s = ((sp[0] + sp[528]) + (sp[2 * 528] + sp[3 * 528])) + ((sp[4 * 528] + sp[5 * 528]) + (sp[6 * 528] + sp[7 * 528]));
    s *= 0.0625f * 1.4426950408889634f;
    const u32 sn = dppu<0xB1>(__float_as_uint(s));
    if ((lane & 1) == 0) ST2(sco, __float_as_uint(s), sn);
    fl_add(lb + F_SR, lane);
    col_reads(mk_rsrc(ckv, 2u * SK * DC * 2u), gq * 2048u + (((u32)lane & 15u) ^ (gq << 2)) * 16u,
              (u32)(b * SK + g * 256 + slot * 32) * 2048u + (u32)j * 256u, lb + L_RA + (u32)slot * 8192u);
    if (w == 3) pre0 = PRE_RD(WS_PV + (u32)(((b * 16 + (lane >> 2)) * 16 + g) * 512 + j * 64) * 8u + (u32)(lane & 3) * 128u);
  }
  const u32 bq = (u32)w >> 2;
  u32 pvo = WS_PV + (((bq * 16u + (u32)h1i) * 16u + (u32)(w & 3) * 4u + gq) * 512u + (u32)cs * 32u + (u32)(lane & 15) * 2u) * 8u;
  {
    u32 pw = L_P + (u32)(2 * w) * 528u + (u32)lane * 4u;
    u32 po = L_P + (u32)(lane & 15) * 528u + gq * 16u;
    u32 k0 = L_RA + off_b(8 * gq + qq, 2 * w + (pp >> 1)) + 8 * (pp & 1);
    u32 k1 = L_RA + off_b(8 * gq + 4 + qq, 2 * w + (pp >> 1)) + 8 * (pp & 1);
    const int hi8 = (lane >> 3) & 1, od = lane & 1;
    u32 go = WS_PV + (u32)(((b * 16 + (lane >> 4) * 4 + 2 * hi8 + od) * 16 + g) * 512 + j * 64 + w * 8 + (lane & 6)) * 8u;
    PIN(pw); PIN(po); PIN(k0); PIN(k1); PIN(go); PIN(pvo);
    v4u gs[4];
    bf8 kb[8];
    if (CDLY) __builtin_amdgcn_s_sleep(CDLY);
    PISSUE(gs, 4, so, 1024u)
    fl_add(lb + F_E2, lane);
    fl_wait(lb + F_SR, 8);
    PCHECK(gs, 4, so, 1024u, 3u)
    asm volatile("s_waitcnt vmcnt(0)" ::: "memory");
    fl_add(lb + F_COL, lane);
    asm volatile("" :: "v"(pre0));
    STAMP(6);
    float sv[8];
#pragma unroll
    for (int q = 0; q < 4; ++q) { sv[2 * q] = __uint_as_float(gs[q][0]); sv[2 * q + 1] = __uint_as_float(gs[q][2]); }
    float m0 = fmaxf(fmaxf(sv[0], sv[1]), fmaxf(sv[2], sv[3])), m1 = fmaxf(fmaxf(sv[4], sv[5]), fmaxf(sv[6], sv[7])), mj;
    wave_max2(m0, m1, mj);
    u32 pk[4];
#pragma unroll
    for (int q = 0; q < 4; ++q) {
      const float m = q < 2 ? m0 : m1;
      pk[q] = pk2(__builtin_amdgcn_exp2f(sv[2 * q] - m), __builtin_amdgcn_exp2f(sv[2 * q + 1] - m));
      *reinterpret_cast<u32*>(lds + pw + (q >> 1) * 528 + (q & 1) * 256) = pk[q];
    }
    fl_add(lb + F_B + 4, lane);
    if (w == j) {
      float l = j32_add((lo_f(pk[0]) + hi_f(pk[0])) + (lo_f(pk[1]) + hi_f(pk[1])), (lo_f(pk[2]) + hi_f(pk[2])) + (lo_f(pk[3]) + hi_f(pk[3])));
      l = x16_add(l);
      l = ror_add<8>(l); l = ror_add<4>(l); l = ror_add<2>(l); l = ror_add<1>(l);
      if ((lane & 31) == 0) ST2(WS_ML + (u32)((b * 16 + 2 * w + (lane >> 5)) * 16 + g) * 16u, __float_as_uint(mj), __float_as_uint(l));
    }
    fl_wait(lb + F_COL, 8);
#pragma unroll
    for (int ks = 0; ks < 8; ++ks) kb[ks] = tr_pair(lds, k0 + ks * 8192, k1 + ks * 8192);
    STAMP(22);
    fl_wait(lb + F_B + 4, 8);
    STAMP(7);
    v4f acc0 = {0.f, 0.f, 0.f, 0.f}, acc1 = {0.f, 0.f, 0.f, 0.f};
#pragma unroll
    for (int ks = 0; ks < 4; ++ks) {
      acc0 = __builtin_amdgcn_mfma_f32_16x16x32_bf16(as_bf8(lds16(lds, po + ks * 64)), kb[ks], acc0, 0, 0, 0);
      acc1 = __builtin_amdgcn_mfma_f32_16x16x32_bf16(as_bf8(lds16(lds, po + (ks + 4) * 64)), kb[ks + 4], acc1, 0, 0, 0);
    }
    const v4f acc = acc0 + acc1;
    const float s0 = hi8 ? acc[0] : acc[2], s1 = hi8 ? acc[1] : acc[3];
    const float n0 = dppf<0x128>(s0), n1 = dppf<0x128>(s1);
    const u32 g0 = hi8 ? pk2(n0, acc[2]) : pk2(acc[0], n0);
    const u32 g1 = hi8 ? pk2(n1, acc[3]) : pk2(acc[1], n1);
    const u32 gn = dppu<0xB1>(od ? g0 : g1);
    ST2(go, od ? gn : g0, od ? g1 : gn);
  }
  if (w == 3) pre0 = PRE_RD(lane < 32 ? WS_CP + ((u32)(h1i * 16 + (lane >> 1)) * 16u + (u32)cs) * 256u + (u32)(lane & 1) * 128u
                                      : WS_OG + (u32)(((lane >> 4) & 1) * 128 + hd) * 3584u + (u32)ch * 1792u + (u32)((lane & 15) < 14 ? (lane & 15) : 0) * 128u);
  STAMP(8);
  u32 cvo = WS_CP + (((u32)h1i * 2u + bq) * 8u + (u32)ds) * 4096u + (u32)(lane >> 2) * 256u + (u32)(w & 3) * 64u + pp * 16u;
  {
    u32 aw = L_AT + (bq * 16u + (u32)(w & 3) * 4u + gq) * 144u + (u32)(lane & 15) * 8u;
    u32 ar = L_AT + (u32)(lane & 31) * 144u + (u32)(lane >> 5) * 16u;
    u32 wr = L_WG + (u32)(lane >> 5) * 16u;
    u32 bo = L_UV + ((gq >> 1) * 4u + (qq >> 1) + 8u * (qq & 1u)) * 512u
             + ((((u32)w * 4u + (gq & 1u) * 2u + (pp >> 1)) ^ (((qq >> 1) << 2) | ((qq & 1u) << 3))) * 16u) + (pp & 1u) * 8u;
    u32 co = WS_CP + ((((u32)h1i * 2u + (u32)(lane >> 5)) * 8u + (u32)w) * 16u + (u32)cs) * 256u + (u32)(lane & 31) * 8u;
    u32 mlo = WS_ML + (((gq & 1u) * 16u + (u32)h1i) * 16u + (u32)(lane & 15)) * 16u;
    PIN(aw); PIN(ar); PIN(wr); PIN(bo); PIN(co); PIN(mlo); PIN(cvo);
    v4u pv[1], ml[1];
    bf8 ub[4];
    if (DDLY) __builtin_amdgcn_s_sleep(DDLY);
    if (w == MLW) { PISSUE(ml, 1, mlo, 0u) }
    PISSUE(pv, 1, pvo, 0u)
    fl_add(lb + F_E3, lane);
    const bool have = fl_read(lb + F_PROG) >= P_UV;
    if (have) {
#pragma unroll
      for (int T = 0; T < 4; ++T) ub[T] = tr_pair(lds, bo + T * 8192, bo + T * 8192 + 1024);
    }
    if (w == MLW) {
      PCHECK(ml, 1, mlo, 0u, 7u)
      const float mg = __uint_as_float(ml[0][0]);
      const float eg = __builtin_amdgcn_exp2f(mg - row_max(mg));
      float ls = eg * __uint_as_float(ml[0][2]);
      ls = ror_add<8>(ls); ls = ror_add<4>(ls); ls = ror_add<2>(ls); ls = ror_add<1>(ls);
      const float rl = __builtin_amdgcn_rcpf(ls);
      *reinterpret_cast<float*>(lds + L_WG + (lane & 31) * 4) = fdiv(eg, ls, rl);
    }
    PCHECK(pv, 1, pvo, 0u, 4u)
    asm volatile("" :: "v"(pre0));
    STAMP(9);
    *reinterpret_cast<v2u*>(lds + aw) = v2u{pv[0][0], pv[0][2]};
    fl_add(lb + F_B + 8, lane);
    if (!have) {
      fl_wait(lb + F_PROG, P_UV);
#pragma unroll
      for (int T = 0; T < 4; ++T) ub[T] = tr_pair(lds, bo + T * 8192, bo + T * 8192 + 1024);
    }
    STAMP(23);
    fl_wait(lb + F_B + 8, 8);
    STAMP(10);
    v16f acc;
#pragma unroll
    for (int q = 0; q < 16; ++q) acc[q] = 0.f;
    v4u at[4];
#pragma unroll
    for (int T = 0; T < 4; ++T) at[T] = lds16(lds, ar + T * 32);
    v4f wt[4];
#pragma unroll
    for (int q = 0; q < 4; ++q) wt[q] = lds16f(lds, wr + q * 32);
#pragma unroll
    for (int T = 0; T < 4; ++T) acc = __builtin_amdgcn_mfma_f32_32x32x16_bf16(as_bf8(at[T]), ub[T], acc, 0, 0, 0);
    float c[2];
#pragma unroll
    for (int e = 0; e < 2; ++e) {
      float u0 = wt[2 * e][0] * acc[8 * e], u1 = wt[2 * e][1] * acc[8 * e + 1];
      u0 = fmaf(wt[2 * e][2], acc[8 * e + 2], u0); u1 = fmaf(wt[2 * e][3], acc[8 * e + 3], u1);
      u0 = fmaf(wt[2 * e + 1][0], acc[8 * e + 4], u0); u1 = fmaf(wt[2 * e + 1][1], acc[8 * e + 5], u1);
      u0 = fmaf(wt[2 * e + 1][2], acc[8 * e + 6], u0); u1 = fmaf(wt[2 * e + 1][3], acc[8 * e + 7], u1);
      c[e] = u0 + u1;
    }
    const u32 cv = __float_as_uint(j32_add(c[0], c[1])), cn = dppu<0xB1>(cv);
    if ((lane & 1) == 0) ST2(co, cv, cn);
  }
  STAMP(11);
  const int fb = i >= 56 ? 1 : 0, fc = i - fb * 56;
  u32 oo = WS_OG + ((u32)(fb * 128 + (t >> 2)) * M_ + (u32)(fc * 8 + (t & 3) * 2)) * 8u;
  {
    u32 vw = L_VST + bq * 64u + (u32)(w & 3) * 16u + pp * 4u + (gq & 1u) * 2u;
    u32 va = (u32)(lane & 1) * 64u + gq * 16u;
    u32 og = WS_OG + (u32)(((lane >> 5) * 128 + hd) * M_ + ch * 224 + w * 32 + (lane & 31)) * 8u;
    const u32 wc = w < 7 ? (u32)w : 0u;
    u32 bo0 = L_WO + (8u * gq + qq) * 448u + wc * 64u + pp * 8u;
    u32 bo1 = L_W2 + ((8u * gq + qq) & 15u) * 448u + wc * 64u + pp * 8u;
    PIN(vw); PIN(va); PIN(og); PIN(bo0); PIN(bo1); PIN(oo);
    v4u cv[1];
    if (FDLY) __builtin_amdgcn_s_sleep(FDLY);
    PISSUE(cv, 1, cvo, 0u)
    fl_wait(lb + F_PROG, P_END);
    const bf8 a1 = as_bf8(lds16(lds, L_VH + va));
    const v4f z = {0.f, 0.f, 0.f, 0.f};
    v4f oa = __builtin_amdgcn_mfma_f32_16x16x32_bf16(a1, tr_pair(lds, bo1, bo1 + 1792), z, 0, 0, 0);
    v4f ob = __builtin_amdgcn_mfma_f32_16x16x32_bf16(a1, tr_pair(lds, bo1 + 32, bo1 + 32 + 1792), z, 0, 0, 0);
    bf8 wa = tr_pair(lds, bo0, bo0 + 1792), wb = tr_pair(lds, bo0 + 32, bo0 + 32 + 1792);
    asm volatile("" : "+v"(oa), "+v"(ob), "+v"(wa), "+v"(wb));
    PCHECK(cv, 1, cvo, 0u, 6u)
    float c = x32_add(j16_add(__uint_as_float(cv[0][0]), __uint_as_float(cv[0][2])));
    c = ror_add<4>(c); c = ror_add<8>(c);
    *reinterpret_cast<u16*>(lds + vw) = f2bf(c);
    STAMP(15);
    sbar(lb + F_B + 12, lane);
    STAMP(16);
    const bf8 a0 = as_bf8(lds16(lds, L_VST + va));
    oa = __builtin_amdgcn_mfma_f32_16x16x32_bf16(a0, wa, oa, 0, 0, 0);
    ob = __builtin_amdgcn_mfma_f32_16x16x32_bf16(a0, wb, ob, 0, 0, 0);
    const float o0 = (gq & 1u) ? ob[0] : oa[0], o1 = (gq & 1u) ? ob[1] : oa[1];
    const u32 ov = __float_as_uint(lane >= 32 ? o1 : o0), on = dppu<0xB1>(ov);
    if (w < 7 && (lane & 1) == 0) ST2(og, ov, on);
  }
  STAMP(12);
  if (i < 112) {
    u32 fw = L_FR + (pp * 2u + (gq & 1u)) * 32u + (u32)w * 4u;
    PIN(fw);
    float xr = 0.f;
    if (w == 0) xr = bf2f(ldh(x + fb * M_ + fc * 8 + (lane >> 3))) + bf2f(ldh(b2 + fc * 8 + (lane >> 3)));
    v4u ov[1];
    if (EDLY) __builtin_amdgcn_s_sleep(EDLY);
    PISSUE(ov, 1, oo, 0u)
    PCHECK(ov, 1, oo, 0u, 5u)
    float c = x32_add(j16_add(__uint_as_float(ov[0][0]), __uint_as_float(ov[0][2])));
    c = ror_add<4>(c); c = ror_add<8>(c);
    *reinterpret_cast<float*>(lds + fw) = c;
    fl_add(lb + F_B + 16, lane);
    if (w == 0) {
      fl_wait(lb + F_B + 16, 8);
      STAMP(13);
      float a = *reinterpret_cast<const float*>(lds + L_FR + lane * 4);
      a += dppf<0xB1>(a); a += dppf<0x4E>(a); a += dppf<0x141>(a);
      if ((lane & 7) == 0) *(GAS u16*)(out + fb * M_ + fc * 8 + (lane >> 3)) = f2bf(xr + a);
      if (i == 0 && lane == 0) __hip_atomic_store((gu32*)(ws + WS_EP), tag, RLX_AGENT);
      STAMP(14);
    }
  }
  STAMP_FLUSH;
}

extern "C" void kernel_launch(void* const* d_in, const int* in_sizes, int n_in,
                              void* d_out, int out_size, void* d_ws, size_t ws_size,
                              hipStream_t stream) {
  const u16* x = (const u16*)d_in[0];
  const u16* ckv = (const u16*)d_in[1];
  const u16* wq = (const u16*)d_in[2];
  const u16* wuk = (const u16*)d_in[3];
  const u16* wuv = (const u16*)d_in[4];
  const u16* wo = (const u16*)d_in[5];
  const u16* w1 = (const u16*)d_in[7];
  const u16* b1 = (const u16*)d_in[8];
  const u16* w2 = (const u16*)d_in[9];
  const u16* b2 = (const u16*)d_in[10];
  mla_decode<<<256, 576, 0, stream>>>(x, ckv, wq, wuk, wuv, wo, w1, b1, w2, b2, (char*)d_ws, (u16*)d_out);
}
```

```cpp
#include <hip/hip_runtime.h>
#include <hip/hip_bf16.h>
#include <stdint.h>

typedef unsigned short u16;
typedef unsigned int u32;
typedef unsigned long long u64;
typedef __attribute__((ext_vector_type(4))) unsigned v4u;
typedef __attribute__((ext_vector_type(2))) unsigned v2u;
typedef __attribute__((ext_vector_type(4))) float v4f;
typedef __attribute__((ext_vector_type(8))) __bf16 bf8;
typedef __attribute__((ext_vector_type(4))) short s4;
typedef __attribute__((ext_vector_type(8))) short s8;

#define GAS __attribute__((address_space(1)))
#define LAS __attribute__((address_space(3)))
typedef GAS u32 gu32;
typedef GAS u64 gu64;
#define RLX_AGENT __ATOMIC_RELAXED, __HIP_MEMORY_SCOPE_AGENT

#define M_ 448
#define F_ 1792
#define DC 1024
#define SK 4096

constexpr u32 SPIN_CAP = 1u << 18;

constexpr u32 WS_EP = 0;
constexpr u32 WS_QL = 65536;
constexpr u32 WS_SC = 262144;
constexpr u32 WS_PV = 1310720;
constexpr u32 WS_ML = 3407872;
constexpr u32 WS_CP = 3416064;
constexpr u32 WS_OG = 4464640;
constexpr u32 WS_QG = WS_OG + 2 * 128 * 448 * 8;
constexpr u32 WS_END = WS_QG + 262144;

#define STAMP(k) do {} while (0)
#define STAMP_DECL
#define STAMP_FLUSH do {} while (0)

#define BAR() do { asm volatile("s_waitcnt lgkmcnt(0)" ::: "memory"); __builtin_amdgcn_s_barrier(); asm volatile("" ::: "memory"); } while (0)

__device__ __forceinline__ float bf2f(u16 v) { return __uint_as_float(((u32)v) << 16); }
__device__ __forceinline__ u16 f2bf(float f) { __hip_bfloat16 b = __float2bfloat16(f); return *reinterpret_cast<u16*>(&b); }
__device__ __forceinline__ float rbf(float f) { return bf2f(f2bf(f)); }
__device__ __forceinline__ float lo_f(u32 v) { return __uint_as_float(v << 16); }
__device__ __forceinline__ float hi_f(u32 v) { return __uint_as_float(v & 0xffff0000u); }
__device__ __forceinline__ u32 pk2(float a, float b) { return (u32)f2bf(a) | ((u32)f2bf(b) << 16); }
__device__ __forceinline__ v4u ld16(const u16* p) { return __builtin_nontemporal_load((const GAS v4u*)p); }
__device__ __forceinline__ v4u ldp16(const u16* p) { return *(const GAS v4u*)p; }
__device__ __forceinline__ u16 ldh(const u16* p) { return *(const GAS u16*)p; }
__device__ __forceinline__ bf8 as_bf8(v4u v) { return __builtin_bit_cast(bf8, v); }

template <int CTRL>
__device__ __forceinline__ float dppf(float v) {
  return __int_as_float(__builtin_amdgcn_update_dpp(0, __float_as_int(v), CTRL, 0xf, 0xf, true));
}
template <int N> __device__ __forceinline__ float ror_add(float v) { return v + dppf<0x120 + N>(v); }
template <int N> __device__ __forceinline__ float ror_max(float v) { return fmaxf(v, dppf<0x120 + N>(v)); }
__device__ __forceinline__ float x16_add(float v) {
  const u32 b = __float_as_uint(v);
  auto p = __builtin_amdgcn_permlane16_swap(b, b, false, false);
  return __uint_as_float(p[0]) + __uint_as_float(p[1]);
}
__device__ __forceinline__ float x32_add(float v) {
  const u32 b = __float_as_uint(v);
  auto p = __builtin_amdgcn_permlane32_swap(b, b, false, false);
  return __uint_as_float(p[0]) + __uint_as_float(p[1]);
}
__device__ __forceinline__ float x16_max(float v) {
  const u32 b = __float_as_uint(v);
  auto p = __builtin_amdgcn_permlane16_swap(b, b, false, false);
  return fmaxf(__uint_as_float(p[0]), __uint_as_float(p[1]));
}
__device__ __forceinline__ float x32_max(float v) {
  const u32 b = __float_as_uint(v);
  auto p = __builtin_amdgcn_permlane32_swap(b, b, false, false);
  return fmaxf(__uint_as_float(p[0]), __uint_as_float(p[1]));
}
__device__ __forceinline__ float wave_sum(float v) {
  v = ror_add<8>(v); v = ror_add<4>(v); v = ror_add<2>(v); v = ror_add<1>(v);
  return x32_add(x16_add(v));
}
__device__ __forceinline__ float wave_max(float v) {
  v = ror_max<8>(v); v = ror_max<4>(v); v = ror_max<2>(v); v = ror_max<1>(v);
  return x32_max(x16_max(v));
}

__device__ __forceinline__ u32 swz(u32 row) { return ((row & 3u) << 2) | ((row >> 2) & 3u); }
__device__ __forceinline__ u32 off_b(u32 row, u32 ch) { return 256u * row + 16u * (ch ^ swz(row)); }

constexpr u32 L_RA = 0;
constexpr u32 L_UV = 65536;
constexpr u32 L_WO = 98304;
constexpr u32 L_WQ = L_WO;
constexpr u32 L_W1 = L_WO + 14336;
constexpr u32 L_W2 = 126976;
constexpr u32 L_XN = 134144;
constexpr u32 L_P = L_XN + 1792;
constexpr u32 L_QST = L_P;
constexpr u32 L_SRED = L_P;
constexpr u32 L_AT = L_P + 16896;
constexpr u32 L_WG = L_AT + 4608;
constexpr u32 L_VST = L_WG + 128;
constexpr u32 L_VH = L_VST + 128;
constexpr u32 L_FR = L_VH + 128;
constexpr u32 L_BI = L_FR + 256;
constexpr u32 L_FLAG = L_BI + 64;
constexpr u32 L_END = L_FLAG + 128;
static_assert(L_END <= 163840, "LDS");
constexpr u32 F_PROG = L_FLAG;
constexpr u32 F_COL = L_FLAG + 4;
constexpr u32 F_UK = L_FLAG + 36;
constexpr u32 F_RO = L_FLAG + 40;
constexpr u32 F_SR = L_FLAG + 44;
constexpr u32 F_B = L_FLAG + 48;
constexpr u32 F_QP = L_FLAG + 68;
constexpr u32 F_XN = L_FLAG + 72;
constexpr u32 F_QS = L_FLAG + 76;
constexpr u32 F_E1 = L_FLAG + 80;
constexpr u32 F_E2 = L_FLAG + 84;
constexpr u32 F_E3 = L_FLAG + 88;
constexpr u32 F_E0 = L_FLAG + 92;
#define SELFPRE 15
#define MLW 5
#define TLBW 0
#define EARLYQ 1
#define GPOS 1
#define GE1 8
#define GE2 8
#define GE3 0
#define LORD 0
constexpr int P_WQ = 14, P_UK = 46, P_ROWS = 110, P_UV = 142, P_W1 = 156, P_W2 = 163, P_WO = 177;
constexpr int P_END = 177;

#define WIN 16
#define WIN2 16
#define WR1 16
#define WR2 16
#define WSW F_QS
#define BFIRST 1
#define GSUC 0
#define WIN3 16
#define CLAG 99
#define GSTEP 2
#define MPOS 0
#define ROT 0
constexpr int WMAX0 = WR2 > WIN2 ? WIN2 + 9 : WIN2;
constexpr int WMAX = WMAX0 > WIN3 ? WMAX0 : WIN3;
static_assert(WR2 >= WR1 && WR2 <= 57 && WIN <= 46, "window");
#define AUX_COL 2
#define PSLP 2
#define QDLY 12
#define BDLY 28
#define CDLY 14
#define DDLY 12
#define FDLY 16
#define EDLY 0
#define QGATE 4
#define PSTR 128
#define PRE_AUX 0
#define PRE_SC 0
#define POUT 0
#define PRE_RD(off) __builtin_amdgcn_raw_buffer_load_b32(rw, (off), 0, PRE_AUX)
#define PRE 110
#define CPRIO 2
#define SPRIO 3
constexpr u32 LSPIN = 1u << 22;

#define SPIN_FAIL(code) if (++spins > SPIN_CAP) { if (lane == 0) __hip_atomic_store(tmo, (code), RLX_AGENT); break; } __builtin_amdgcn_s_sleep(PSLP); asm volatile("" ::: "memory");

#define POLL1(v, off, code) __builtin_amdgcn_s_setprio(0); _Pragma("unroll 1") for (u32 spins = 0;;) { v = __builtin_amdgcn_raw_buffer_load_b128(rw, (off), 0, 16); if (__all((v[1] == tag) & (v[3] == tag))) break; SPIN_FAIL(code) } __builtin_amdgcn_s_setprio(CPRIO);

#define POLLN(v, N, off, stride, code, ev) { u32 pend = (1u << (N)) - 1u; __builtin_amdgcn_s_setprio(0); _Pragma("unroll 1") for (u32 spins = 0;;) { \
    _Pragma("unroll") for (int s_ = 0; s_ < (N); ++s_) if (pend >> s_ & 1u) v[s_] = __builtin_amdgcn_raw_buffer_load_b128(rw, (off) + s_ * (stride), 0, 16); \
    if ((ev) && spins == 0) fl_add(lb + (ev), lane); \
    _Pragma("unroll") for (int s_ = 0; s_ < (N); ++s_) if (pend >> s_ & 1u) { if (__all((v[s_][1] == tag) & (v[s_][3] == tag))) pend &= ~(1u << s_); } \
    if (!pend) break; SPIN_FAIL(code) } __builtin_amdgcn_s_setprio(CPRIO); }

#define PIN(v) asm volatile("" : "+v"(v))
#define PISSUE(v, N, off, stride) _Pragma("unroll") for (int s_ = 0; s_ < (N); ++s_) v[s_] = __builtin_amdgcn_raw_buffer_load_b128(rw, (off) + ((s_ * (stride)) & 4095u), (s_ * (stride)) & ~4095u, 16);
#define PBAD(v) (((v)[1] ^ tag) | ((v)[3] ^ tag))
#define PRETRY(v, N, off, stride, code) { u32 pend = 0; \
    _Pragma("unroll") for (int s_ = 0; s_ < (N); ++s_) if (!__all(PBAD(v[s_]) == 0u)) pend |= 1u << s_; \
    __builtin_amdgcn_s_setprio(0); _Pragma("unroll 1") for (u32 spins = 0; pend;) { SPIN_FAIL(code) \
    _Pragma("unroll") for (int s_ = 0; s_ < (N); ++s_) if (pend >> s_ & 1u) v[s_] = __builtin_amdgcn_raw_buffer_load_b128(rw, (off) + ((s_ * (stride)) & 4095u), (s_ * (stride)) & ~4095u, 16); \
    _Pragma("unroll") for (int s_ = 0; s_ < (N); ++s_) if (pend >> s_ & 1u) { if (__all(PBAD(v[s_]) == 0u)) pend &= ~(1u << s_); } \
    } __builtin_amdgcn_s_setprio(CPRIO); }
#define PCHECK(v, N, off, stride, code) { u32 bad_ = 0; _Pragma("unroll") for (int s_ = 0; s_ < (N); ++s_) bad_ |= PBAD(v[s_]); \
    if (__builtin_expect(!__all(bad_ == 0u), 0)) PRETRY(v, N, off, stride, code) }

#define PLOAD(off) ({ asm volatile("" ::: "memory"); __builtin_amdgcn_raw_buffer_load_b128(rw, (off), 0, 16); })
#define PCHECK2(v, off, code, S) { v4u a_ = v[0]; if (S) __builtin_amdgcn_s_sleep(S); v4u b_ = PLOAD(off); u32 r0_, r2_; \
    _Pragma("unroll 1") for (u32 spins = 0;;) { \
      r0_ = a_[0]; r2_ = a_[2]; if (__all(PBAD(a_) == 0u)) break; \
      a_ = PLOAD(off); \
      r0_ = b_[0]; r2_ = b_[2]; if (__all(PBAD(b_) == 0u)) break; \
      b_ = PLOAD(off); \
      if (++spins > SPIN_CAP) { if (lane == 0) __hip_atomic_store(tmo, (code), RLX_AGENT); break; } } \
    v[0][0] = r0_; v[0][2] = r2_; }
#define P2D -1
#define P2F -1
#define P2E -1

#define DPPMAX(n) "s_nop 1\n v_max_f32_dpp %0, %0, %0 row_ror:" #n " row_mask:0xf bank_mask:0xf\n"
__device__ __forceinline__ void wave_max2(float& a, float& b, float& jn) {
  asm volatile("s_nop 1\n v_permlane32_swap_b32 %0, %1\n v_max_f32 %0, %0, %1\n v_mov_b32 %1, %0\n"
               "s_nop 1\n v_permlane16_swap_b32 %0, %1\n v_max_f32 %0, %0, %1\n"
               DPPMAX(8) DPPMAX(4) DPPMAX(2) DPPMAX(1)
               "v_mov_b32 %2, %0\n v_mov_b32 %1, %0\n s_nop 1\n v_permlane32_swap_b32 %0, %1\n s_nop 1"
               : "+v"(a), "+v"(b), "=&v"(jn));
}
__device__ __forceinline__ float row_max(float a) {
  asm volatile(DPPMAX(8) DPPMAX(4) DPPMAX(2) DPPMAX(1) "s_nop 1" : "+v"(a));
  return a;
}
__device__ __forceinline__ float j32_add(float a, float b) {
  auto p = __builtin_amdgcn_permlane32_swap(__float_as_uint(a), __float_as_uint(b), false, false);
  return __uint_as_float(p[0]) + __uint_as_float(p[1]);
}
__device__ __forceinline__ float j16_add(float a, float b) {
  auto p = __builtin_amdgcn_permlane16_swap(__float_as_uint(a), __float_as_uint(b), false, false);
  return __uint_as_float(p[0]) + __uint_as_float(p[1]);
}
typedef __attribute__((ext_vector_type(16))) float v16f;
#define COLNT " nt"
__device__ __forceinline__ v4u mk_rsrc(const void* p, u32 bytes) {
  const u64 a = (u64)(uintptr_t)p;
  return v4u{(u32)a, (u32)(a >> 32) & 0xffffu, bytes, 0x00020000u};
}
#define COL_REQ(V) "s_nop 0\n buffer_load_dwordx4 " V ", %2, %0 offen" COLNT " lds\n s_add_u32 m0, m0, 0x400\n s_add_u32 %0, %0, 0x2000\n"
__device__ __forceinline__ void col_reads(v4u rk4, u32 vk, u32 ck, u32 ld) {
  u32 t0, t1;
  asm volatile(
      "s_mov_b32 %1, m0\n s_mov_b32 m0, %8\n s_mov_b32 %0, %7\n"
      COL_REQ("%3") COL_REQ("%4") COL_REQ("%5") COL_REQ("%6") COL_REQ("%3") COL_REQ("%4") COL_REQ("%5") COL_REQ("%6")
      "s_mov_b32 m0, %1\n"
      : "=&s"(t0), "=&s"(t1)
      : "s"(rk4), "v"(vk), "v"(vk ^ 16u), "v"(vk ^ 32u), "v"(vk ^ 48u), "s"(ck), "s"(ld)
      : "memory", "scc");
}

__device__ __forceinline__ void st_gran(char* ws, u32 off, u32 tag, u32 val) {
  __hip_atomic_store((gu64*)(ws + off), ((u64)tag << 32) | val, RLX_AGENT);
}
#define ST2(off, a, b) __builtin_amdgcn_raw_buffer_store_b128(v4u{(a), tag, (b), tag}, rw, (off), 0, 16)
template <int CTRL> __device__ __forceinline__ u32 dppu(u32 v) { return (u32)__builtin_amdgcn_update_dpp(0, (int)v, CTRL, 0xf, 0xf, true); }
__device__ __forceinline__ bf8 tr_pair(char* lds, u32 a0, u32 a1) {
  const s4 b0 = __builtin_amdgcn_ds_read_tr16_b64_v4i16((LAS s4*)(lds + a0));
  const s4 b1 = __builtin_amdgcn_ds_read_tr16_b64_v4i16((LAS s4*)(lds + a1));
  const s8 bb = {b0[0], b0[1], b0[2], b0[3], b1[0], b1[1], b1[2], b1[3]};
  return __builtin_bit_cast(bf8, bb);
}
__device__ __forceinline__ v4u lds16(char* lds, u32 a) { return *reinterpret_cast<const v4u*>(lds + a); }
__device__ __forceinline__ v4f lds16f(char* lds, u32 a) { return *reinterpret_cast<const v4f*>(lds + a); }
__device__ __forceinline__ float fdiv(float a, float d, float r) {
  const float q = a * r;
  return fmaf(fmaf(-q, d, a), r, q);
}
__device__ __forceinline__ v4u frag(v4u v, int pa) {
  v4u o;
#pragma unroll
  for (int e = 0; e < 4; ++e) o[e] = (u32)__builtin_amdgcn_ds_bpermute(pa, (int)v[e]);
  return o;
}
__device__ __forceinline__ void gemv448(char* lds, u32 wt, int lane, float& a0, float& a1) {
  const u32 gq = lane >> 4, qq = (lane & 15) >> 2, pp = lane & 3;
  const u32 ao = L_XN + (u32)(lane & 1) * 896u + gq * 16u;
  const u32 bo = wt + (8u * gq + qq) * 32u + pp * 8u;
  v4f acc0 = {0.f, 0.f, 0.f, 0.f}, acc1 = {0.f, 0.f, 0.f, 0.f};
#pragma unroll
  for (int s = 0; s < 7; ++s) {
    acc0 = __builtin_amdgcn_mfma_f32_16x16x32_bf16(as_bf8(lds16(lds, ao + s * 64)), tr_pair(lds, bo + s * 1024, bo + s * 1024 + 128), acc0, 0, 0, 0);
    acc1 = __builtin_amdgcn_mfma_f32_16x16x32_bf16(as_bf8(lds16(lds, ao + (s + 7) * 64)), tr_pair(lds, bo + (s + 7) * 1024, bo + (s + 7) * 1024 + 128), acc1, 0, 0, 0);
  }
  a0 = acc0[0] + acc1[0]; a1 = acc0[1] + acc1[1];
}

__device__ __forceinline__ void fl_write(u32 a, u32 v) { asm volatile("ds_write_b32 %0, %1" :: "v"(a), "v"(v) : "memory"); }
__device__ __forceinline__ void fl_add(u32 a, int lane) { if (lane == 0) asm volatile("ds_add_u32 %0, %1" :: "v"(a), "v"(1u) : "memory"); }
__device__ __forceinline__ int fl_read(u32 a) {
  u32 v; asm volatile("ds_read_b32 %0, %1\n s_waitcnt lgkmcnt(0)" : "=v"(v) : "v"(a) : "memory");
  return (int)__builtin_amdgcn_readfirstlane(v);
}
__device__ __forceinline__ void fl_wait(u32 a, int v) {
#pragma unroll 1
  for (u32 n = 0; n < LSPIN; ++n) { if (fl_read(a) >= v) break; __builtin_amdgcn_s_sleep(0); }
}
__device__ __forceinline__ void sbar(u32 a, int lane) { fl_add(a, lane); fl_wait(a, 8); }
template <int K> __device__ __forceinline__ void drain(u32 fp, int q, int lo) {
  asm volatile("s_waitcnt vmcnt(%0)" :: "n"(K) : "memory");
  fl_write(fp, (u32)(q - K > lo ? q - K : lo));
  if constexpr (K > 0) drain<K - 1>(fp, q, lo);
}
template <int K, int TO> __device__ __forceinline__ void drain8(u32 fp, int q, int& lo) {
  if constexpr (K >= TO) {
    asm volatile("s_waitcnt vmcnt(%0)" :: "n"(K) : "memory");
    lo = q - K > lo ? q - K : lo;
    fl_write(fp, (u32)lo);
    drain8<K - 8, TO>(fp, q, lo);
  }
}
template <int K> __device__ __forceinline__ bool gdrain(u32 fp, int q, u32 a, int v, int& lo) {
  if (q - K > lo) {
    if (fl_read(a) >= v) return true;
    asm volatile("s_waitcnt vmcnt(%0)" :: "n"(K) : "memory");
    lo = q - K;
    fl_write(fp, (u32)lo);
  }
  if constexpr (K >= GSTEP) return gdrain<K - GSTEP>(fp, q, a, v, lo);
  else return false;
}
__device__ __forceinline__ void gate(u32 fp, int q, u32 a, int v, int& lo) {
  if (gdrain<(WMAX - 1) / GSTEP * GSTEP>(fp, q, a, v, lo)) return;
  fl_wait(a, v);
}
template <int AUX> __device__ __forceinline__ void dma(v4u rs, u32 vo, u32 so, u32 ld) {
  u32 t0;
  if constexpr (AUX == 2)
    asm volatile("s_mov_b32 %0, m0\n s_mov_b32 m0, %4\n s_nop 0\n buffer_load_dwordx4 %1, %2, %3 offen nt lds\n s_mov_b32 m0, %0"
                 : "=&s"(t0) : "v"(vo), "s"(rs), "s"(so), "s"(ld) : "memory");
  else
    asm volatile("s_mov_b32 %0, m0\n s_mov_b32 m0, %4\n s_nop 0\n buffer_load_dwordx4 %1, %2, %3 offen lds\n s_mov_b32 m0, %0"
                 : "=&s"(t0) : "v"(vo), "s"(rs), "s"(so), "s"(ld) : "memory");
}
template <int N, int U, int XM, int SEG, int AUX, int W>
__device__ __forceinline__ void stream(char* lds, u32 lb, const void* base, u32 bytes, u32 vb, u32 sb, u32 ss, u32 ld, int q0, int& lo) {
  const v4u rs = mk_rsrc(base, bytes);
#pragma unroll 1
  for (u32 k = 0; k < (u32)(N / U); ++k) {
    const int qk = q0 + (int)(k * U);
    const u32 sk = SEG == 1 ? sb + (k < 3u ? k + 1u : k == 3u ? 0u : k) * 256u : sb + k * (U * ss);
#pragma unroll
    for (u32 n = 0; n < (u32)U; ++n) {
      const int q = qk + (int)n;
      asm volatile("s_waitcnt vmcnt(%0)" :: "n"(W - 1) : "memory");
      lo = q - (W - 1) > lo ? q - (W - 1) : lo;
      fl_write(lb + F_PROG, (u32)lo);
      const u32 xm = XM == 1 ? (n & 3u) << 4 : XM == 2 ? ((2u * n) & 15u) << 4 : XM == 3 ? (((n >> 2) & 1u) << 3) << 4 : 0u;
      if (XM == 4) {
        const u32 c = vb + (k * U + n) * 64u, rr = (c * 2341u) >> 16;
        dma<AUX>(rs, c * 16u + rr * 448u, sb, lb + ld + k * (U * 1024u) + n * 1024u);
      } else
      dma<AUX>(rs, vb ^ xm, sk + n * ss, lb + ld + k * (U * 1024u) + n * 1024u);
    }
  }
}

template <int W>
__device__ __forceinline__ void row_chunk(char* lds, u32 lb, v4u rs, u32 vb, u32 sk, u32 k, int& lo) {
#pragma unroll
  for (u32 n = 0; n < 8u; ++n) {
    const int q = P_UK + (int)(k * 8u + n);
    asm volatile("s_waitcnt vmcnt(%0)" :: "n"(W - 1) : "memory");
    lo = q - (W - 1) > lo ? q - (W - 1) : lo;
    fl_write(lb + F_PROG, (u32)lo);
    dma<0>(rs, vb ^ ((n & 3u) << 4), sk + n * 8192u, lb + L_RA + k * 8192u + n * 1024u);
  }
}

#define MLP_BLOCK { \
      float a0, a1; \
      gemv448(lds, L_W1, lane, a0, a1); \
      const float hv = rbf(rbf(lane >= 32 ? a1 : a0) + *reinterpret_cast<const float*>(lds + L_BI + (lane & 15) * 4)); \
      const float ge = 0.5f * hv * (1.f + tanhf(0.7978845608028654f * (hv + 0.044715f * hv * hv * hv))); \
      *reinterpret_cast<u16*>(lds + L_VH + lane * 2) = ((lane & 16) == 0 && hd < 112) ? f2bf(ge) : (u16)0; \
      asm volatile("s_waitcnt lgkmcnt(0)" ::: "memory"); }

__global__ __launch_bounds__(576) void mla_decode(const u16* x, const u16* ckv, const u16* wq, const u16* wuk,
                                                  const u16* wuv, const u16* wo, const u16* w1, const u16* b1,
                                                  const u16* w2, const u16* b2, char* ws, u16* out) {
  __shared__ __attribute__((aligned(16))) char lds[L_END];
  const int t = threadIdx.x, lane = t & 63;
  const int w = __builtin_amdgcn_readfirstlane(t >> 6);
  const int i = blockIdx.x, xcd = i & 7, r = i >> 3;
  const int h1i = xcd * 2 + (r >> 4), cs = r & 15;
  const int G = xcd * 4 + (r >> 3), b = G >> 4, g = G & 15, j = r & 7;
  const int ch = r & 1, hd = xcd * 16 + (r >> 1), ds = hd & 7;
  const int fsl = hd < 112 ? hd : 0;
  STAMP_DECL
  STAMP(0);
  asm volatile("" : "+s"(x), "+s"(ckv), "+s"(wq), "+s"(wuk), "+s"(wuv), "+s"(wo), "+s"(w1), "+s"(b1), "+s"(w2), "+s"(b2), "+s"(ws), "+s"(out));
  gu32* tmo = (gu32*)(ws + WS_EP + 128);
  const __amdgpu_buffer_rsrc_t rw = __builtin_amdgcn_make_buffer_rsrc(ws, 0, WS_END, 0x00020000);
  const u32 gq = lane >> 4, qq = (lane & 15) >> 2, pp = lane & 3;

  const u32 lb = (u32)(uintptr_t)(LAS char*)lds;

  if (w == 8) {
    __builtin_amdgcn_s_setprio(SPRIO);
    if (lane < 32) fl_write(lb + L_FLAG + lane * 4, 0u);
    if (!EARLYQ) BAR();
    int lo = 0;
    const u32 hb = (u32)lane >> 5;
    const u32 vk = gq * 2048u + (((u32)lane & 15u) ^ (gq << 2)) * 16u;
    stream<14, 7, 0, 0, 0, WIN>(lds, lb, wq, M_ * 8192u, (u32)(lane >> 3) * 8192u + (((u32)lane & 7u) ^ ((((u32)lane >> 3) & 3u) << 1)) * 16u, (u32)(cs & 3) * (112u * 8192u) + (u32)h1i * 512u + (u32)(cs >> 2) * 128u, 8u * 8192u, L_WQ, 0, lo);
    if (EARLYQ) BAR();
    stream<32, 8, 2, 0, 2, WIN>(lds, lb, wuk, 1024u * 8192u, hb * 8192u + (((u32)lane & 31u) ^ hb) * 16u, (u32)(cs * 64) * 8192u + (u32)h1i * 512u, 16384u, L_UV, P_UK - 32, lo);
    STAMP(24);
    if (WIN > 32) drain8<32, 32>(lb + F_PROG, P_UK, lo);
    {
      const v4u rs = mk_rsrc(ckv, 2u * SK * DC * 2u);
      const u32 sb = (u32)(b * SK + g * 256 + j * 32) * 2048u;
      bool big = WR1 == WR2;
#pragma unroll 1
      for (u32 k = 0; k < 8u; ++k) {
        const u32 sk = sb + ((k + (u32)j + 1u) & 7u) * 256u;
        if (!big) big = fl_read(lb + WSW) >= 4;
        if (big) row_chunk<WR2>(lds, lb, rs, vk, sk, k, lo); else row_chunk<WR1>(lds, lb, rs, vk, sk, k, lo);
      }
      drain8<(WR2 - 1) / 8 * 8, WIN2>(lb + F_PROG, P_ROWS, lo);
    }
    STAMP(25);
    if (GE1) gate(lb + F_PROG, P_ROWS, lb + F_E1, GE1, lo);
    STAMP(30);
    gate(lb + F_PROG, P_UV - 32, lb + F_UK, 4, lo);
    if (GPOS == 0) gate(lb + F_PROG, P_ROWS, lb + (GSUC ? F_COL : F_E2), 8, lo);
    stream<32, 8, 3, 0, 2, WIN2>(lds, lb, wuv, 1024u * 8192u, hb * 8192u + (((u32)lane & 31u) ^ (hb << 2)) * 16u, (u32)(cs * 64) * 8192u + (u32)h1i * 512u, 16384u, L_UV, P_UV - 32, lo);
    STAMP(26);
    if (GPOS == 1) gate(lb + F_PROG, P_UV, lb + (GSUC ? F_COL : F_E2), 8, lo);
    STAMP(31);
    stream<14, 7, 0, 0, 0, WIN3>(lds, lb, w1, M_ * F_ * 2u, (u32)(lane >> 1) * (F_ * 2u) + (u32)(lane & 1) * 16u, (u32)fsl * 32u, 32u * F_ * 2u, L_W1, P_W1 - 14, lo);
    STAMP(27);
    if (GPOS == 2) gate(lb + F_PROG, P_W1, lb + (GSUC ? F_COL : F_E2), 8, lo);
    if (GE3) gate(lb + F_PROG, P_W1, lb + F_E3, GE3, lo);
    stream<7, 7, 4, 0, 2, WIN3>(lds, lb, w2, 1792u * 896u, (u32)lane, (u32)(fsl * 16) * 896u + (u32)ch * 448u, 0u, L_W2, P_W2 - 7, lo);
    gate(lb + F_PROG, P_W2, lb + F_RO, 4, lo);
    stream<14, 7, 4, 0, 2, WIN3>(lds, lb, wo, 4096u * 896u, (u32)lane, (u32)(hd * 32) * 896u + (u32)ch * 448u, 0u, L_WO, P_W2, lo);
    STAMP(28);
    if (WIN3 > P_END - P_W1) drain8<P_END - P_UV, P_END - P_UV>(lb + F_PROG, P_END, lo);
    asm volatile("s_waitcnt vmcnt(%0)" :: "n"(P_END - P_W1) : "memory");
    MLP_BLOCK
    drain<0>(lb + F_PROG, P_END, lo);
    STAMP(29);
    STAMP_FLUSH;
    return;
  }

  __builtin_amdgcn_s_setprio(CPRIO);
  const u32 epv = __builtin_amdgcn_raw_buffer_load_b32(rw, WS_EP, 0, 0);
  const int pa = (4 * (lane & 15) + (lane >> 4)) * 4;
  u32 tag, pre0 = 0;
  if (w == 0) {
    const int lc = lane < 56 ? lane : 0;
    const v4u xr0 = ldp16(x + lc * 8), xr1 = ldp16(x + M_ + lc * 8);
    STAMP(21);
    BAR();
    float xa[8], xb[8];
#pragma unroll
    for (int p = 0; p < 4; ++p) { xa[2 * p] = lo_f(xr0[p]); xa[2 * p + 1] = hi_f(xr0[p]); xb[2 * p] = lo_f(xr1[p]); xb[2 * p + 1] = hi_f(xr1[p]); }
    const float act = lane < 56 ? 1.f : 0.f;
    float s0 = 0.f, s1 = 0.f;
#pragma unroll
    for (int k = 0; k < 8; ++k) { s0 += xa[k]; s1 += xb[k]; }
    s0 = wave_sum(s0 * act); s1 = wave_sum(s1 * act);
    const float m0 = s0 * (1.f / M_), m1 = s1 * (1.f / M_);
    float v0 = 0.f, v1 = 0.f;
#pragma unroll
    for (int k = 0; k < 8; ++k) { const float d0 = xa[k] - m0, d1 = xb[k] - m1; v0 += d0 * d0; v1 += d1 * d1; }
    v0 = wave_sum(v0 * act); v1 = wave_sum(v1 * act);
    const float mb0 = rbf(m0), mb1 = rbf(m1);
    const float den0 = rbf(sqrtf(rbf(rbf(v0 * (1.f / M_)) + 1e-5f)));
    const float den1 = rbf(sqrtf(rbf(rbf(v1 * (1.f / M_)) + 1e-5f)));
    const float r0 = __builtin_amdgcn_rcpf(den0), r1 = __builtin_amdgcn_rcpf(den1);
    v4u na, nb;
#pragma unroll
    for (int p = 0; p < 4; ++p) {
      na[p] = pk2(fdiv(rbf(xa[2 * p] - mb0), den0, r0), fdiv(rbf(xa[2 * p + 1] - mb0), den0, r0));
      nb[p] = pk2(fdiv(rbf(xb[2 * p] - mb1), den1, r1), fdiv(rbf(xb[2 * p + 1] - mb1), den1, r1));
    }
    if (lane < 56) {
      *reinterpret_cast<v4u*>(lds + L_XN + lane * 16) = na;
      *reinterpret_cast<v4u*>(lds + L_XN + 896 + lane * 16) = nb;
    }
    fl_add(lb + F_XN, lane);
    STAMP(17);
  } else if (w < 4) {
    float bias = 0.f;
    if (w == 1) bias = bf2f(ldh(b1 + fsl * 16 + (lane & 15)));
    if (w == 3) pre0 = __builtin_amdgcn_raw_buffer_load_b32(rw, lane < 8 ? WS_QL + (u32)(lane >> 2) * 65536u + (u32)(cs * 4 + (lane & 3)) * 1024u + (u32)h1i * 64u : WS_QG + (u32)h1i * 16384u + (u32)(cs & 3) * 4096u + (u32)(cs >> 2) * 1024u + (u32)(lane & 7) * 128u, 0, PRE_AUX);
    BAR();
    if (w == 1 && lane < 16) *reinterpret_cast<float*>(lds + L_BI + lane * 4) = bias;
    fl_wait(lb + F_XN, 1);
  }
  if (w < 4) {
    fl_wait(lb + F_PROG, P_WQ);
    STAMP(18);
    const u32 ao = L_XN + (u32)(lane & 1) * 896u + (u32)(cs & 3) * 224u;
    const u32 ch = ((((u32)w * 2u + (pp >> 1)) ^ (qq << 1)) * 16u) + (pp & 1u) * 8u;
    const u32 bo = L_WQ + (8u * gq + qq) * 128u + ch;
    v4f acc0 = {0.f, 0.f, 0.f, 0.f}, acc1 = {0.f, 0.f, 0.f, 0.f};
    acc0 = __builtin_amdgcn_mfma_f32_16x16x32_bf16(as_bf8(lds16(lds, ao + gq * 16u)), tr_pair(lds, bo, bo + 512), acc0, 0, 0, 0);
    acc1 = __builtin_amdgcn_mfma_f32_16x16x32_bf16(as_bf8(lds16(lds, ao + gq * 16u + 64)), tr_pair(lds, bo + 4096, bo + 4096 + 512), acc1, 0, 0, 0);
    acc0 = __builtin_amdgcn_mfma_f32_16x16x32_bf16(as_bf8(lds16(lds, ao + gq * 16u + 128)), tr_pair(lds, bo + 8192, bo + 8192 + 512), acc0, 0, 0, 0);
    acc1 = __builtin_amdgcn_mfma_f32_16x16x16bf16_1k(*reinterpret_cast<const s4*>(lds + ao + 192u + gq * 8u),
                                                     __builtin_amdgcn_ds_read_tr16_b64_v4i16((LAS s4*)(lds + L_WQ + (96u + 4u * gq + qq) * 128u + ch)), acc1, 0, 0, 0);
    tag = __builtin_amdgcn_readfirstlane(epv) + 1u;
    {
      const u32 qv = __float_as_uint(gq ? acc0[1] + acc1[1] : acc0[0] + acc1[0]), qn = dppu<0xB1>(qv);
      if ((lane & 33) == 0) ST2(WS_QG + (u32)h1i * 16384u + (u32)(cs & 3) * 4096u + (u32)(cs >> 2) * 1024u + (u32)w * 256u + (u32)lane * 8u, qv, qn);
    }
    STAMP(1);
    fl_add(lb + F_RO, lane);
    asm volatile("" :: "v"(pre0));
    if (w == 3) pre0 = PRE_RD(lane < 32 ? WS_SC + (u32)((G * 16 + (lane >> 1)) * 256 + j * 32) * 8u + (u32)(lane & 1) * 128u : WS_ML + (u32)((b * 16 + 2 * j + (lane & 1)) * 16 + (g & 8)) * 16u);
  } else {
    v4u wk[8];
    BAR();
    tag = __builtin_amdgcn_readfirstlane(epv) + 1u;
    u32 qo = WS_QG + (u32)h1i * 16384u + (u32)(w - 4) * 1024u + (u32)lane * 16u;
    u32 qw = L_QST + (u32)((lane >> 3) & 1) * 512u + (u32)(w - 4) * 128u + gq * 32u + (u32)(lane & 7) * 4u;
    u32 ao = L_QST + (u32)(lane & 1) * 512u + gq * 16u;
    const int bb = lane & 1, wq4 = w - 4;
    u32 qa = WS_QL + (u32)bb * 65536u + (u32)((cs * 2 + (wq4 >> 1)) * 2 + ((lane >> 2) & 1)) * 1024u + (u32)h1i * 64u
             + (u32)((wq4 & 1) * 2 + ((lane >> 3) & 1)) * 16u;
    PIN(qo); PIN(qw); PIN(ao); PIN(qa);
    fl_wait(lb + F_RO, QGATE);
    if (QDLY) __builtin_amdgcn_s_sleep(QDLY);
    STAMP(19);
    v4u ga[4];
    PISSUE(ga, 4, qo, 4096u)
    fl_add(lb + F_E0, lane);
    fl_wait(lb + F_PROG, P_UK);
    {
      const u32 ko = L_UV + (u32)((w - 4) * 16 + (lane & 15)) * 512u;
#pragma unroll
      for (int s = 0; s < 8; ++s) wk[s] = lds16(lds, ko + ((((u32)s * 4u + gq) ^ ((u32)lane & 15u)) * 16u));
    }
    fl_add(lb + F_UK, lane);
    PCHECK(ga, 4, qo, 4096u, 1u)
    {
      const float q0 = (__uint_as_float(ga[0][0]) + __uint_as_float(ga[1][0])) + (__uint_as_float(ga[2][0]) + __uint_as_float(ga[3][0]));
      const float q1 = (__uint_as_float(ga[0][2]) + __uint_as_float(ga[1][2])) + (__uint_as_float(ga[2][2]) + __uint_as_float(ga[3][2]));
      *reinterpret_cast<u32*>(lds + qw) = pk2(q0, q1);
    }
    fl_add(lb + F_QS, lane);
    STAMP(20);
    fl_wait(lb + F_QS, 4);
    v4f acc0 = {0.f, 0.f, 0.f, 0.f}, acc1 = {0.f, 0.f, 0.f, 0.f};
#pragma unroll
    for (int s = 0; s < 4; ++s) {
      acc0 = __builtin_amdgcn_mfma_f32_16x16x32_bf16(as_bf8(lds16(lds, ao + s * 64)), as_bf8(wk[s]), acc0, 0, 0, 0);
      acc1 = __builtin_amdgcn_mfma_f32_16x16x32_bf16(as_bf8(lds16(lds, ao + (s + 4) * 64)), as_bf8(wk[s + 4]), acc1, 0, 0, 0);
    }
    const float a0 = acc0[0] + acc1[0], a1 = acc0[1] + acc1[1];
    const float own = bb ? a1 : a0, oth = dppf<0xB1>(bb ? a0 : a1);
    {
      const u32 qv = bb ? pk2(oth, own) : pk2(own, oth), qn = dppu<0x4E>(qv);
      if ((lane & 50) == 0) ST2(qa, qv, qn);
    }
    STAMP(2);
    if (BDLY) __builtin_amdgcn_s_sleep(BDLY);
    fl_add(lb + F_QP, lane);
  }
  v4u bv[8];
  const int slot = w == 0 ? 3 : w < 4 ? w - 1 : w;
  const u32 colk = (u32)(b * SK + g * 256 + ((j - 1 - slot) & 7) * 32) * 2048u + (u32)j * 256u;
  u32 so = WS_SC + (u32)((G * 16 + 2 * w) * 256 + 2 * lane) * 8u;
  {
    u32 qo = WS_QL + (u32)b * 65536u + (u32)((slot + j + 1) & 7) * 8192u + (u32)lane * 16u;
    u32 srw = L_SRED + (u32)((w * 16 + (int)gq * 4) * 33 + (lane & 15)) * 4u;
    u32 srr = L_SRED + (u32)((t >> 5) * 33 + (t & 31)) * 4u;
    u32 sco = WS_SC + (u32)((G * 16 + (t >> 5)) * 256 + j * 32 + (t & 31)) * 8u;
    int pav = pa;
    PIN(qo); PIN(srw); PIN(srr); PIN(sco); PIN(so); PIN(pav);
    v4u gl[8];
    bool cpend = slot != 7;
    const u32 key = (u32)lane & 15u;
    const u32 ro = L_RA + (u32)slot * 8192u + key * 256u;
    fl_wait(lb + F_QP, 4);
    const int prg = fl_read(lb + F_PROG);
    const bool have = prg >= P_UK + 8 * (slot + 1);
    if (have) {
#pragma unroll
      for (int s = 0; s < 8; ++s) bv[s] = lds16(lds, ro + (u32)(s >> 2) * 4096u + ((((u32)(s & 3) * 4u + gq) ^ swz(key)) * 16u));
    }
    PISSUE(gl, 8, qo, 1024u)
    fl_add(lb + F_E1, lane);
    v4u av[4];
#pragma unroll 1
    for (int pass = 0;; ++pass) {
      u32 bad = 0;
#pragma unroll
      for (int s = 0; s < 4; ++s) {
        bad |= PBAD(gl[2 * s]) | PBAD(gl[2 * s + 1]);
        av[s] = frag(v4u{gl[2 * s][0], gl[2 * s][2], gl[2 * s + 1][0], gl[2 * s + 1][2]}, pav);
      }
      if (__builtin_expect(pass || __all(bad == 0u), 1)) break;
      PRETRY(gl, 8, qo, 1024u, 2u)
    }
    STAMP(3);
    if (!have) {
      fl_wait(lb + F_PROG, P_UK + 8 * (slot + 1));
#pragma unroll
      for (int s = 0; s < 8; ++s) bv[s] = lds16(lds, ro + (u32)(s >> 2) * 4096u + ((((u32)(s & 3) * 4u + gq) ^ swz(key)) * 16u));
    }
    v4f acc0 = {0.f, 0.f, 0.f, 0.f}, acc1 = {0.f, 0.f, 0.f, 0.f};
#pragma unroll
    for (int s = 0; s < 4; ++s) {
      acc0 = __builtin_amdgcn_mfma_f32_16x16x32_bf16(as_bf8(av[s]), as_bf8(bv[s]), acc0, 0, 0, 0);
      acc1 = __builtin_amdgcn_mfma_f32_16x16x32_bf16(as_bf8(av[s]), as_bf8(bv[4 + s]), acc1, 0, 0, 0);
    }
    asm volatile("" :: "v"(pre0));
    STAMP(4);
    float* sr = reinterpret_cast<float*>(lds + srw);
#pragma unroll
    for (int q = 0; q < 4; ++q) { sr[q * 33] = acc0[q]; sr[q * 33 + 16] = acc1[q]; }
    sbar(lb + F_B, lane);
    STAMP(5);
    const float* sp = reinterpret_cast<const float*>(lds + srr);
    float s = ((sp[0] + sp[528]) + (sp[2 * 528] + sp[3 * 528])) + ((sp[4 * 528] + sp[5 * 528]) + (sp[6 * 528] + sp[7 * 528]));
    s *= 0.0625f * 1.4426950408889634f;
    const u32 sn = dppu<0xB1>(__float_as_uint(s));
    if ((lane & 1) == 0) ST2(sco, __float_as_uint(s), sn);
    fl_add(lb + F_SR, lane);
    if (cpend)
    col_reads(mk_rsrc(ckv, 2u * SK * DC * 2u), gq * 2048u + (((u32)lane & 15u) ^ (gq << 2)) * 16u, colk, lb + L_RA + (u32)slot * 8192u);
    if (w == 3) pre0 = PRE_RD(WS_PV + (u32)(((b * 16 + (lane >> 2)) * 16 + g) * 512 + j * 64) * 8u + (u32)(lane & 3) * 128u);
  }
  const u32 bq = (u32)w >> 2;
  u32 pvo = WS_PV + (((bq * 16u + (u32)h1i) * 16u + (u32)(w & 3) * 4u + gq) * 512u + (u32)cs * 32u + (u32)(lane & 15) * 2u) * 8u;
  {
    const u32 ps = ((u32)j - 1u - gq) & 7u;
    u32 pw = L_P + (u32)(2 * w) * 528u + ps * 64u + (u32)(lane & 15) * 4u;
    u32 pw1 = L_P + (u32)(2 * w) * 528u + (ps ^ 4u) * 64u + (u32)(lane & 15) * 4u;
    u32 po = L_P + (u32)(lane & 15) * 528u + gq * 16u;
    u32 k0 = L_RA + off_b(8 * gq + qq, 2 * w + (pp >> 1)) + 8 * (pp & 1);
    u32 k1 = L_RA + off_b(8 * gq + 4 + qq, 2 * w + (pp >> 1)) + 8 * (pp & 1);
    const int hi8 = (lane >> 3) & 1, od = lane & 1;
    u32 go = WS_PV + (u32)(((b * 16 + (lane >> 4) * 4 + 2 * hi8 + od) * 16 + g) * 512 + j * 64 + w * 8 + (lane & 6)) * 8u;
    PIN(pw); PIN(pw1); PIN(po); PIN(k0); PIN(k1); PIN(go); PIN(pvo);
    v4u gs[4];
    bf8 kb[8];
    if (CDLY) __builtin_amdgcn_s_sleep(CDLY);
    PISSUE(gs, 4, so, 1024u)
    fl_add(lb + F_E2, lane);
    fl_wait(lb + F_SR, 8);
    PCHECK(gs, 4, so, 1024u, 3u)
    asm volatile("s_waitcnt vmcnt(0)" ::: "memory");
    fl_add(lb + F_COL, lane);
    asm volatile("" :: "v"(pre0));
    STAMP(6);
    float sv[8];
#pragma unroll
    for (int q = 0; q < 4; ++q) { sv[2 * q] = __uint_as_float(gs[q][0]); sv[2 * q + 1] = __uint_as_float(gs[q][2]); }
    float m0 = fmaxf(fmaxf(sv[0], sv[1]), fmaxf(sv[2], sv[3])), m1 = fmaxf(fmaxf(sv[4], sv[5]), fmaxf(sv[6], sv[7])), mj;
    wave_max2(m0, m1, mj);
    u32 pk[4];
#pragma unroll
    for (int q = 0; q < 4; ++q) {
      const float m = q < 2 ? m0 : m1;
      pk[q] = pk2(__builtin_amdgcn_exp2f(sv[2 * q] - m), __builtin_amdgcn_exp2f(sv[2 * q + 1] - m));
      *reinterpret_cast<u32*>(lds + ((q & 1) ? pw1 : pw) + (q >> 1) * 528) = pk[q];
    }
    fl_add(lb + F_B + 4, lane);
    if (w == j) {
      float l = j32_add((lo_f(pk[0]) + hi_f(pk[0])) + (lo_f(pk[1]) + hi_f(pk[1])), (lo_f(pk[2]) + hi_f(pk[2])) + (lo_f(pk[3]) + hi_f(pk[3])));
      l = x16_add(l);
      l = ror_add<8>(l); l = ror_add<4>(l); l = ror_add<2>(l); l = ror_add<1>(l);
      if ((lane & 31) == 0) ST2(WS_ML + (u32)((b * 16 + 2 * w + (lane >> 5)) * 16 + g) * 16u, __float_as_uint(mj), __float_as_uint(l));
    }
    fl_wait(lb + F_COL, 8);
#pragma unroll
    for (int ks = 0; ks < 8; ++ks) kb[ks] = tr_pair(lds, k0 + ks * 8192, k1 + ks * 8192);
    STAMP(22);
    fl_wait(lb + F_B + 4, 8);
    STAMP(7);
    v4f acc0 = {0.f, 0.f, 0.f, 0.f}, acc1 = {0.f, 0.f, 0.f, 0.f};
#pragma unroll
    for (int ks = 0; ks < 4; ++ks) {
      acc0 = __builtin_amdgcn_mfma_f32_16x16x32_bf16(as_bf8(lds16(lds, po + ks * 64)), kb[ks], acc0, 0, 0, 0);
      acc1 = __builtin_amdgcn_mfma_f32_16x16x32_bf16(as_bf8(lds16(lds, po + (ks + 4) * 64)), kb[ks + 4], acc1, 0, 0, 0);
    }
    const v4f acc = acc0 + acc1;
    const float s0 = hi8 ? acc[0] : acc[2], s1 = hi8 ? acc[1] : acc[3];
    const float n0 = dppf<0x128>(s0), n1 = dppf<0x128>(s1);
    const u32 g0 = hi8 ? pk2(n0, acc[2]) : pk2(acc[0], n0);
    const u32 g1 = hi8 ? pk2(n1, acc[3]) : pk2(acc[1], n1);
    const u32 gn = dppu<0xB1>(od ? g0 : g1);
    ST2(go, od ? gn : g0, od ? g1 : gn);
  }
  if (w == 3) pre0 = PRE_RD(lane < 32 ? WS_CP + ((u32)(h1i * 16 + (lane >> 1)) * 16u + (u32)cs) * 256u + (u32)(lane & 1) * 128u
                                      : WS_OG + (u32)(((lane >> 4) & 1) * 128 + hd) * 3584u + (u32)ch * 1792u + (u32)((lane & 15) < 14 ? (lane & 15) : 0) * 128u);
  STAMP(8);
  u32 cvo = WS_CP + (((u32)h1i * 2u + bq) * 8u + (u32)ds) * 4096u + (u32)(lane >> 2) * 256u + (u32)(w & 3) * 64u + pp * 16u;
  {
    u32 aw = L_AT + (bq * 16u + (u32)(w & 3) * 4u + gq) * 144u + (u32)(lane & 15) * 8u;
    u32 ar = L_AT + (u32)(lane & 31) * 144u + (u32)(lane >> 5) * 16u;
    u32 wr = L_WG + (u32)(lane >> 5) * 16u;
    u32 bo = L_UV + ((gq >> 1) * 4u + (qq >> 1) + 8u * (qq & 1u)) * 512u
             + ((((u32)w * 4u + (gq & 1u) * 2u + (pp >> 1)) ^ (((qq >> 1) << 2) | ((qq & 1u) << 3))) * 16u) + (pp & 1u) * 8u;
    u32 co = WS_CP + ((((u32)h1i * 2u + (u32)(lane >> 5)) * 8u + (u32)w) * 16u + (u32)cs) * 256u + (u32)(lane & 31) * 8u;
    u32 mlo = WS_ML + (((gq & 1u) * 16u + (u32)h1i) * 16u + (u32)(lane & 15)) * 16u;
    PIN(aw); PIN(ar); PIN(wr); PIN(bo); PIN(co); PIN(mlo); PIN(cvo);
    v4u pv[1], ml[1];
    bf8 ub[4];
    if (DDLY) __builtin_amdgcn_s_sleep(DDLY);
    if (w == MLW) { PISSUE(ml, 1, mlo, 0u) }
    PISSUE(pv, 1, pvo, 0u)
    fl_add(lb + F_E3, lane);
    const bool have = fl_read(lb + F_PROG) >= P_UV;
    if (have) {
#pragma unroll
      for (int T = 0; T < 4; ++T) ub[T] = tr_pair(lds, bo + T * 8192, bo + T * 8192 + 1024);
    }
    if (w == MLW) {
      PCHECK(ml, 1, mlo, 0u, 7u)
      const float mg = __uint_as_float(ml[0][0]);
      const float eg = __builtin_amdgcn_exp2f(mg - row_max(mg));
      float ls = eg * __uint_as_float(ml[0][2]);
      ls = ror_add<8>(ls); ls = ror_add<4>(ls); ls = ror_add<2>(ls); ls = ror_add<1>(ls);
      const float rl = __builtin_amdgcn_rcpf(ls);
      *reinterpret_cast<float*>(lds + L_WG + (lane & 31) * 4) = fdiv(eg, ls, rl);
    }
    PCHECK(pv, 1, pvo, 0u, 4u)
    asm volatile("" :: "v"(pre0));
    STAMP(9);
    *reinterpret_cast<v2u*>(lds + aw) = v2u{pv[0][0], pv[0][2]};
    fl_add(lb + F_B + 8, lane);
    if (!have) {
      fl_wait(lb + F_PROG, P_UV);
#pragma unroll
      for (int T = 0; T < 4; ++T) ub[T] = tr_pair(lds, bo + T * 8192, bo + T * 8192 + 1024);
    }
    STAMP(23);
    fl_wait(lb + F_B + 8, 8);
    STAMP(10);
    v16f acc;
#pragma unroll
    for (int q = 0; q < 16; ++q) acc[q] = 0.f;
    v4u at[4];
#pragma unroll
    for (int T = 0; T < 4; ++T) at[T] = lds16(lds, ar + T * 32);
    v4f wt[4];
#pragma unroll
    for (int q = 0; q < 4; ++q) wt[q] = lds16f(lds, wr + q * 32);
#pragma unroll
    for (int T = 0; T < 4; ++T) acc = __builtin_amdgcn_mfma_f32_32x32x16_bf16(as_bf8(at[T]), ub[T], acc, 0, 0, 0);
    float c[2];
#pragma unroll
    for (int e = 0; e < 2; ++e) {
      float u0 = wt[2 * e][0] * acc[8 * e], u1 = wt[2 * e][1] * acc[8 * e + 1];
      u0 = fmaf(wt[2 * e][2], acc[8 * e + 2], u0); u1 = fmaf(wt[2 * e][3], acc[8 * e + 3], u1);
      u0 = fmaf(wt[2 * e + 1][0], acc[8 * e + 4], u0); u1 = fmaf(wt[2 * e + 1][1], acc[8 * e + 5], u1);
      u0 = fmaf(wt[2 * e + 1][2], acc[8 * e + 6], u0); u1 = fmaf(wt[2 * e + 1][3], acc[8 * e + 7], u1);
      c[e] = u0 + u1;
    }
    const u32 cv = __float_as_uint(j32_add(c[0], c[1])), cn = dppu<0xB1>(cv);
    if ((lane & 1) == 0) ST2(co, cv, cn);
  }
  STAMP(11);
  const int fb = i >= 56 ? 1 : 0, fc = i - fb * 56;
  u32 oo = WS_OG + ((u32)(fb * 128 + (t >> 2)) * M_ + (u32)(fc * 8 + (t & 3) * 2)) * 8u;
  {
    u32 vw = L_VST + bq * 64u + (u32)(w & 3) * 16u + pp * 4u + (gq & 1u) * 2u;
    u32 va = (u32)(lane & 1) * 64u + gq * 16u;
    u32 og = WS_OG + (u32)(((lane >> 5) * 128 + hd) * M_ + ch * 224 + w * 32 + (lane & 31)) * 8u;
    const u32 wc = w < 7 ? (u32)w : 0u;
    u32 bo0 = L_WO + (8u * gq + qq) * 448u + wc * 64u + pp * 8u;
    u32 bo1 = L_W2 + ((8u * gq + qq) & 15u) * 448u + wc * 64u + pp * 8u;
    PIN(vw); PIN(va); PIN(og); PIN(bo0); PIN(bo1); PIN(oo);
    v4u cv[1];
    if (FDLY) __builtin_amdgcn_s_sleep(FDLY);
    PISSUE(cv, 1, cvo, 0u)
    fl_wait(lb + F_PROG, P_END);
    const bf8 a1 = as_bf8(lds16(lds, L_VH + va));
    const v4f z = {0.f, 0.f, 0.f, 0.f};
    v4f oa = __builtin_amdgcn_mfma_f32_16x16x32_bf16(a1, tr_pair(lds, bo1, bo1 + 1792), z, 0, 0, 0);
    v4f ob = __builtin_amdgcn_mfma_f32_16x16x32_bf16(a1, tr_pair(lds, bo1 + 32, bo1 + 32 + 1792), z, 0, 0, 0);
    bf8 wa = tr_pair(lds, bo0, bo0 + 1792), wb = tr_pair(lds, bo0 + 32, bo0 + 32 + 1792);
    asm volatile("" : "+v"(oa), "+v"(ob), "+v"(wa), "+v"(wb));
    PCHECK(cv, 1, cvo, 0u, 6u)
    float c = x32_add(j16_add(__uint_as_float(cv[0][0]), __uint_as_float(cv[0][2])));
    c = ror_add<4>(c); c = ror_add<8>(c);
    *reinterpret_cast<u16*>(lds + vw) = f2bf(c);
    STAMP(15);
    sbar(lb + F_B + 12, lane);
    STAMP(16);
    const bf8 a0 = as_bf8(lds16(lds, L_VST + va));
    oa = __builtin_amdgcn_mfma_f32_16x16x32_bf16(a0, wa, oa, 0, 0, 0);
    ob = __builtin_amdgcn_mfma_f32_16x16x32_bf16(a0, wb, ob, 0, 0, 0);
    const float o0 = (gq & 1u) ? ob[0] : oa[0], o1 = (gq & 1u) ? ob[1] : oa[1];
    const u32 ov = __float_as_uint(lane >= 32 ? o1 : o0), on = dppu<0xB1>(ov);
    if (w < 7 && (lane & 1) == 0) ST2(og, ov, on);
  }
  STAMP(12);
  if (i < 112) {
    u32 fw = L_FR + (pp * 2u + (gq & 1u)) * 32u + (u32)w * 4u;
    PIN(fw);
    float xr = 0.f;
    if (w == 0) xr = bf2f(ldh(x + fb * M_ + fc * 8 + (lane >> 3))) + bf2f(ldh(b2 + fc * 8 + (lane >> 3)));
    v4u ov[1];
    if (EDLY) __builtin_amdgcn_s_sleep(EDLY);
    PISSUE(ov, 1, oo, 0u)
    PCHECK(ov, 1, oo, 0u, 5u)
    float c = x32_add(j16_add(__uint_as_float(ov[0][0]), __uint_as_float(ov[0][2])));
    c = ror_add<4>(c); c = ror_add<8>(c);
    *reinterpret_cast<float*>(lds + fw) = c;
    fl_add(lb + F_B + 16, lane);
    if (w == 0) {
      fl_wait(lb + F_B + 16, 8);
      STAMP(13);
      float a = *reinterpret_cast<const float*>(lds + L_FR + lane * 4);
      a += dppf<0xB1>(a); a += dppf<0x4E>(a); a += dppf<0x141>(a);
      if ((lane & 7) == 0) *(GAS u16*)(out + fb * M_ + fc * 8 + (lane >> 3)) = f2bf(xr + a);
      if (i == 0 && lane == 0) __hip_atomic_store((gu32*)(ws + WS_EP), tag, RLX_AGENT);
      STAMP(14);
    }
  }
  STAMP_FLUSH;
}

extern "C" void kernel_launch(void* const* d_in, const int* in_sizes, int n_in,
                              void* d_out, int out_size, void* d_ws, size_t ws_size,
                              hipStream_t stream) {
  const u16* x = (const u16*)d_in[0];
  const u16* ckv = (const u16*)d_in[1];
  const u16* wq = (const u16*)d_in[2];
  const u16* wuk = (const u16*)d_in[3];
  const u16* wuv = (const u16*)d_in[4];
  const u16* wo = (const u16*)d_in[5];
  const u16* w1 = (const u16*)d_in[7];
  const u16* b1 = (const u16*)d_in[8];
  const u16* w2 = (const u16*)d_in[9];
  const u16* b2 = (const u16*)d_in[10];
  mla_decode<<<256, 576, 0, stream>>>(x, ckv, wq, wuk, wuv, wo, w1, b1, w2, b2, (char*)d_ws, (u16*)d_out);
}
```

```cpp
#define MEAS 18
#define MWG 0
#define MWAVE 0
#include <hip/hip_runtime.h>
#include <hip/hip_bf16.h>
#include <stdint.h>

typedef unsigned short u16;
typedef unsigned int u32;
typedef unsigned long long u64;
typedef __attribute__((ext_vector_type(4))) unsigned v4u;
typedef __attribute__((ext_vector_type(2))) unsigned v2u;
typedef __attribute__((ext_vector_type(4))) float v4f;
typedef __attribute__((ext_vector_type(8))) __bf16 bf8;
typedef __attribute__((ext_vector_type(4))) short s4;
typedef __attribute__((ext_vector_type(8))) short s8;

#define GAS __attribute__((address_space(1)))
#define LAS __attribute__((address_space(3)))
typedef GAS u32 gu32;
typedef GAS u64 gu64;
#define RLX_AGENT __ATOMIC_RELAXED, __HIP_MEMORY_SCOPE_AGENT

#define M_ 448
#define F_ 1792
#define DC 1024
#define SK 4096

constexpr u32 SPIN_CAP = 1u << 18;

constexpr u32 WS_EP = 0;
constexpr u32 WS_QL = 65536;
constexpr u32 WS_SC = 262144;
constexpr u32 WS_PV = 1310720;
constexpr u32 WS_ML = 3407872;
constexpr u32 WS_CP = 3416064;
constexpr u32 WS_OG = 4464640;
constexpr u32 WS_QG = WS_OG + 2 * 128 * 448 * 8;
constexpr u32 WS_END = WS_QG + 262144;

#define STAMP(k) do { if ((k) == 0) st0 = __builtin_amdgcn_s_memrealtime(); if ((k) == MEAS) st1 = __builtin_amdgcn_s_memrealtime(); } while (0)
#define STAMP_DECL u64 st0 = 0, st1 = 0;
#define STAMP_FLUSH do { if (blockIdx.x == MWG && w == MWAVE) { const u64 te = __builtin_amdgcn_s_memrealtime() + 2 * (st1 - st0); for (u32 n_ = 0; n_ < 100000u; ++n_) { if (__builtin_amdgcn_s_memrealtime() >= te) break; __builtin_amdgcn_s_sleep(1); } } } while (0)

#define BAR() do { asm volatile("s_waitcnt lgkmcnt(0)" ::: "memory"); __builtin_amdgcn_s_barrier(); asm volatile("" ::: "memory"); } while (0)

__device__ __forceinline__ float bf2f(u16 v) { return __uint_as_float(((u32)v) << 16); }
__device__ __forceinline__ u16 f2bf(float f) { __hip_bfloat16 b = __float2bfloat16(f); return *reinterpret_cast<u16*>(&b); }
__device__ __forceinline__ float rbf(float f) { return bf2f(f2bf(f)); }
__device__ __forceinline__ float lo_f(u32 v) { return __uint_as_float(v << 16); }
__device__ __forceinline__ float hi_f(u32 v) { return __uint_as_float(v & 0xffff0000u); }
__device__ __forceinline__ u32 pk2(float a, float b) { return (u32)f2bf(a) | ((u32)f2bf(b) << 16); }
__device__ __forceinline__ v4u ld16(const u16* p) { return __builtin_nontemporal_load((const GAS v4u*)p); }
__device__ __forceinline__ v4u ldp16(const u16* p) { return *(const GAS v4u*)p; }
__device__ __forceinline__ u16 ldh(const u16* p) { return *(const GAS u16*)p; }
__device__ __forceinline__ bf8 as_bf8(v4u v) { return __builtin_bit_cast(bf8, v); }

template <int CTRL>
__device__ __forceinline__ float dppf(float v) {
  return __int_as_float(__builtin_amdgcn_update_dpp(0, __float_as_int(v), CTRL, 0xf, 0xf, true));
}
template <int N> __device__ __forceinline__ float ror_add(float v) { return v + dppf<0x120 + N>(v); }
template <int N> __device__ __forceinline__ float ror_max(float v) { return fmaxf(v, dppf<0x120 + N>(v)); }
__device__ __forceinline__ float x16_add(float v) {
  const u32 b = __float_as_uint(v);
  auto p = __builtin_amdgcn_permlane16_swap(b, b, false, false);
  return __uint_as_float(p[0]) + __uint_as_float(p[1]);
}
__device__ __forceinline__ float x32_add(float v) {
  const u32 b = __float_as_uint(v);
  auto p = __builtin_amdgcn_permlane32_swap(b, b, false, false);
  return __uint_as_float(p[0]) + __uint_as_float(p[1]);
}
__device__ __forceinline__ float x16_max(float v) {
  const u32 b = __float_as_uint(v);
  auto p = __builtin_amdgcn_permlane16_swap(b, b, false, false);
  return fmaxf(__uint_as_float(p[0]), __uint_as_float(p[1]));
}
__device__ __forceinline__ float x32_max(float v) {
  const u32 b = __float_as_uint(v);
  auto p = __builtin_amdgcn_permlane32_swap(b, b, false, false);
  return fmaxf(__uint_as_float(p[0]), __uint_as_float(p[1]));
}
__device__ __forceinline__ float wave_sum(float v) {
  v = ror_add<8>(v); v = ror_add<4>(v); v = ror_add<2>(v); v = ror_add<1>(v);
  return x32_add(x16_add(v));
}
__device__ __forceinline__ float wave_max(float v) {
  v = ror_max<8>(v); v = ror_max<4>(v); v = ror_max<2>(v); v = ror_max<1>(v);
  return x32_max(x16_max(v));
}

__device__ __forceinline__ u32 swz(u32 row) { return ((row & 3u) << 2) | ((row >> 2) & 3u); }
__device__ __forceinline__ u32 off_b(u32 row, u32 ch) { return 256u * row + 16u * (ch ^ swz(row)); }

constexpr u32 L_RA = 0;
constexpr u32 L_UV = 65536;
constexpr u32 L_WO = 98304;
constexpr u32 L_WQ = L_WO;
constexpr u32 L_W1 = L_WO + 14336;
constexpr u32 L_W2 = 126976;
constexpr u32 L_XN = 134144;
constexpr u32 L_P = L_XN + 1792;
constexpr u32 L_QST = L_P;
constexpr u32 L_SRED = L_P;
constexpr u32 L_AT = L_P + 16896;
constexpr u32 L_WG = L_AT + 4608;
constexpr u32 L_VST = L_WG + 128;
constexpr u32 L_VH = L_VST + 128;
constexpr u32 L_FR = L_VH + 128;
constexpr u32 L_BI = L_FR + 256;
constexpr u32 L_FLAG = L_BI + 64;
constexpr u32 L_END = L_FLAG + 128;
static_assert(L_END <= 163840, "LDS");
constexpr u32 F_PROG = L_FLAG;
constexpr u32 F_COL = L_FLAG + 4;
constexpr u32 F_UK = L_FLAG + 36;
constexpr u32 F_RO = L_FLAG + 40;
constexpr u32 F_SR = L_FLAG + 44;
constexpr u32 F_B = L_FLAG + 48;
constexpr u32 F_QP = L_FLAG + 68;
constexpr u32 F_XN = L_FLAG + 72;
constexpr u32 F_QS = L_FLAG + 76;
constexpr u32 F_E1 = L_FLAG + 80;
constexpr u32 F_E2 = L_FLAG + 84;
constexpr u32 F_E3 = L_FLAG + 88;
#define SELFPRE 15
#define MLW 5
#define TLBW 0
#define EARLYQ 1
#define GPOS 1
#define GE1 0
#define GE2 8
#define GE3 0
#define LORD 0
constexpr int P_WQ = 14, P_UK = 46, P_ROWS = 110, P_UV = 142, P_W1 = 156, P_W2 = 163, P_WO = 177;
constexpr int P_END = 177;

#define WIN 16
#define WIN2 16
#define WINC 16
constexpr int WMAX = WIN > WIN2 ? (WIN > WINC ? WIN : WINC) : (WIN2 > WINC ? WIN2 : WINC);
#define AUX_COL 2
#define PSLP 2
#define QDLY 12
#define BDLY 16
#define CDLY 14
#define DDLY 12
#define FDLY 16
#define EDLY 0
#define QGATE 4
#define PSTR 128
#define PRE_AUX 0
#define PRE_SC 0
#define POUT 0
#define PRE_RD(off) __builtin_amdgcn_raw_buffer_load_b32(rw, (off), 0, PRE_AUX)
#define PRE 110
#define CPRIO 2
#define SPRIO 3
constexpr u32 LSPIN = 1u << 22;

#define SPIN_FAIL(code) if (++spins > SPIN_CAP) { if (lane == 0) __hip_atomic_store(tmo, (code), RLX_AGENT); break; } __builtin_amdgcn_s_sleep(PSLP); asm volatile("" ::: "memory");

#define POLL1(v, off, code) __builtin_amdgcn_s_setprio(0); _Pragma("unroll 1") for (u32 spins = 0;;) { v = __builtin_amdgcn_raw_buffer_load_b128(rw, (off), 0, 16); if (__all((v[1] == tag) & (v[3] == tag))) break; SPIN_FAIL(code) } __builtin_amdgcn_s_setprio(CPRIO);

#define POLLN(v, N, off, stride, code, ev) { u32 pend = (1u << (N)) - 1u; __builtin_amdgcn_s_setprio(0); _Pragma("unroll 1") for (u32 spins = 0;;) { \
    _Pragma("unroll") for (int s_ = 0; s_ < (N); ++s_) if (pend >> s_ & 1u) v[s_] = __builtin_amdgcn_raw_buffer_load_b128(rw, (off) + s_ * (stride), 0, 16); \
    if ((ev) && spins == 0) fl_add(lb + (ev), lane); \
    _Pragma("unroll") for (int s_ = 0; s_ < (N); ++s_) if (pend >> s_ & 1u) { if (__all((v[s_][1] == tag) & (v[s_][3] == tag))) pend &= ~(1u << s_); } \
    if (!pend) break; SPIN_FAIL(code) } __builtin_amdgcn_s_setprio(CPRIO); }

#define PIN(v) asm volatile("" : "+v"(v))
#define PISSUE(v, N, off, stride) _Pragma("unroll") for (int s_ = 0; s_ < (N); ++s_) v[s_] = __builtin_amdgcn_raw_buffer_load_b128(rw, (off) + ((s_ * (stride)) & 4095u), (s_ * (stride)) & ~4095u, 16);
#define PBAD(v) (((v)[1] ^ tag) | ((v)[3] ^ tag))
#define PRETRY(v, N, off, stride, code) { u32 pend = 0; \
    _Pragma("unroll") for (int s_ = 0; s_ < (N); ++s_) if (!__all(PBAD(v[s_]) == 0u)) pend |= 1u << s_; \
    __builtin_amdgcn_s_setprio(0); _Pragma("unroll 1") for (u32 spins = 0; pend;) { SPIN_FAIL(code) \
    _Pragma("unroll") for (int s_ = 0; s_ < (N); ++s_) if (pend >> s_ & 1u) v[s_] = __builtin_amdgcn_raw_buffer_load_b128(rw, (off) + ((s_ * (stride)) & 4095u), (s_ * (stride)) & ~4095u, 16); \
    _Pragma("unroll") for (int s_ = 0; s_ < (N); ++s_) if (pend >> s_ & 1u) { if (__all(PBAD(v[s_]) == 0u)) pend &= ~(1u << s_); } \
    } __builtin_amdgcn_s_setprio(CPRIO); }
#define PCHECK(v, N, off, stride, code) { u32 bad_ = 0; _Pragma("unroll") for (int s_ = 0; s_ < (N); ++s_) bad_ |= PBAD(v[s_]); \
    if (__builtin_expect(!__all(bad_ == 0u), 0)) PRETRY(v, N, off, stride, code) }

#define DPPMAX(n) "s_nop 1\n v_max_f32_dpp %0, %0, %0 row_ror:" #n " row_mask:0xf bank_mask:0xf\n"
__device__ __forceinline__ void wave_max2(float& a, float& b, float& jn) {
  asm volatile("s_nop 1\n v_permlane32_swap_b32 %0, %1\n v_max_f32 %0, %0, %1\n v_mov_b32 %1, %0\n"
               "s_nop 1\n v_permlane16_swap_b32 %0, %1\n v_max_f32 %0, %0, %1\n"
               DPPMAX(8) DPPMAX(4) DPPMAX(2) DPPMAX(1)
               "v_mov_b32 %2, %0\n v_mov_b32 %1, %0\n s_nop 1\n v_permlane32_swap_b32 %0, %1\n s_nop 1"
               : "+v"(a), "+v"(b), "=&v"(jn));
}
__device__ __forceinline__ float row_max(float a) {
  asm volatile(DPPMAX(8) DPPMAX(4) DPPMAX(2) DPPMAX(1) "s_nop 1" : "+v"(a));
  return a;
}
__device__ __forceinline__ float j32_add(float a, float b) {
  auto p = __builtin_amdgcn_permlane32_swap(__float_as_uint(a), __float_as_uint(b), false, false);
  return __uint_as_float(p[0]) + __uint_as_float(p[1]);
}
__device__ __forceinline__ float j16_add(float a, float b) {
  auto p = __builtin_amdgcn_permlane16_swap(__float_as_uint(a), __float_as_uint(b), false, false);
  return __uint_as_float(p[0]) + __uint_as_float(p[1]);
}
typedef __attribute__((ext_vector_type(16))) float v16f;
#define COLNT " nt"
__device__ __forceinline__ v4u mk_rsrc(const void* p, u32 bytes) {
  const u64 a = (u64)(uintptr_t)p;
  return v4u{(u32)a, (u32)(a >> 32) & 0xffffu, bytes, 0x00020000u};
}
#define COL_REQ(V) "s_nop 0\n buffer_load_dwordx4 " V ", %2, %0 offen" COLNT " lds\n s_add_u32 m0, m0, 0x400\n s_add_u32 %0, %0, 0x2000\n"
__device__ __forceinline__ void col_reads(v4u rk4, u32 vk, u32 ck, u32 ld) {
  u32 t0, t1;
  asm volatile(
      "s_mov_b32 %1, m0\n s_mov_b32 m0, %8\n s_mov_b32 %0, %7\n"
      COL_REQ("%3") COL_REQ("%4") COL_REQ("%5") COL_REQ("%6") COL_REQ("%3") COL_REQ("%4") COL_REQ("%5") COL_REQ("%6")
      "s_mov_b32 m0, %1\n"
      : "=&s"(t0), "=&s"(t1)
      : "s"(rk4), "v"(vk), "v"(vk ^ 16u), "v"(vk ^ 32u), "v"(vk ^ 48u), "s"(ck), "s"(ld)
      : "memory", "scc");
}

__device__ __forceinline__ void st_gran(char* ws, u32 off, u32 tag, u32 val) {
  __hip_atomic_store((gu64*)(ws + off), ((u64)tag << 32) | val, RLX_AGENT);
}
#define ST2(off, a, b) __builtin_amdgcn_raw_buffer_store_b128(v4u{(a), tag, (b), tag}, rw, (off), 0, 16)
template <int CTRL> __device__ __forceinline__ u32 dppu(u32 v) { return (u32)__builtin_amdgcn_update_dpp(0, (int)v, CTRL, 0xf, 0xf, true); }
__device__ __forceinline__ bf8 tr_pair(char* lds, u32 a0, u32 a1) {
  const s4 b0 = __builtin_amdgcn_ds_read_tr16_b64_v4i16((LAS s4*)(lds + a0));
  const s4 b1 = __builtin_amdgcn_ds_read_tr16_b64_v4i16((LAS s4*)(lds + a1));
  const s8 bb = {b0[0], b0[1], b0[2], b0[3], b1[0], b1[1], b1[2], b1[3]};
  return __builtin_bit_cast(bf8, bb);
}
__device__ __forceinline__ v4u lds16(char* lds, u32 a) { return *reinterpret_cast<const v4u*>(lds + a); }
__device__ __forceinline__ v4f lds16f(char* lds, u32 a) { return *reinterpret_cast<const v4f*>(lds + a); }
__device__ __forceinline__ float fdiv(float a, float d, float r) {
  const float q = a * r;
  return fmaf(fmaf(-q, d, a), r, q);
}
__device__ __forceinline__ v4u frag(v4u v, int pa) {
  v4u o;
#pragma unroll
  for (int e = 0; e < 4; ++e) o[e] = (u32)__builtin_amdgcn_ds_bpermute(pa, (int)v[e]);
  return o;
}
__device__ __forceinline__ void gemv448(char* lds, u32 wt, int lane, float& a0, float& a1) {
  const u32 gq = lane >> 4, qq = (lane & 15) >> 2, pp = lane & 3;
  const u32 ao = L_XN + (u32)(lane & 1) * 896u + gq * 16u;
  const u32 bo = wt + (8u * gq + qq) * 32u + pp * 8u;
  v4f acc0 = {0.f, 0.f, 0.f, 0.f}, acc1 = {0.f, 0.f, 0.f, 0.f};
#pragma unroll
  for (int s = 0; s < 7; ++s) {
    acc0 = __builtin_amdgcn_mfma_f32_16x16x32_bf16(as_bf8(lds16(lds, ao + s * 64)), tr_pair(lds, bo + s * 1024, bo + s * 1024 + 128), acc0, 0, 0, 0);
    acc1 = __builtin_amdgcn_mfma_f32_16x16x32_bf16(as_bf8(lds16(lds, ao + (s + 7) * 64)), tr_pair(lds, bo + (s + 7) * 1024, bo + (s + 7) * 1024 + 128), acc1, 0, 0, 0);
  }
  a0 = acc0[0] + acc1[0]; a1 = acc0[1] + acc1[1];
}

__device__ __forceinline__ void fl_write(u32 a, u32 v) { asm volatile("ds_write_b32 %0, %1" :: "v"(a), "v"(v) : "memory"); }
__device__ __forceinline__ void fl_add(u32 a, int lane) { if (lane == 0) asm volatile("ds_add_u32 %0, %1" :: "v"(a), "v"(1u) : "memory"); }
__device__ __forceinline__ int fl_read(u32 a) {
  u32 v; asm volatile("ds_read_b32 %0, %1\n s_waitcnt lgkmcnt(0)" : "=v"(v) : "v"(a) : "memory");
  return (int)__builtin_amdgcn_readfirstlane(v);
}
__device__ __forceinline__ void fl_wait(u32 a, int v) {
#pragma unroll 1
  for (u32 n = 0; n < LSPIN; ++n) { if (fl_read(a) >= v) break; __builtin_amdgcn_s_sleep(0); }
}
__device__ __forceinline__ void sbar(u32 a, int lane) { fl_add(a, lane); fl_wait(a, 8); }
template <int K> __device__ __forceinline__ void drain(u32 fp, int q, int lo) {
  asm volatile("s_waitcnt vmcnt(%0)" :: "n"(K) : "memory");
  fl_write(fp, (u32)(q - K > lo ? q - K : lo));
  if constexpr (K > 0) drain<K - 1>(fp, q, lo);
}
__device__ __forceinline__ void gate(u32 fp, int q, u32 a, int v, int& lo) {
  if (fl_read(a) >= v) return;
  if (lo < q) drain<WMAX - 2>(fp, q, lo);
  lo = q;
  fl_wait(a, v);
}
template <int N, int U, int XM, int SEG, int AUX, int W>
__device__ __forceinline__ void stream(char* lds, u32 lb, const void* base, u32 bytes, u32 vb, u32 sb, u32 ss, u32 ld, int q0, int& lo) {
  const __amdgpu_buffer_rsrc_t rs = __builtin_amdgcn_make_buffer_rsrc((void*)base, 0, bytes, 0x00020000);
#pragma unroll 1
  for (u32 k = 0; k < (u32)(N / U); ++k) {
    const int qk = q0 + (int)(k * U);
    const u32 sk = SEG == 1 ? sb + (k < 3u ? k + 1u : k == 3u ? 0u : k) * 256u : sb + k * (U * ss);
#pragma unroll
    for (u32 n = 0; n < (u32)U; ++n) {
      const int q = qk + (int)n;
      asm volatile("s_waitcnt vmcnt(%0)" :: "n"(W - 1) : "memory");
      lo = q - (W - 1) > lo ? q - (W - 1) : lo;
      fl_write(lb + F_PROG, (u32)lo);
      const u32 xm = XM == 1 ? (n & 3u) << 4 : XM == 2 ? ((2u * n) & 15u) << 4 : XM == 3 ? (((n >> 2) & 1u) << 3) << 4 : 0u;
      if (XM == 4) {
        const u32 c = vb + (k * U + n) * 64u, rr = (c * 2341u) >> 16;
        __builtin_amdgcn_raw_ptr_buffer_load_lds(rs, (LAS void*)(lds + ld + k * (U * 1024u) + n * 1024u), 16, c * 16u + rr * 448u, sb, 0, AUX);
      } else
      __builtin_amdgcn_raw_ptr_buffer_load_lds(rs, (LAS void*)(lds + ld + k * (U * 1024u) + n * 1024u), 16, vb ^ xm, sk + n * ss, 0, AUX);
    }
  }
}

__global__ __launch_bounds__(576) void mla_decode(const u16* x, const u16* ckv, const u16* wq, const u16* wuk,
                                                  const u16* wuv, const u16* wo, const u16* w1, const u16* b1,
                                                  const u16* w2, const u16* b2, char* ws, u16* out) {
  __shared__ __attribute__((aligned(16))) char lds[L_END];
  const int t = threadIdx.x, lane = t & 63;
  const int w = __builtin_amdgcn_readfirstlane(t >> 6);
  const int i = blockIdx.x, xcd = i & 7, r = i >> 3;
  const int h1i = xcd * 2 + (r >> 4), cs = r & 15;
  const int G = xcd * 4 + (r >> 3), b = G >> 4, g = G & 15, j = r & 7;
  const int ch = r & 1, hd = xcd * 16 + (r >> 1), ds = hd & 7;
  const int fsl = hd < 112 ? hd : 0;
  STAMP_DECL
  STAMP(0);
  asm volatile("" : "+s"(x), "+s"(ckv), "+s"(wq), "+s"(wuk), "+s"(wuv), "+s"(wo), "+s"(w1), "+s"(b1), "+s"(w2), "+s"(b2), "+s"(ws), "+s"(out));
  gu32* tmo = (gu32*)(ws + WS_EP + 128);
  const __amdgpu_buffer_rsrc_t rw = __builtin_amdgcn_make_buffer_rsrc(ws, 0, WS_END, 0x00020000);
  const u32 gq = lane >> 4, qq = (lane & 15) >> 2, pp = lane & 3;

  const u32 lb = (u32)(uintptr_t)(LAS char*)lds;

  if (w == 8) {
    __builtin_amdgcn_s_setprio(SPRIO);
    if (lane < 32) fl_write(lb + L_FLAG + lane * 4, 0u);
    if (!EARLYQ) BAR();
    int lo = 0;
    const u32 hb = (u32)lane >> 5;
    const u32 vk = gq * 2048u + (((u32)lane & 15u) ^ (gq << 2)) * 16u;
    stream<14, 7, 0, 0, 0, WIN>(lds, lb, wq, M_ * 8192u, (u32)(lane >> 3) * 8192u + (((u32)lane & 7u) ^ ((((u32)lane >> 3) & 3u) << 1)) * 16u, (u32)(cs & 3) * (112u * 8192u) + (u32)h1i * 512u + (u32)(cs >> 2) * 128u, 8u * 8192u, L_WQ, 0, lo);
    if (EARLYQ) BAR();
    stream<32, 8, 2, 0, 2, WIN>(lds, lb, wuk, 1024u * 8192u, hb * 8192u + (((u32)lane & 31u) ^ hb) * 16u, (u32)(cs * 64) * 8192u + (u32)h1i * 512u, 16384u, L_UV, P_UK - 32, lo);
    STAMP(24);
    stream<64, 8, 1, 1, 0, WIN>(lds, lb, ckv, 2u * SK * DC * 2u, vk, (u32)(b * SK + g * 256 + j * 32) * 2048u, 8192u, L_RA, P_UK, lo);
    STAMP(25);
    if (GE1) gate(lb + F_PROG, P_ROWS, lb + F_E1, GE1, lo);
    STAMP(30);
    gate(lb + F_PROG, P_UV - 32, lb + F_UK, 4, lo);
    if (GPOS == 0) gate(lb + F_PROG, P_ROWS, lb + F_E2, 8, lo);
    stream<32, 8, 3, 0, 2, WIN2>(lds, lb, wuv, 1024u * 8192u, hb * 8192u + (((u32)lane & 31u) ^ (hb << 2)) * 16u, (u32)(cs * 64) * 8192u + (u32)h1i * 512u, 16384u, L_UV, P_UV - 32, lo);
    STAMP(26);
    if (GPOS == 1) gate(lb + F_PROG, P_UV, lb + F_E2, 8, lo);
    STAMP(31);
    stream<14, 7, 0, 0, 0, WIN2>(lds, lb, w1, M_ * F_ * 2u, (u32)(lane >> 1) * (F_ * 2u) + (u32)(lane & 1) * 16u, (u32)fsl * 32u, 32u * F_ * 2u, L_W1, P_W1 - 14, lo);
    STAMP(27);
    if (GPOS == 2) gate(lb + F_PROG, P_W1, lb + F_E2, 8, lo);
    if (GE3) gate(lb + F_PROG, P_W1, lb + F_E3, GE3, lo);
    stream<7, 7, 4, 0, 2, WIN2>(lds, lb, w2, 1792u * 896u, (u32)lane, (u32)(fsl * 16) * 896u + (u32)ch * 448u, 0u, L_W2, P_W2 - 7, lo);
    gate(lb + F_PROG, P_W2, lb + F_RO, 4, lo);
    stream<14, 7, 4, 0, 2, WIN2>(lds, lb, wo, 4096u * 896u, (u32)lane, (u32)(hd * 32) * 896u + (u32)ch * 448u, 0u, L_WO, P_W2, lo);
    STAMP(28);
    {
      asm volatile("s_waitcnt vmcnt(%0)" :: "n"(P_END - P_W1) : "memory");
      float a0, a1;
      gemv448(lds, L_W1, lane, a0, a1);
      const float hv = rbf(rbf(lane >= 32 ? a1 : a0) + *reinterpret_cast<const float*>(lds + L_BI + (lane & 15) * 4));
      const float ge = 0.5f * hv * (1.f + tanhf(0.7978845608028654f * (hv + 0.044715f * hv * hv * hv)));
      *reinterpret_cast<u16*>(lds + L_VH + lane * 2) = ((lane & 16) == 0 && hd < 112) ? f2bf(ge) : (u16)0;
      asm volatile("s_waitcnt lgkmcnt(0)" ::: "memory");
    }
    drain<WMAX - 1>(lb + F_PROG, P_END, lo);
    STAMP(29);
    STAMP_FLUSH;
    return;
  }

  __builtin_amdgcn_s_setprio(CPRIO);
  const u32 epv = __builtin_amdgcn_raw_buffer_load_b32(rw, WS_EP, 0, 0);
  const int pa = (4 * (lane & 15) + (lane >> 4)) * 4;
  u32 tag, pre0 = 0;
  if (w == 0) {
    const int lc = lane < 56 ? lane : 0;
    const v4u xr0 = ldp16(x + lc * 8), xr1 = ldp16(x + M_ + lc * 8);
    STAMP(21);
    BAR();
    float xa[8], xb[8];
#pragma unroll
    for (int p = 0; p < 4; ++p) { xa[2 * p] = lo_f(xr0[p]); xa[2 * p + 1] = hi_f(xr0[p]); xb[2 * p] = lo_f(xr1[p]); xb[2 * p + 1] = hi_f(xr1[p]); }
    const float act = lane < 56 ? 1.f : 0.f;
    float s0 = 0.f, s1 = 0.f;
#pragma unroll
    for (int k = 0; k < 8; ++k) { s0 += xa[k]; s1 += xb[k]; }
    s0 = wave_sum(s0 * act); s1 = wave_sum(s1 * act);
    const float m0 = s0 * (1.f / M_), m1 = s1 * (1.f / M_);
    float v0 = 0.f, v1 = 0.f;
#pragma unroll
    for (int k = 0; k < 8; ++k) { const float d0 = xa[k] - m0, d1 = xb[k] - m1; v0 += d0 * d0; v1 += d1 * d1; }
    v0 = wave_sum(v0 * act); v1 = wave_sum(v1 * act);
    const float mb0 = rbf(m0), mb1 = rbf(m1);
    const float den0 = rbf(sqrtf(rbf(rbf(v0 * (1.f / M_)) + 1e-5f)));
    const float den1 = rbf(sqrtf(rbf(rbf(v1 * (1.f / M_)) + 1e-5f)));
    const float r0 = __builtin_amdgcn_rcpf(den0), r1 = __builtin_amdgcn_rcpf(den1);
    v4u na, nb;
#pragma unroll
    for (int p = 0; p < 4; ++p) {
      na[p] = pk2(fdiv(rbf(xa[2 * p] - mb0), den0, r0), fdiv(rbf(xa[2 * p + 1] - mb0), den0, r0));
      nb[p] = pk2(fdiv(rbf(xb[2 * p] - mb1), den1, r1), fdiv(rbf(xb[2 * p + 1] - mb1), den1, r1));
    }
    if (lane < 56) {
      *reinterpret_cast<v4u*>(lds + L_XN + lane * 16) = na;
      *reinterpret_cast<v4u*>(lds + L_XN + 896 + lane * 16) = nb;
    }
    fl_add(lb + F_XN, lane);
    STAMP(17);
  } else if (w < 4) {
    float bias = 0.f;
    if (w == 1) bias = bf2f(ldh(b1 + fsl * 16 + (lane & 15)));
    if (w == 3) pre0 = __builtin_amdgcn_raw_buffer_load_b32(rw, lane < 8 ? WS_QL + (u32)(lane >> 2) * 65536u + (u32)(cs * 4 + (lane & 3)) * 1024u + (u32)h1i * 64u : WS_QG + (u32)h1i * 16384u + (u32)(cs & 3) * 4096u + (u32)(cs >> 2) * 1024u + (u32)(lane & 7) * 128u, 0, PRE_AUX);
    BAR();
    if (w == 1 && lane < 16) *reinterpret_cast<float*>(lds + L_BI + lane * 4) = bias;
    fl_wait(lb + F_XN, 1);
  }
  if (w < 4) {
    fl_wait(lb + F_PROG, P_WQ);
    STAMP(18);
    const u32 ao = L_XN + (u32)(lane & 1) * 896u + (u32)(cs & 3) * 224u;
    const u32 ch = ((((u32)w * 2u + (pp >> 1)) ^ (qq << 1)) * 16u) + (pp & 1u) * 8u;
    const u32 bo = L_WQ + (8u * gq + qq) * 128u + ch;
    v4f acc0 = {0.f, 0.f, 0.f, 0.f}, acc1 = {0.f, 0.f, 0.f, 0.f};
    acc0 = __builtin_amdgcn_mfma_f32_16x16x32_bf16(as_bf8(lds16(lds, ao + gq * 16u)), tr_pair(lds, bo, bo + 512), acc0, 0, 0, 0);
    acc1 = __builtin_amdgcn_mfma_f32_16x16x32_bf16(as_bf8(lds16(lds, ao + gq * 16u + 64)), tr_pair(lds, bo + 4096, bo + 4096 + 512), acc1, 0, 0, 0);
    acc0 = __builtin_amdgcn_mfma_f32_16x16x32_bf16(as_bf8(lds16(lds, ao + gq * 16u + 128)), tr_pair(lds, bo + 8192, bo + 8192 + 512), acc0, 0, 0, 0);
    acc1 = __builtin_amdgcn_mfma_f32_16x16x16bf16_1k(*reinterpret_cast<const s4*>(lds + ao + 192u + gq * 8u),
                                                     __builtin_amdgcn_ds_read_tr16_b64_v4i16((LAS s4*)(lds + L_WQ + (96u + 4u * gq + qq) * 128u + ch)), acc1, 0, 0, 0);
    tag = __builtin_amdgcn_readfirstlane(epv) + 1u;
    {
      const u32 qv = __float_as_uint(gq ? acc0[1] + acc1[1] : acc0[0] + acc1[0]), qn = dppu<0xB1>(qv);
      if ((lane & 33) == 0) ST2(WS_QG + (u32)h1i * 16384u + (u32)(cs & 3) * 4096u + (u32)(cs >> 2) * 1024u + (u32)w * 256u + (u32)lane * 8u, qv, qn);
    }
    STAMP(1);
    fl_add(lb + F_RO, lane);
    asm volatile("" :: "v"(pre0));
    if (w == 3) pre0 = PRE_RD(lane < 32 ? WS_SC + (u32)((G * 16 + (lane >> 1)) * 256 + j * 32) * 8u + (u32)(lane & 1) * 128u : WS_ML + (u32)((b * 16 + 2 * j + (lane & 1)) * 16 + (g & 8)) * 16u);
  } else {
    v4u wk[8];
    BAR();
    tag = __builtin_amdgcn_readfirstlane(epv) + 1u;
    u32 qo = WS_QG + (u32)h1i * 16384u + (u32)(w - 4) * 1024u + (u32)lane * 16u;
    u32 qw = L_QST + (u32)((lane >> 3) & 1) * 512u + (u32)(w - 4) * 128u + gq * 32u + (u32)(lane & 7) * 4u;
    u32 ao = L_QST + (u32)(lane & 1) * 512u + gq * 16u;
    const int bb = lane & 1, wq4 = w - 4;
    u32 qa = WS_QL + (u32)bb * 65536u + (u32)((cs * 2 + (wq4 >> 1)) * 2 + ((lane >> 2) & 1)) * 1024u + (u32)h1i * 64u
             + (u32)((wq4 & 1) * 2 + ((lane >> 3) & 1)) * 16u;
    PIN(qo); PIN(qw); PIN(ao); PIN(qa);
    fl_wait(lb + F_RO, QGATE);
    if (QDLY) __builtin_amdgcn_s_sleep(QDLY);
    STAMP(19);
    v4u ga[4];
    PISSUE(ga, 4, qo, 4096u)
    fl_wait(lb + F_PROG, P_UK);
    {
      const u32 ko = L_UV + (u32)((w - 4) * 16 + (lane & 15)) * 512u;
#pragma unroll
      for (int s = 0; s < 8; ++s) wk[s] = lds16(lds, ko + ((((u32)s * 4u + gq) ^ ((u32)lane & 15u)) * 16u));
    }
    fl_add(lb + F_UK, lane);
    PCHECK(ga, 4, qo, 4096u, 1u)
    {
      const float q0 = (__uint_as_float(ga[0][0]) + __uint_as_float(ga[1][0])) + (__uint_as_float(ga[2][0]) + __uint_as_float(ga[3][0]));
      const float q1 = (__uint_as_float(ga[0][2]) + __uint_as_float(ga[1][2])) + (__uint_as_float(ga[2][2]) + __uint_as_float(ga[3][2]));
      *reinterpret_cast<u32*>(lds + qw) = pk2(q0, q1);
    }
    fl_add(lb + F_QS, lane);
    STAMP(20);
    fl_wait(lb + F_QS, 4);
    v4f acc0 = {0.f, 0.f, 0.f, 0.f}, acc1 = {0.f, 0.f, 0.f, 0.f};
#pragma unroll
    for (int s = 0; s < 4; ++s) {
      acc0 = __builtin_amdgcn_mfma_f32_16x16x32_bf16(as_bf8(lds16(lds, ao + s * 64)), as_bf8(wk[s]), acc0, 0, 0, 0);
      acc1 = __builtin_amdgcn_mfma_f32_16x16x32_bf16(as_bf8(lds16(lds, ao + (s + 4) * 64)), as_bf8(wk[s + 4]), acc1, 0, 0, 0);
    }
    const float a0 = acc0[0] + acc1[0], a1 = acc0[1] + acc1[1];
    const float own = bb ? a1 : a0, oth = dppf<0xB1>(bb ? a0 : a1);
    {
      const u32 qv = bb ? pk2(oth, own) : pk2(own, oth), qn = dppu<0x4E>(qv);
      if ((lane & 50) == 0) ST2(qa, qv, qn);
    }
    STAMP(2);
    if (BDLY) __builtin_amdgcn_s_sleep(BDLY);
    fl_add(lb + F_QP, lane);
  }
  v4u bv[8];
  const int slot = w == 0 ? 3 : w < 4 ? w - 1 : w;
  {
    fl_wait(lb + F_PROG, P_UK + 8 * (slot + 1));
    const u32 key = (u32)lane & 15u;
    const u32 ro = L_RA + (u32)slot * 8192u + key * 256u;
#pragma unroll
    for (int s = 0; s < 8; ++s) bv[s] = lds16(lds, ro + (u32)(s >> 2) * 4096u + ((((u32)(s & 3) * 4u + gq) ^ swz(key)) * 16u));
  }
  STAMP(3);
  u32 so = WS_SC + (u32)((G * 16 + 2 * w) * 256 + 2 * lane) * 8u;
  {
    u32 qo = WS_QL + (u32)b * 65536u + (u32)w * 8192u + (u32)lane * 16u;
    u32 srw = L_SRED + (u32)((w * 16 + (int)gq * 4) * 33 + (lane & 15)) * 4u;
    u32 srr = L_SRED + (u32)((t >> 5) * 33 + (t & 31)) * 4u;
    u32 sco = WS_SC + (u32)((G * 16 + (t >> 5)) * 256 + j * 32 + (t & 31)) * 8u;
    int pav = pa;
    PIN(qo); PIN(srw); PIN(srr); PIN(sco); PIN(so); PIN(pav);
    v4u gl[8];
    fl_wait(lb + F_QP, 4);
    PISSUE(gl, 8, qo, 1024u)
    fl_add(lb + F_E1, lane);
    v4f acc0, acc1;
#pragma unroll 1
    for (int pass = 0;; ++pass) {
      u32 bad = 0;
      acc0 = v4f{0.f, 0.f, 0.f, 0.f}; acc1 = v4f{0.f, 0.f, 0.f, 0.f};
#pragma unroll
      for (int s = 0; s < 4; ++s) {
        bad |= PBAD(gl[2 * s]) | PBAD(gl[2 * s + 1]);
        const v4u av = frag(v4u{gl[2 * s][0], gl[2 * s][2], gl[2 * s + 1][0], gl[2 * s + 1][2]}, pav);
        acc0 = __builtin_amdgcn_mfma_f32_16x16x32_bf16(as_bf8(av), as_bf8(bv[s]), acc0, 0, 0, 0);
        acc1 = __builtin_amdgcn_mfma_f32_16x16x32_bf16(as_bf8(av), as_bf8(bv[4 + s]), acc1, 0, 0, 0);
      }
      if (__builtin_expect(pass || __all(bad == 0u), 1)) break;
      PRETRY(gl, 8, qo, 1024u, 2u)
    }
    asm volatile("" :: "v"(pre0));
    STAMP(4);
    float* sr = reinterpret_cast<float*>(lds + srw);
#pragma unroll
    for (int q = 0; q < 4; ++q) { sr[q * 33] = acc0[q]; sr[q * 33 + 16] = acc1[q]; }
    sbar(lb + F_B, lane);
    STAMP(5);
    const float* sp = reinterpret_cast<const float*>(lds + srr);
    float s = ((sp[0] + sp[528]) + (sp[2 * 528] + sp[3 * 528])) + ((sp[4 * 528] + sp[5 * 528]) + (sp[6 * 528] + sp[7 * 528]));
    s *= 0.0625f * 1.4426950408889634f;
    const u32 sn = dppu<0xB1>(__float_as_uint(s));
    if ((lane & 1) == 0) ST2(sco, __float_as_uint(s), sn);
    fl_add(lb + F_SR, lane);
    col_reads(mk_rsrc(ckv, 2u * SK * DC * 2u), gq * 2048u + (((u32)lane & 15u) ^ (gq << 2)) * 16u,
              (u32)(b * SK + g * 256 + slot * 32) * 2048u + (u32)j * 256u, lb + L_RA + (u32)slot * 8192u);
    if (w == 3) pre0 = PRE_RD(WS_PV + (u32)(((b * 16 + (lane >> 2)) * 16 + g) * 512 + j * 64) * 8u + (u32)(lane & 3) * 128u);
  }
  const u32 bq = (u32)w >> 2;
  u32 pvo = WS_PV + (((bq * 16u + (u32)h1i) * 16u + (u32)(w & 3) * 4u + gq) * 512u + (u32)cs * 32u + (u32)(lane & 15) * 2u) * 8u;
  {
    u32 pw = L_P + (u32)(2 * w) * 528u + (u32)lane * 4u;
    u32 po = L_P + (u32)(lane & 15) * 528u + gq * 16u;
    u32 k0 = L_RA + off_b(8 * gq + qq, 2 * w + (pp >> 1)) + 8 * (pp & 1);
    u32 k1 = L_RA + off_b(8 * gq + 4 + qq, 2 * w + (pp >> 1)) + 8 * (pp & 1);
    const int hi8 = (lane >> 3) & 1, od = lane & 1;
    u32 go = WS_PV + (u32)(((b * 16 + (lane >> 4) * 4 + 2 * hi8 + od) * 16 + g) * 512 + j * 64 + w * 8 + (lane & 6)) * 8u;
    PIN(pw); PIN(po); PIN(k0); PIN(k1); PIN(go); PIN(pvo);
    v4u gs[4];
    bf8 kb[8];
    if (CDLY) __builtin_amdgcn_s_sleep(CDLY);
    PISSUE(gs, 4, so, 1024u)
    fl_add(lb + F_E2, lane);
    fl_wait(lb + F_SR, 8);
    PCHECK(gs, 4, so, 1024u, 3u)
    asm volatile("s_waitcnt vmcnt(0)" ::: "memory");
    fl_add(lb + F_COL, lane);
    asm volatile("" :: "v"(pre0));
    STAMP(6);
    float sv[8];
#pragma unroll
    for (int q = 0; q < 4; ++q) { sv[2 * q] = __uint_as_float(gs[q][0]); sv[2 * q + 1] = __uint_as_float(gs[q][2]); }
    float m0 = fmaxf(fmaxf(sv[0], sv[1]), fmaxf(sv[2], sv[3])), m1 = fmaxf(fmaxf(sv[4], sv[5]), fmaxf(sv[6], sv[7])), mj;
    wave_max2(m0, m1, mj);
    u32 pk[4];
#pragma unroll
    for (int q = 0; q < 4; ++q) {
      const float m = q < 2 ? m0 : m1;
      pk[q] = pk2(__builtin_amdgcn_exp2f(sv[2 * q] - m), __builtin_amdgcn_exp2f(sv[2 * q + 1] - m));
      *reinterpret_cast<u32*>(lds + pw + (q >> 1) * 528 + (q & 1) * 256) = pk[q];
    }
    fl_add(lb + F_B + 4, lane);
    if (w == j) {
      float l = j32_add((lo_f(pk[0]) + hi_f(pk[0])) + (lo_f(pk[1]) + hi_f(pk[1])), (lo_f(pk[2]) + hi_f(pk[2])) + (lo_f(pk[3]) + hi_f(pk[3])));
      l = x16_add(l);
      l = ror_add<8>(l); l = ror_add<4>(l); l = ror_add<2>(l); l = ror_add<1>(l);
      if ((lane & 31) == 0) ST2(WS_ML + (u32)((b * 16 + 2 * w + (lane >> 5)) * 16 + g) * 16u, __float_as_uint(mj), __float_as_uint(l));
    }
    fl_wait(lb + F_COL, 8);
#pragma unroll
    for (int ks = 0; ks < 8; ++ks) kb[ks] = tr_pair(lds, k0 + ks * 8192, k1 + ks * 8192);
    STAMP(22);
    fl_wait(lb + F_B + 4, 8);
    STAMP(7);
    v4f acc0 = {0.f, 0.f, 0.f, 0.f}, acc1 = {0.f, 0.f, 0.f, 0.f};
#pragma unroll
    for (int ks = 0; ks < 4; ++ks) {
      acc0 = __builtin_amdgcn_mfma_f32_16x16x32_bf16(as_bf8(lds16(lds, po + ks * 64)), kb[ks], acc0, 0, 0, 0);
      acc1 = __builtin_amdgcn_mfma_f32_16x16x32_bf16(as_bf8(lds16(lds, po + (ks + 4) * 64)), kb[ks + 4], acc1, 0, 0, 0);
    }
    const v4f acc = acc0 + acc1;
    const float s0 = hi8 ? acc[0] : acc[2], s1 = hi8 ? acc[1] : acc[3];
    const float n0 = dppf<0x128>(s0), n1 = dppf<0x128>(s1);
    const u32 g0 = hi8 ? pk2(n0, acc[2]) : pk2(acc[0], n0);
    const u32 g1 = hi8 ? pk2(n1, acc[3]) : pk2(acc[1], n1);
    const u32 gn = dppu<0xB1>(od ? g0 : g1);
    ST2(go, od ? gn : g0, od ? g1 : gn);
  }
  if (w == 3) pre0 = PRE_RD(lane < 32 ? WS_CP + ((u32)(h1i * 16 + (lane >> 1)) * 16u + (u32)cs) * 256u + (u32)(lane & 1) * 128u
                                      : WS_OG + (u32)(((lane >> 4) & 1) * 128 + hd) * 3584u + (u32)ch * 1792u + (u32)((lane & 15) < 14 ? (lane & 15) : 0) * 128u);
  STAMP(8);
  u32 cvo = WS_CP + (((u32)h1i * 2u + bq) * 8u + (u32)ds) * 4096u + (u32)(lane >> 2) * 256u + (u32)(w & 3) * 64u + pp * 16u;
  {
    u32 aw = L_AT + (bq * 16u + (u32)(w & 3) * 4u + gq) * 144u + (u32)(lane & 15) * 8u;
    u32 ar = L_AT + (u32)(lane & 31) * 144u + (u32)(lane >> 5) * 16u;
    u32 wr = L_WG + (u32)(lane >> 5) * 16u;
    u32 bo = L_UV + ((gq >> 1) * 4u + (qq >> 1) + 8u * (qq & 1u)) * 512u
             + ((((u32)w * 4u + (gq & 1u) * 2u + (pp >> 1)) ^ (((qq >> 1) << 2) | ((qq & 1u) << 3))) * 16u) + (pp & 1u) * 8u;
    u32 co = WS_CP + ((((u32)h1i * 2u + (u32)(lane >> 5)) * 8u + (u32)w) * 16u + (u32)cs) * 256u + (u32)(lane & 31) * 8u;
    u32 mlo = WS_ML + (((gq & 1u) * 16u + (u32)h1i) * 16u + (u32)(lane & 15)) * 16u;
    PIN(aw); PIN(ar); PIN(wr); PIN(bo); PIN(co); PIN(mlo); PIN(cvo);
    v4u pv[1], ml[1];
    bf8 ub[4];
    if (DDLY) __builtin_amdgcn_s_sleep(DDLY);
    if (w == MLW) { PISSUE(ml, 1, mlo, 0u) }
    PISSUE(pv, 1, pvo, 0u)
    fl_add(lb + F_E3, lane);
    const bool have = fl_read(lb + F_PROG) >= P_UV;
    if (have) {
#pragma unroll
      for (int T = 0; T < 4; ++T) ub[T] = tr_pair(lds, bo + T * 8192, bo + T * 8192 + 1024);
    }
    if (w == MLW) {
      PCHECK(ml, 1, mlo, 0u, 7u)
      const float mg = __uint_as_float(ml[0][0]);
      const float eg = __builtin_amdgcn_exp2f(mg - row_max(mg));
      float ls = eg * __uint_as_float(ml[0][2]);
      ls = ror_add<8>(ls); ls = ror_add<4>(ls); ls = ror_add<2>(ls); ls = ror_add<1>(ls);
      const float rl = __builtin_amdgcn_rcpf(ls);
      *reinterpret_cast<float*>(lds + L_WG + (lane & 31) * 4) = fdiv(eg, ls, rl);
    }
    PCHECK(pv, 1, pvo, 0u, 4u)
    asm volatile("" :: "v"(pre0));
    STAMP(9);
    *reinterpret_cast<v2u*>(lds + aw) = v2u{pv[0][0], pv[0][2]};
    fl_add(lb + F_B + 8, lane);
    if (!have) {
      fl_wait(lb + F_PROG, P_UV);
#pragma unroll
      for (int T = 0; T < 4; ++T) ub[T] = tr_pair(lds, bo + T * 8192, bo + T * 8192 + 1024);
    }
    STAMP(23);
    fl_wait(lb + F_B + 8, 8);
    STAMP(10);
    v16f acc;
#pragma unroll
    for (int q = 0; q < 16; ++q) acc[q] = 0.f;
    v4u at[4];
#pragma unroll
    for (int T = 0; T < 4; ++T) at[T] = lds16(lds, ar + T * 32);
    v4f wt[4];
#pragma unroll
    for (int q = 0; q < 4; ++q) wt[q] = lds16f(lds, wr + q * 32);
#pragma unroll
    for (int T = 0; T < 4; ++T) acc = __builtin_amdgcn_mfma_f32_32x32x16_bf16(as_bf8(at[T]), ub[T], acc, 0, 0, 0);
    float c[2];
#pragma unroll
    for (int e = 0; e < 2; ++e) {
      float u0 = wt[2 * e][0] * acc[8 * e], u1 = wt[2 * e][1] * acc[8 * e + 1];
      u0 = fmaf(wt[2 * e][2], acc[8 * e + 2], u0); u1 = fmaf(wt[2 * e][3], acc[8 * e + 3], u1);
      u0 = fmaf(wt[2 * e + 1][0], acc[8 * e + 4], u0); u1 = fmaf(wt[2 * e + 1][1], acc[8 * e + 5], u1);
      u0 = fmaf(wt[2 * e + 1][2], acc[8 * e + 6], u0); u1 = fmaf(wt[2 * e + 1][3], acc[8 * e + 7], u1);
      c[e] = u0 + u1;
    }
    const u32 cv = __float_as_uint(j32_add(c[0], c[1])), cn = dppu<0xB1>(cv);
    if ((lane & 1) == 0) ST2(co, cv, cn);
  }
  STAMP(11);
  const int fb = i >= 56 ? 1 : 0, fc = i - fb * 56;
  u32 oo = WS_OG + ((u32)(fb * 128 + (t >> 2)) * M_ + (u32)(fc * 8 + (t & 3) * 2)) * 8u;
  {
    u32 vw = L_VST + bq * 64u + (u32)(w & 3) * 16u + pp * 4u + (gq & 1u) * 2u;
    u32 va = (u32)(lane & 1) * 64u + gq * 16u;
    u32 og = WS_OG + (u32)(((lane >> 5) * 128 + hd) * M_ + ch * 224 + w * 32 + (lane & 31)) * 8u;
    const u32 wc = w < 7 ? (u32)w : 0u;
    u32 bo0 = L_WO + (8u * gq + qq) * 448u + wc * 64u + pp * 8u;
    u32 bo1 = L_W2 + ((8u * gq + qq) & 15u) * 448u + wc * 64u + pp * 8u;
    PIN(vw); PIN(va); PIN(og); PIN(bo0); PIN(bo1); PIN(oo);
    v4u cv[1];
    if (FDLY) __builtin_amdgcn_s_sleep(FDLY);
    PISSUE(cv, 1, cvo, 0u)
    fl_wait(lb + F_PROG, P_END);
    const bf8 a1 = as_bf8(lds16(lds, L_VH + va));
    const v4f z = {0.f, 0.f, 0.f, 0.f};
    v4f oa = __builtin_amdgcn_mfma_f32_16x16x32_bf16(a1, tr_pair(lds, bo1, bo1 + 1792), z, 0, 0, 0);
    v4f ob = __builtin_amdgcn_mfma_f32_16x16x32_bf16(a1, tr_pair(lds, bo1 + 32, bo1 + 32 + 1792), z, 0, 0, 0);
    bf8 wa = tr_pair(lds, bo0, bo0 + 1792), wb = tr_pair(lds, bo0 + 32, bo0 + 32 + 1792);
    asm volatile("" : "+v"(oa), "+v"(ob), "+v"(wa), "+v"(wb));
    PCHECK(cv, 1, cvo, 0u, 6u)
    float c = x32_add(j16_add(__uint_as_float(cv[0][0]), __uint_as_float(cv[0][2])));
    c = ror_add<4>(c); c = ror_add<8>(c);
    *reinterpret_cast<u16*>(lds + vw) = f2bf(c);
    STAMP(15);
    sbar(lb + F_B + 12, lane);
    STAMP(16);
    const bf8 a0 = as_bf8(lds16(lds, L_VST + va));
    oa = __builtin_amdgcn_mfma_f32_16x16x32_bf16(a0, wa, oa, 0, 0, 0);
    ob = __builtin_amdgcn_mfma_f32_16x16x32_bf16(a0, wb, ob, 0, 0, 0);
    const float o0 = (gq & 1u) ? ob[0] : oa[0], o1 = (gq & 1u) ? ob[1] : oa[1];
    const u32 ov = __float_as_uint(lane >= 32 ? o1 : o0), on = dppu<0xB1>(ov);
    if (w < 7 && (lane & 1) == 0) ST2(og, ov, on);
  }
  STAMP(12);
  if (i < 112) {
    u32 fw = L_FR + (pp * 2u + (gq & 1u)) * 32u + (u32)w * 4u;
    PIN(fw);
    float xr = 0.f;
    if (w == 0) xr = bf2f(ldh(x + fb * M_ + fc * 8 + (lane >> 3))) + bf2f(ldh(b2 + fc * 8 + (lane >> 3)));
    v4u ov[1];
    if (EDLY) __builtin_amdgcn_s_sleep(EDLY);
    PISSUE(ov, 1, oo, 0u)
    PCHECK(ov, 1, oo, 0u, 5u)
    float c = x32_add(j16_add(__uint_as_float(ov[0][0]), __uint_as_float(ov[0][2])));
    c = ror_add<4>(c); c = ror_add<8>(c);
    *reinterpret_cast<float*>(lds + fw) = c;
    fl_add(lb + F_B + 16, lane);
    if (w == 0) {
      fl_wait(lb + F_B + 16, 8);
      STAMP(13);
      float a = *reinterpret_cast<const float*>(lds + L_FR + lane * 4);
      a += dppf<0xB1>(a); a += dppf<0x4E>(a); a += dppf<0x141>(a);
      if ((lane & 7) == 0) *(GAS u16*)(out + fb * M_ + fc * 8 + (lane >> 3)) = f2bf(xr + a);
      if (i == 0 && lane == 0) __hip_atomic_store((gu32*)(ws + WS_EP), tag, RLX_AGENT);
      STAMP(14);
    }
  }
  STAMP_FLUSH;
}

extern "C" void kernel_launch(void* const* d_in, const int* in_sizes, int n_in,
                              void* d_out, int out_size, void* d_ws, size_t ws_size,
                              hipStream_t stream) {
  const u16* x = (const u16*)d_in[0];
  const u16* ckv = (const u16*)d_in[1];
  const u16* wq = (const u16*)d_in[2];
  const u16* wuk = (const u16*)d_in[3];
  const u16* wuv = (const u16*)d_in[4];
  const u16* wo = (const u16*)d_in[5];
  const u16* w1 = (const u16*)d_in[7];
  const u16* b1 = (const u16*)d_in[8];
  const u16* w2 = (const u16*)d_in[9];
  const u16* b2 = (const u16*)d_in[10];
  mla_decode<<<256, 576, 0, stream>>>(x, ckv, wq, wuk, wuv, wo, w1, b1, w2, b2, (char*)d_ws, (u16*)d_out);
}
```

```cpp
#include <hip/hip_runtime.h>
#include <hip/hip_bf16.h>
#include <stdint.h>

typedef unsigned short u16;
typedef unsigned int u32;
typedef unsigned long long u64;
typedef __attribute__((ext_vector_type(4))) unsigned v4u;
typedef __attribute__((ext_vector_type(2))) unsigned v2u;
typedef __attribute__((ext_vector_type(4))) float v4f;
typedef __attribute__((ext_vector_type(8))) __bf16 bf8;
typedef __attribute__((ext_vector_type(4))) short s4;
typedef __attribute__((ext_vector_type(8))) short s8;

#define GAS __attribute__((address_space(1)))
#define LAS __attribute__((address_space(3)))
typedef GAS u32 gu32;
typedef GAS u64 gu64;
#define RLX_AGENT __ATOMIC_RELAXED, __HIP_MEMORY_SCOPE_AGENT

#define M_ 448
#define F_ 1792
#define DC 1024
#define SK 4096

constexpr u32 SPIN_CAP = 1u << 18;

constexpr u32 WS_EP = 0;
constexpr u32 WS_QL = 65536;
constexpr u32 WS_SC = 262144;
constexpr u32 WS_PV = 1310720;
constexpr u32 WS_ML = 3407872;
constexpr u32 WS_CP = 3416064;
constexpr u32 WS_OG = 4464640;
constexpr u32 WS_QG = WS_OG + 2 * 128 * 448 * 8;
constexpr u32 WS_HP = WS_QG + 262144;
constexpr u32 WS_END = WS_HP + 112 * 7 * 256;

#define STAMP(k) do {} while (0)
#define STAMP_DECL
#define STAMP_FLUSH do {} while (0)

#define BAR() do { asm volatile("s_waitcnt lgkmcnt(0)" ::: "memory"); __builtin_amdgcn_s_barrier(); asm volatile("" ::: "memory"); } while (0)

__device__ __forceinline__ float bf2f(u16 v) { return __uint_as_float(((u32)v) << 16); }
__device__ __forceinline__ u16 f2bf(float f) { __hip_bfloat16 b = __float2bfloat16(f); return *reinterpret_cast<u16*>(&b); }
__device__ __forceinline__ float rbf(float f) { return bf2f(f2bf(f)); }
__device__ __forceinline__ float lo_f(u32 v) { return __uint_as_float(v << 16); }
__device__ __forceinline__ float hi_f(u32 v) { return __uint_as_float(v & 0xffff0000u); }
typedef __attribute__((ext_vector_type(2))) float v2f;
typedef __attribute__((ext_vector_type(2))) __bf16 bf2;
__device__ __forceinline__ u32 pk2(float a, float b) { return __builtin_bit_cast(u32, __builtin_convertvector(v2f{a, b}, bf2)); }
__device__ __forceinline__ v4u ld16(const u16* p) { return __builtin_nontemporal_load((const GAS v4u*)p); }
__device__ __forceinline__ v4u ldp16(const u16* p) { return *(const GAS v4u*)p; }
__device__ __forceinline__ u16 ldh(const u16* p) { return *(const GAS u16*)p; }
__device__ __forceinline__ bf8 as_bf8(v4u v) { return __builtin_bit_cast(bf8, v); }

template <int CTRL>
__device__ __forceinline__ float dppf(float v) {
  return __int_as_float(__builtin_amdgcn_update_dpp(0, __float_as_int(v), CTRL, 0xf, 0xf, true));
}
template <int N> __device__ __forceinline__ float ror_add(float v) { return v + dppf<0x120 + N>(v); }
template <int N> __device__ __forceinline__ float ror_max(float v) { return fmaxf(v, dppf<0x120 + N>(v)); }
__device__ __forceinline__ float x16_add(float v) {
  const u32 b = __float_as_uint(v);
  auto p = __builtin_amdgcn_permlane16_swap(b, b, false, false);
  return __uint_as_float(p[0]) + __uint_as_float(p[1]);
}
__device__ __forceinline__ float x32_add(float v) {
  const u32 b = __float_as_uint(v);
  auto p = __builtin_amdgcn_permlane32_swap(b, b, false, false);
  return __uint_as_float(p[0]) + __uint_as_float(p[1]);
}
__device__ __forceinline__ float x16_max(float v) {
  const u32 b = __float_as_uint(v);
  auto p = __builtin_amdgcn_permlane16_swap(b, b, false, false);
  return fmaxf(__uint_as_float(p[0]), __uint_as_float(p[1]));
}
__device__ __forceinline__ float x32_max(float v) {
  const u32 b = __float_as_uint(v);
  auto p = __builtin_amdgcn_permlane32_swap(b, b, false, false);
  return fmaxf(__uint_as_float(p[0]), __uint_as_float(p[1]));
}
__device__ __forceinline__ float wave_sum(float v) {
  v = ror_add<8>(v); v = ror_add<4>(v); v = ror_add<2>(v); v = ror_add<1>(v);
  return x32_add(x16_add(v));
}
__device__ __forceinline__ float wave_max(float v) {
  v = ror_max<8>(v); v = ror_max<4>(v); v = ror_max<2>(v); v = ror_max<1>(v);
  return x32_max(x16_max(v));
}

__device__ __forceinline__ u32 swz(u32 row) { return ((row & 3u) << 2) | ((row >> 2) & 3u); }
__device__ __forceinline__ u32 off_b(u32 row, u32 ch) { return 256u * row + 16u * (ch ^ swz(row)); }

constexpr u32 L_RA = 0;
constexpr u32 L_UV = 65536;
constexpr u32 L_WO = 98304;
constexpr u32 L_WQ = L_WO;
constexpr u32 L_W1 = L_WO + 14336;
constexpr u32 L_W2 = 126976;
constexpr u32 L_XN = 134144;
constexpr u32 L_P = L_XN + 1792;
constexpr u32 L_QST = L_P;
constexpr u32 L_SRED = L_P;
constexpr u32 L_AT = L_P + 16896;
constexpr u32 L_WG = L_AT + 4608;
constexpr u32 L_VST = L_WG + 128;
constexpr u32 L_VH = L_VST + 128;
constexpr u32 L_FR = L_VH + 128;
constexpr u32 L_BI = L_FR + 256;
constexpr u32 L_XR = L_BI + 64;
constexpr u32 L_TAG = L_XR + 32;
constexpr u32 L_FLAG = L_TAG + 32;
constexpr u32 L_END = L_FLAG + 14 * 256;
static_assert(L_END <= 163840, "LDS");
constexpr u32 F_PROG = 0;
constexpr u32 F_COL = 256;
constexpr u32 F_UK = 512;
constexpr u32 F_RO = 768;
constexpr u32 F_SR = 1024;
constexpr u32 F_B = 1280;
constexpr u32 F_QP = 2560;
constexpr u32 F_XN = 2816;
constexpr u32 F_QS = 3072;
constexpr u32 F_E2 = 3328;
#define KBH 1
#define SELFPRE 15
#define MLW 5
#define TLBW 0
#define EARLYQ 1
#define GPOS 1
#define GE1 0
#define GE2 8
#define GE3 0
#define LORD 0
constexpr int P_WQ = 14, P_UK = 46, P_ROWS = 110, P_W1 = 118, P_UV = 150, P_W2 = 157, P_WO = 171;
constexpr int P_END = 171;
#define CPOS 0
#define LGATE 0

#define WIN 16
#define WIN2 16
#define WINC 16
#define WIN3 16
constexpr int WMAX = WIN > WIN2 ? (WIN > WIN3 ? WIN : WIN3) : (WIN2 > WIN3 ? WIN2 : WIN3);
#define AUX_COL 2
#define PSLP 2
#define QDLY 18
#define BDLY 22
#define CDLY 14
#define DDLY 12
#define FDLY 16
#define EDLY 12
#define QGATE 4
#define PSTR 128
#define PRE_AUX 0
#define PRE_SC 0
#define POUT 0
#define PRE_RD(off) __builtin_amdgcn_raw_buffer_load_b32(rw, (off), 0, PRE_AUX)
#define PRE 110
#define CPRIO 2
#define SPRIO 3
constexpr u32 LSPIN = 1u << 22;

#define SPIN_FAIL(code) if (++spins > SPIN_CAP) { if (lane == 0) __hip_atomic_store(tmo, (code), RLX_AGENT); break; } __builtin_amdgcn_s_sleep(PSLP); asm volatile("" ::: "memory");

#define PIN(v) asm volatile("" : "+v"(v))
#define PINS(v) asm volatile("" : "+s"(v))
#define PISSUE(v, N, off, stride) _Pragma("unroll") for (int s_ = 0; s_ < (N); ++s_) v[s_] = __builtin_amdgcn_raw_buffer_load_b128(rw, (off) + ((s_ * (stride)) & 4095u), (s_ * (stride)) & ~4095u, 16);
#define PBAD(v) __builtin_amdgcn_bitop3_b32((v)[1], (v)[3], tag, 0x7E)
#define ANY(x) (__builtin_amdgcn_ballot_w64((x) != 0u) != 0ull)
#define RB "96"
#define RD(k) "v[" RB "+4*" #k ":" RB "+4*" #k "+3]"
#define RT(t, k) "v_bitop3_b32 " t ", v[" RB "+4*" #k "+1], v[" RB "+4*" #k "+3], %[tag] bitop3:0x7e\n"
#define RL(k, hi, lo) "s_movk_i32 %[x], " #hi "\n buffer_load_dwordx4 " RD(k) ", %[off], %[rs], %[x] offen offset:" #lo " sc1\n"
#define RIF(g, body) "s_bitcmp1_b32 %[pe], " #g "\n s_cbranch_scc0 1" #g "f\n" body "1" #g ":\n"
#define RM(t, bit) "v_cmp_ne_u32_e32 vcc, 0, " t "\n s_cmp_lg_u64 vcc, 0\n s_cselect_b32 %[x], " #bit ", 0\n s_or_b32 %[pe], %[pe], %[x]\n"
#define STR_(x) #x
#define STR(x) STR_(x)
#define RHEAD "s_nop 7\n s_setprio 0\n s_mov_b32 %[n], 0x40000\n s_branch 3f\n 1:\n s_sleep " STR(PSLP) "\n"
#define RTAIL4 "v_or3_b32 %[u], %[t0], %[t1], %[t2]\n v_or_b32_e32 %[u], %[u], %[t3]\n v_cmp_ne_u32_e32 vcc, 0, %[u]\n s_cbranch_vccz 2f\n s_mov_b32 %[pe], 0\n" \
               RM("%[t0]", 1) RM("%[t1]", 2) RM("%[t2]", 4) RM("%[t3]", 8) "s_sub_u32 %[n], %[n], 1\n s_cbranch_scc0 1b\n 2:\n s_setprio " STR(CPRIO)
#define RTMO(code) if (__builtin_expect(n_ == ~0u, 0)) { if (lane == 0) __hip_atomic_store(tmo, (code), RLX_AGENT); }
#define RETRY1(v0, K, REG, of, code) { u32 n_, t_; \
    asm volatile("s_setprio 0\n s_mov_b32 %[n], 0x40000\n 1:\n s_sleep " STR(PSLP) "\n buffer_load_dwordx4 " RD(K) ", %[off], %[rs], 0 offen sc1\n s_sub_u32 %[n], %[n], 1\n" \
                 "s_waitcnt vmcnt(0)\n s_cbranch_scc1 2f\n" RT("%[t]", K) "v_cmp_ne_u32_e32 vcc, 0, %[t]\n s_cbranch_vccnz 1b\n 2:\n s_setprio " STR(CPRIO) \
                 : "+{" REG "}"(v0), [n] "=&s"(n_), [t] "=&v"(t_) : [off] "v"(of), [rs] "s"(rw4), [tag] "s"(tag) : "vcc", "scc", "memory"); RTMO(code) }
#define RETRY4(v, of, code, h1, l1, h2, l2, h3, l3) { u32 n_, x_, pe_, t0_, t1_, t2_, t3_, u_; \
    asm volatile(RHEAD RIF(0, RL(0, 0, 0)) RIF(1, RL(1, h1, l1)) RIF(2, RL(2, h2, l2)) RIF(3, RL(3, h3, l3)) "s_waitcnt vmcnt(0)\n 3:\n" \
                 RT("%[t0]", 0) RT("%[t1]", 1) RT("%[t2]", 2) RT("%[t3]", 3) RTAIL4 \
                 : "+{v[96:99]}"(v[0]), "+{v[100:103]}"(v[1]), "+{v[104:107]}"(v[2]), "+{v[108:111]}"(v[3]), [n] "=&s"(n_), [x] "=&s"(x_), [pe] "=&s"(pe_), \
                   [t0] "=&v"(t0_), [t1] "=&v"(t1_), [t2] "=&v"(t2_), [t3] "=&v"(t3_), [u] "=&v"(u_) \
                 : [off] "v"(of), [rs] "s"(rw4), [tag] "s"(tag) : "vcc", "scc", "memory"); RTMO(code) }
#define RT2(t, k0, k1) RT(t, k0) RT("%[u]", k1) "v_or_b32_e32 " t ", " t ", %[u]\n"
#define RETRY8(v, of, code) { u32 n_, x_, pe_, t0_, t1_, t2_, t3_, u_; \
    asm volatile(RHEAD RIF(0, RL(0, 0, 0) RL(1, 0, 1024)) RIF(1, RL(2, 0, 2048) RL(3, 0, 3072)) RIF(2, RL(4, 4096, 0) RL(5, 4096, 1024)) RIF(3, RL(6, 4096, 2048) RL(7, 4096, 3072)) \
                 "s_waitcnt vmcnt(0)\n 3:\n" RT2("%[t0]", 0, 1) RT2("%[t1]", 2, 3) RT2("%[t2]", 4, 5) RT2("%[t3]", 6, 7) RTAIL4 \
                 : "+{v[96:99]}"(v[0]), "+{v[100:103]}"(v[1]), "+{v[104:107]}"(v[2]), "+{v[108:111]}"(v[3]), \
                   "+{v[112:115]}"(v[4]), "+{v[116:119]}"(v[5]), "+{v[120:123]}"(v[6]), "+{v[124:127]}"(v[7]), [n] "=&s"(n_), [x] "=&s"(x_), [pe] "=&s"(pe_), \
                   [t0] "=&v"(t0_), [t1] "=&v"(t1_), [t2] "=&v"(t2_), [t3] "=&v"(t3_), [u] "=&v"(u_) \
                 : [off] "v"(of), [rs] "s"(rw4), [tag] "s"(tag) : "vcc", "scc", "memory"); RTMO(code) }
#define PCHECK1(v, K, REG, off, code) if (__builtin_expect(ANY(PBAD(v[0])), 0)) RETRY1(v[0], K, REG, off, code)
#define PCHECK4(v, off, code, h1, l1, h2, l2, h3, l3) if (__builtin_expect(ANY((PBAD(v[0]) | PBAD(v[1])) | (PBAD(v[2]) | PBAD(v[3]))), 0)) RETRY4(v, off, code, h1, l1, h2, l2, h3, l3)
#define STM(d, off, mask) asm volatile("s_mov_b64 exec, %3\n buffer_store_dwordx4 %0, %1, %2, 0 offen sc1\n s_mov_b64 exec, -1" :: "v"(d), "v"(off), "s"(rw4), "s"(mask) : "memory")
#define STA(d, off) asm volatile("buffer_store_dwordx4 %0, %1, %2, 0 offen sc1" :: "v"(d), "v"(off), "s"(rw4) : "memory")
#define GRAN(d) v4u d = {0u, tag, 0u, tag}; PIN(d)
#define SCHED() __builtin_amdgcn_sched_barrier(0)

#define DPPMAX(n) "s_nop 1\n v_max_f32_dpp %0, %0, %0 row_ror:" #n " row_mask:0xf bank_mask:0xf\n"
__device__ __forceinline__ void wave_max2(float& a, float& b, float& jn) {
  asm volatile("s_nop 1\n v_permlane32_swap_b32 %0, %1\n v_max_f32 %0, %0, %1\n v_mov_b32 %1, %0\n"
               "s_nop 1\n v_permlane16_swap_b32 %0, %1\n v_max_f32 %0, %0, %1\n"
               DPPMAX(8) DPPMAX(4) DPPMAX(2) DPPMAX(1)
               "v_mov_b32 %2, %0\n v_mov_b32 %1, %0\n s_nop 1\n v_permlane32_swap_b32 %0, %1\n s_nop 1"
               : "+v"(a), "+v"(b), "=&v"(jn));
}
__device__ __forceinline__ float row_max(float a) {
  asm volatile(DPPMAX(8) DPPMAX(4) DPPMAX(2) DPPMAX(1) "s_nop 1" : "+v"(a));
  return a;
}
__device__ __forceinline__ float j32_add(float a, float b) {
  auto p = __builtin_amdgcn_permlane32_swap(__float_as_uint(a), __float_as_uint(b), false, false);
  return __uint_as_float(p[0]) + __uint_as_float(p[1]);
}
__device__ __forceinline__ float j16_add(float a, float b) {
  auto p = __builtin_amdgcn_permlane16_swap(__float_as_uint(a), __float_as_uint(b), false, false);
  return __uint_as_float(p[0]) + __uint_as_float(p[1]);
}
typedef __attribute__((ext_vector_type(16))) float v16f;
#define COLNT " nt"
__device__ __forceinline__ v4u mk_rsrc(const void* p, u32 bytes) {
  const u64 a = (u64)(uintptr_t)p;
  return v4u{(u32)a, (u32)(a >> 32) & 0xffffu, bytes, 0x00020000u};
}
#define COL_REQ(V) "s_nop 0\n buffer_load_dwordx4 " V ", %2, %0 offen" COLNT " lds\n s_add_u32 m0, m0, 0x400\n s_add_u32 %0, %0, 0x2000\n"
__device__ __forceinline__ void col_reads(v4u rk4, u32 vk, u32 ck, u32 ld) {
  u32 t0, t1;
  asm volatile(
      "s_mov_b32 %1, m0\n s_mov_b32 m0, %8\n s_mov_b32 %0, %7\n"
      COL_REQ("%3") COL_REQ("%4") COL_REQ("%5") COL_REQ("%6") COL_REQ("%3") COL_REQ("%4") COL_REQ("%5") COL_REQ("%6")
      "s_mov_b32 m0, %1\n"
      : "=&s"(t0), "=&s"(t1)
      : "s"(rk4), "v"(vk), "v"(vk ^ 16u), "v"(vk ^ 32u), "v"(vk ^ 48u), "s"(ck), "s"(ld)
      : "memory", "scc");
}

__device__ __forceinline__ void st_gran(char* ws, u32 off, u32 tag, u32 val) {
  __hip_atomic_store((gu64*)(ws + off), ((u64)tag << 32) | val, RLX_AGENT);
}
#define ST2(off, a, b) __builtin_amdgcn_raw_buffer_store_b128(v4u{(a), tag, (b), tag}, rw, (off), 0, 16)
template <int CTRL> __device__ __forceinline__ u32 dppu(u32 v) { return (u32)__builtin_amdgcn_update_dpp(0, (int)v, CTRL, 0xf, 0xf, true); }
__device__ __forceinline__ bf8 tr_pair(char* lds, u32 a0, u32 a1) {
  const s4 b0 = __builtin_amdgcn_ds_read_tr16_b64_v4i16((LAS s4*)(lds + a0));
  const s4 b1 = __builtin_amdgcn_ds_read_tr16_b64_v4i16((LAS s4*)(lds + a1));
  const s8 bb = {b0[0], b0[1], b0[2], b0[3], b1[0], b1[1], b1[2], b1[3]};
  return __builtin_bit_cast(bf8, bb);
}
__device__ __forceinline__ v4u lds16(char* lds, u32 a) { return *reinterpret_cast<const v4u*>(lds + a); }
__device__ __forceinline__ v4f lds16f(char* lds, u32 a) { return *reinterpret_cast<const v4f*>(lds + a); }
__device__ __forceinline__ float fdiv(float a, float d, float r) {
  const float q = a * r;
  return fmaf(fmaf(-q, d, a), r, q);
}
__device__ __forceinline__ v4u frag(v4u v, int pa) {
  v4u o;
#pragma unroll
  for (int e = 0; e < 4; ++e) o[e] = (u32)__builtin_amdgcn_ds_bpermute(pa, (int)v[e]);
  return o;
}
__device__ __forceinline__ void fl_write(u32 a, u32 v) { asm volatile("ds_write_b32 %0, %1" :: "v"(a), "v"(v) : "memory"); }
#define FL_ADD(F) asm volatile("ds_add_u32 %0, %1 offset:%2" :: "v"(fa), "v"(one), "n"(F) : "memory")
template <u32 F> __device__ __forceinline__ int fl_read(u32 fr) {
  u32 v; asm volatile("ds_read_b32 %0, %1 offset:%2\n s_waitcnt lgkmcnt(0)" : "=v"(v) : "v"(fr), "n"(F) : "memory");
  return (int)__builtin_amdgcn_readfirstlane(v);
}
#define WSL 0
#define WSLP ""
#define FL_LOOP "s_mov_b32 %1, 0x400000\n 1:\n ds_read_b32 %0, %2 offset:%3\n s_sub_u32 %1, %1, 1\n s_cbranch_scc1 2f\n" WSLP \
                "s_waitcnt lgkmcnt(0)\n v_cmp_gt_i32_e32 vcc, %4, %0\n s_cbranch_vccnz 1b\n 2:\n s_waitcnt lgkmcnt(0)"
template <u32 F, int V> __device__ __forceinline__ void fl_wait(u32 fr) {
  u32 v, n;
  asm volatile(FL_LOOP : "=&v"(v), "=&s"(n) : "v"(fr), "n"(F), "n"(V) : "vcc", "scc", "memory");
}
template <u32 F> __device__ __forceinline__ void fl_waits(u32 fr, int V) {
  u32 v, n;
  asm volatile(FL_LOOP : "=&v"(v), "=&s"(n) : "v"(fr), "n"(F), "s"(V) : "vcc", "scc", "memory");
}
template <int K> __device__ __forceinline__ void drain(u32 fp, int q, int lo) {
  asm volatile("s_waitcnt vmcnt(%0)" :: "n"(K) : "memory");
  fl_write(fp, (u32)(q - K > lo ? q - K : lo));
  if constexpr (K > 0) drain<K - 1>(fp, q, lo);
}
template <u32 F, int V> __device__ __forceinline__ void gate(u32 fp, int q, int& lo) {
  if (fl_read<F>(fp) >= V) return;
  if (lo < q) drain<WMAX - 2>(fp, q, lo);
  lo = q;
  fl_wait<F, V>(fp);
}
template <int N, int U, int XM, int SEG, int AUX, int W>
__device__ __forceinline__ void stream(char* lds, u32 lb, const void* base, u32 bytes, u32 vb, u32 sb, u32 ss, u32 ld, int q0, int& lo) {
  const __amdgpu_buffer_rsrc_t rs = __builtin_amdgcn_make_buffer_rsrc((void*)base, 0, bytes, 0x00020000);
#pragma unroll 1
  for (u32 k = 0; k < (u32)(N / U); ++k) {
    const int qk = q0 + (int)(k * U);
    const u32 sk = SEG == 1 ? sb + (k < 3u ? k + 1u : k == 3u ? 0u : k) * 256u : sb + k * (U * ss);
#pragma unroll
    for (u32 n = 0; n < (u32)U; ++n) {
      const int q = qk + (int)n;
      asm volatile("s_waitcnt vmcnt(%0)" :: "n"(W - 1) : "memory");
      lo = q - (W - 1) > lo ? q - (W - 1) : lo;
      fl_write(lb + L_FLAG, (u32)lo);
      const u32 xm = XM == 1 ? (n & 3u) << 4 : XM == 2 ? ((2u * n) & 15u) << 4 : XM == 3 ? (((n >> 2) & 1u) << 3) << 4 : 0u;
      if (XM == 4) {
        const u32 c = vb + (k * U + n) * 64u, rr = (c * 2341u) >> 16;
        __builtin_amdgcn_raw_ptr_buffer_load_lds(rs, (LAS void*)(lds + ld + k * (U * 1024u) + n * 1024u), 16, c * 16u + rr * 448u, sb, 0, AUX);
      } else
      __builtin_amdgcn_raw_ptr_buffer_load_lds(rs, (LAS void*)(lds + ld + k * (U * 1024u) + n * 1024u), 16, vb ^ xm, sk + n * ss, 0, AUX);
    }
  }
}

__global__ __launch_bounds__(576) void mla_decode(const u16* x, const u16* ckv, const u16* wq, const u16* wuk,
                                                  const u16* wuv, const u16* wo, const u16* w1, const u16* b1,
                                                  const u16* w2, const u16* b2, char* ws, u16* out) {
  __shared__ __attribute__((aligned(16))) char lds[L_END];
  const int t = threadIdx.x, lane = t & 63;
  const int w = __builtin_amdgcn_readfirstlane(t >> 6);
  const int i = blockIdx.x, xcd = i & 7, r = i >> 3;
  const int h1i = xcd * 2 + (r >> 4), cs = r & 15;
  const int G = xcd * 4 + (r >> 3), b = G >> 4, g = G & 15, j = r & 7;
  const int ch = r & 1, hd = xcd * 16 + (r >> 1), ds = hd & 7;
  const int fsl = hd < 112 ? hd : 0;
  STAMP_DECL
  STAMP(0);
  asm volatile("" : "+s"(x), "+s"(ckv), "+s"(wq), "+s"(wuk), "+s"(wuv), "+s"(wo), "+s"(w1), "+s"(b1), "+s"(w2), "+s"(b2), "+s"(ws), "+s"(out));
  gu32* tmo = (gu32*)(ws + WS_EP + 128);
  const __amdgpu_buffer_rsrc_t rw = __builtin_amdgcn_make_buffer_rsrc(ws, 0, WS_END, 0x00020000);
  const u32 gq = lane >> 4, qq = (lane & 15) >> 2, pp = lane & 3;

  const u32 lb = (u32)(uintptr_t)(LAS char*)lds;
  u32 fr = lb + L_FLAG, fa = lb + L_FLAG + (u32)lane * 4u, one = 1u;

  if (w == 8) {
    __builtin_amdgcn_s_setprio(SPRIO);
    if (lane < 14) fl_write(lb + L_FLAG + lane * 256, 0u);
    int lo = 0;
    const u32 hb = (u32)lane >> 5;
    const u32 vk = gq * 2048u + (((u32)lane & 15u) ^ (gq << 2)) * 16u;
    stream<14, 7, 0, 0, 0, WIN>(lds, lb, wq, M_ * 8192u, (u32)(lane >> 3) * 8192u + (((u32)lane & 7u) ^ ((((u32)lane >> 3) & 3u) << 1)) * 16u, (u32)(cs & 3) * (112u * 8192u) + (u32)h1i * 512u + (u32)(cs >> 2) * 128u, 8u * 8192u, L_WQ, 0, lo);
    BAR();
    stream<32, 8, 2, 0, 2, WIN>(lds, lb, wuk, 1024u * 8192u, hb * 8192u + (((u32)lane & 31u) ^ hb) * 16u, (u32)(cs * 64) * 8192u + (u32)h1i * 512u, 16384u, L_UV, P_UK - 32, lo);
    stream<64, 8, 1, 1, 0, WIN>(lds, lb, ckv, 2u * SK * DC * 2u, vk, (u32)(b * SK + g * 256 + j * 32) * 2048u, 8192u, L_RA, P_UK, lo);
    STAMP(25);
    const bool hp = xcd < 7 && r < 28;
    const u32 fg = (u32)xcd * 4u + (u32)(r & 3), ks = (u32)r >> 2;
    const u32 sw = (((u32)lane & 7u) ^ ((((u32)lane >> 3) & 3u) << 1)) * 16u;
    stream<8, 8, 0, 0, 2, WIN>(lds, lb, w1, hp ? M_ * F_ * 2u : 0u, (u32)(lane >> 3) * (F_ * 2u) + sw, hp ? ks * (64u * F_ * 2u) + fg * 128u : 0u, 8u * F_ * 2u, L_W1, P_ROWS, lo);
    gate<F_UK, 4>(fr, P_UV - 32, lo);
    stream<32, 8, 3, 0, 2, WIN2>(lds, lb, wuv, 1024u * 8192u, hb * 8192u + (((u32)lane & 31u) ^ (hb << 2)) * 16u, (u32)(cs * 64) * 8192u + (u32)h1i * 512u, 16384u, L_UV, P_UV - 32, lo);
    STAMP(26);
    drain<WMAX - 2>(fr, P_UV, lo);
    lo = P_UV;
    const u32 tag = (u32)__builtin_amdgcn_readfirstlane(*reinterpret_cast<const u32*>(lds + L_TAG));
    if (hp) {
      const u32 ao = L_XN + (u32)(lane & 1) * 896u + ks * 128u + gq * 16u;
      const bf8 a0 = as_bf8(lds16(lds, ao)), a1 = as_bf8(lds16(lds, ao + 64));
      float h0 = 0.f, h1 = 0.f;
#pragma unroll
      for (u32 T = 0; T < 4; ++T) {
        const u32 bo = L_W1 + (8u * gq + qq) * 128u + (((T * 2u + (pp >> 1)) ^ (qq << 1)) * 16u) + (pp & 1u) * 8u;
        v4f acc = {0.f, 0.f, 0.f, 0.f};
        acc = __builtin_amdgcn_mfma_f32_16x16x32_bf16(a0, tr_pair(lds, bo, bo + 512), acc, 0, 0, 0);
        acc = __builtin_amdgcn_mfma_f32_16x16x32_bf16(a1, tr_pair(lds, bo + 4096, bo + 4096 + 512), acc, 0, 0, 0);
        if (gq == T) { h0 = acc[0]; h1 = acc[1]; }
      }
      ST2(WS_HP + (((fg * 4u + gq) * 7u + ks) * 16u + ((u32)lane & 15u)) * 16u, __float_as_uint(h0), __float_as_uint(h1));
    }
    STAMP(30);
    if (LGATE == 0) fl_wait<F_E2, 8>(fr); else fl_wait<F_B + 256, 8>(fr);
    STAMP(31);
    stream<7, 7, 4, 0, 2, WIN3>(lds, lb, w2, 1792u * 896u, (u32)lane, (u32)(fsl * 16) * 896u + (u32)ch * 448u, 0u, L_W2, P_W2 - 7, lo);
    STAMP(27);
    stream<14, 7, 4, 0, 2, WIN3>(lds, lb, wo, 4096u * 896u, (u32)lane, (u32)(hd * 32) * 896u + (u32)ch * 448u, 0u, L_WO, P_W2, lo);
    STAMP(28);
    {
      float a0 = 0.f, a1 = 0.f;
      if (hd < 112) {
        const u32 ho = WS_HP + (u32)hd * 1792u + (u32)lane * 16u;
        const u32 ho2 = ho + (lane < 48 ? 1024u : 768u);
        v4u u0, u1;
#pragma unroll 1
        for (u32 spins = 0;;) {
          u0 = __builtin_amdgcn_raw_buffer_load_b128(rw, ho, 0, 16);
          u1 = __builtin_amdgcn_raw_buffer_load_b128(rw, ho2, 0, 16);
          if (!ANY(PBAD(u0) | PBAD(u1))) break;
          SPIN_FAIL(8u)
        }
        a0 = __uint_as_float(u0[0]) + (lane < 48 ? __uint_as_float(u1[0]) : 0.f);
        a1 = __uint_as_float(u0[2]) + (lane < 48 ? __uint_as_float(u1[2]) : 0.f);
        a0 = x32_add(x16_add(a0)); a1 = x32_add(x16_add(a1));
      }
      const float hv = rbf(rbf(lane >= 32 ? a1 : a0) + *reinterpret_cast<const float*>(lds + L_BI + (lane & 15) * 4));
      const float ge = 0.5f * hv * (1.f + tanhf(0.7978845608028654f * (hv + 0.044715f * hv * hv * hv)));
      *reinterpret_cast<u16*>(lds + L_VH + lane * 2) = ((lane & 16) == 0 && hd < 112) ? f2bf(ge) : (u16)0;
      asm volatile("s_waitcnt lgkmcnt(0)" ::: "memory");
      STAMP(24);
    }
    drain<0>(fr, P_END, lo);
    STAMP(29);
    STAMP_FLUSH;
    return;
  }

  __builtin_amdgcn_s_setprio(CPRIO);
  u32 epv = __builtin_amdgcn_raw_buffer_load_b32(rw, WS_EP, 0, 0);
  const v4u rw4 = mk_rsrc(ws, WS_END);
  const int pa = (4 * (lane & 15) + (lane >> 4)) * 4;
  const int fb = i >= 56 ? 1 : 0, fc = i - fb * 56;
  u64 m_ev = 0x5555555555555555ull;
  PIN(fr); PIN(fa); PIN(one); PINS(m_ev);
  u32 tag, pre0 = 0;
  if (w == 0) {
    const int lc = lane < 56 ? lane : 0;
    const v4u xr0 = ldp16(x + lc * 8), xr1 = ldp16(x + M_ + lc * 8);
    STAMP(21);
    BAR();
    float xa[8], xb[8];
#pragma unroll
    for (int p = 0; p < 4; ++p) { xa[2 * p] = lo_f(xr0[p]); xa[2 * p + 1] = hi_f(xr0[p]); xb[2 * p] = lo_f(xr1[p]); xb[2 * p + 1] = hi_f(xr1[p]); }
    const float act = lane < 56 ? 1.f : 0.f;
    float s0 = 0.f, s1 = 0.f;
#pragma unroll
    for (int k = 0; k < 8; ++k) { s0 += xa[k]; s1 += xb[k]; }
    s0 = wave_sum(s0 * act); s1 = wave_sum(s1 * act);
    const float m0 = s0 * (1.f / M_), m1 = s1 * (1.f / M_);
    float v0 = 0.f, v1 = 0.f;
#pragma unroll
    for (int k = 0; k < 8; ++k) { const float d0 = xa[k] - m0, d1 = xb[k] - m1; v0 += d0 * d0; v1 += d1 * d1; }
    v0 = wave_sum(v0 * act); v1 = wave_sum(v1 * act);
    const float mb0 = rbf(m0), mb1 = rbf(m1);
    const float den0 = rbf(sqrtf(rbf(rbf(v0 * (1.f / M_)) + 1e-5f)));
    const float den1 = rbf(sqrtf(rbf(rbf(v1 * (1.f / M_)) + 1e-5f)));
    const float r0 = __builtin_amdgcn_rcpf(den0), r1 = __builtin_amdgcn_rcpf(den1);
    v4u na, nb;
#pragma unroll
    for (int p = 0; p < 4; ++p) {
      na[p] = pk2(fdiv(rbf(xa[2 * p] - mb0), den0, r0), fdiv(rbf(xa[2 * p + 1] - mb0), den0, r0));
      nb[p] = pk2(fdiv(rbf(xb[2 * p] - mb1), den1, r1), fdiv(rbf(xb[2 * p + 1] - mb1), den1, r1));
    }
    if (lane < 56) {
      *reinterpret_cast<v4u*>(lds + L_XN + lane * 16) = na;
      *reinterpret_cast<v4u*>(lds + L_XN + 896 + lane * 16) = nb;
    }
    FL_ADD(F_XN);
    STAMP(17);
  } else if (w < 4) {
    u32 bu = 0, bw = 0;
    if (w == 1) bu = ldh(b1 + fsl * 16 + (lane & 15));
    if (w == 2 && i < 112) { bu = ldh(x + fb * M_ + fc * 8 + (lane & 7)); bw = ldh(b2 + fc * 8 + (lane & 7)); }
    if (w == 3) pre0 = PRE_RD(lane < 8 ? WS_QL + (u32)(lane >> 2) * 65536u + (u32)(cs * 4 + (lane & 3)) * 1024u + (u32)h1i * 64u : WS_QG + (u32)h1i * 16384u + (u32)(cs & 3) * 4096u + (u32)(cs >> 2) * 1024u + (u32)(lane & 7) * 128u);
    BAR();
    PIN(bu); PIN(bw);
    if (w < 3 && lane < 16) *reinterpret_cast<float*>(lds + (w == 1 ? L_BI + lane * 4 : L_XR + (lane & 7) * 4)) = lo_f(bu) + lo_f(bw);
  }
  if (w < 4) {
    u32 ao = L_XN + (u32)(lane & 1) * 896u + (u32)(cs & 3) * 224u;
    const u32 ch = ((((u32)w * 2u + (pp >> 1)) ^ (qq << 1)) * 16u) + (pp & 1u) * 8u;
    u32 bo = L_WQ + (8u * gq + qq) * 128u + ch, bo2 = L_WQ + (96u + 4u * gq + qq) * 128u + ch;
    u32 qo = WS_QG + (u32)h1i * 16384u + (u32)(cs & 3) * 4096u + (u32)(cs >> 2) * 1024u + (u32)w * 256u + (u32)lane * 8u;
    u64 m_q = 0x55555555ull;
    tag = __builtin_amdgcn_readfirstlane(epv) + 1u;
    GRAN(d);
    PIN(bo); PIN(bo2); PIN(qo); PINS(m_q);
    if (w) fl_wait<F_XN, 1>(fr);
    v4u a0 = lds16(lds, ao + gq * 16u), a1 = lds16(lds, ao + gq * 16u + 64), a2 = lds16(lds, ao + gq * 16u + 128);
    s4 a3 = *reinterpret_cast<const s4*>(lds + ao + 192u + gq * 8u);
    asm volatile("" : "+v"(a0), "+v"(a1), "+v"(a2), "+v"(a3));
    fl_wait<F_PROG, P_WQ>(fr);
    STAMP(18);
    const bf8 b0 = tr_pair(lds, bo, bo + 512), b1f = tr_pair(lds, bo + 4096, bo + 4096 + 512), b2f = tr_pair(lds, bo + 8192, bo + 8192 + 512);
    const s4 b3 = __builtin_amdgcn_ds_read_tr16_b64_v4i16((LAS s4*)(lds + bo2));
    SCHED();
    v4f acc = {0.f, 0.f, 0.f, 0.f}, acd = {0.f, 0.f, 0.f, 0.f};
    acc = __builtin_amdgcn_mfma_f32_16x16x32_bf16(as_bf8(a0), b0, acc, 0, 0, 0);
    acc = __builtin_amdgcn_mfma_f32_16x16x32_bf16(as_bf8(a1), b1f, acc, 0, 0, 0);
    acc = __builtin_amdgcn_mfma_f32_16x16x32_bf16(as_bf8(a2), b2f, acc, 0, 0, 0);
    acd = __builtin_amdgcn_mfma_f32_16x16x16bf16_1k(a3, b3, acd, 0, 0, 0);
    d[0] = __float_as_uint(gq ? acc[1] + acd[1] : acc[0] + acd[0]);
    d[2] = dppu<0xB1>(d[0]);
    STM(d, qo, m_q);
    STAMP(1);
    FL_ADD(F_RO);
    asm volatile("" :: "v"(pre0));
    if (w == 3) pre0 = PRE_RD(lane < 32 ? WS_SC + (u32)((G * 16 + (lane >> 1)) * 256 + j * 32) * 8u + (u32)(lane & 1) * 128u : WS_ML + (u32)((b * 16 + 2 * j + (lane & 1)) * 16 + (g & 8)) * 16u);
  } else {
    v4u wk[8];
    BAR();
    asm volatile("v_readfirstlane_b32 %0, %1" : "=s"(tag) : "v"(epv));
    tag += 1u;
    *reinterpret_cast<u32*>(lds + L_TAG) = tag;
    u32 qo = WS_QG + (u32)h1i * 16384u + (u32)(w - 4) * 1024u + (u32)lane * 16u;
    u32 qw = L_QST + (u32)((lane >> 3) & 1) * 512u + (u32)(w - 4) * 128u + gq * 32u + (u32)(lane & 7) * 4u;
    u32 ao = L_QST + (u32)(lane & 1) * 512u + gq * 16u;
    const int bb = lane & 1, wq4 = w - 4;
    u32 qa = WS_QL + (u32)bb * 65536u + (u32)((cs * 2 + (wq4 >> 1)) * 2 + ((lane >> 2) & 1)) * 1024u + (u32)h1i * 64u
             + (u32)((wq4 & 1) * 2 + ((lane >> 3) & 1)) * 16u;
    u64 m_l = 0x3333ull;
    GRAN(d);
    PIN(qo); PIN(qw); PIN(ao); PIN(qa); PINS(m_l);
    fl_wait<F_RO, QGATE>(fr);
    if (QDLY) __builtin_amdgcn_s_sleep(QDLY);
    STAMP(19);
    v4u ga[4];
    PISSUE(ga, 4, qo, 4096u)
    fl_wait<F_PROG, P_UK>(fr);
    {
      const u32 ko = L_UV + (u32)((w - 4) * 16 + (lane & 15)) * 512u;
#pragma unroll
      for (int s = 0; s < 8; ++s) wk[s] = lds16(lds, ko + ((((u32)s * 4u + gq) ^ ((u32)lane & 15u)) * 16u));
    }
    FL_ADD(F_UK);
    PCHECK4(ga, qo, 1u, 4096, 0, 8192, 0, 12288, 0)
    {
      const float q0 = (__uint_as_float(ga[0][0]) + __uint_as_float(ga[1][0])) + (__uint_as_float(ga[2][0]) + __uint_as_float(ga[3][0]));
      const float q1 = (__uint_as_float(ga[0][2]) + __uint_as_float(ga[1][2])) + (__uint_as_float(ga[2][2]) + __uint_as_float(ga[3][2]));
      *reinterpret_cast<u32*>(lds + qw) = pk2(q0, q1);
    }
    FL_ADD(F_QS);
    STAMP(20);
    fl_wait<F_QS, 4>(fr);
    v4u qf[8];
#pragma unroll
    for (int s = 0; s < 8; ++s) qf[s] = lds16(lds, ao + s * 64);
    SCHED();
    v4f acc = {0.f, 0.f, 0.f, 0.f};
#pragma unroll
    for (int s = 0; s < 8; ++s) acc = __builtin_amdgcn_mfma_f32_16x16x32_bf16(as_bf8(qf[s]), as_bf8(wk[s]), acc, 0, 0, 0);
    const float a0 = acc[0], a1 = acc[1];
    const float own = bb ? a1 : a0, oth = dppf<0xB1>(bb ? a0 : a1);
    d[0] = pk2(bb ? oth : own, bb ? own : oth);
    d[2] = dppu<0x4E>(d[0]);
    STM(d, qa, m_l);
    STAMP(2);
    if (BDLY) __builtin_amdgcn_s_sleep(BDLY);
    FL_ADD(F_QP);
  }
  v4u bv[8];
  const int slot = w == 0 ? 3 : w < 4 ? w - 1 : w;
  {
    const u32 key = (u32)lane & 15u;
    u32 ro[4];
#pragma unroll
    for (int s = 0; s < 4; ++s) { ro[s] = L_RA + (u32)slot * 8192u + key * 256u + ((((u32)s * 4u + gq) ^ swz(key)) * 16u); PIN(ro[s]); }
    fl_waits<F_PROG>(fr, P_UK + 8 * (slot + 1));
#pragma unroll
    for (int s = 0; s < 8; ++s) bv[s] = lds16(lds, ro[s & 3] + (u32)(s >> 2) * 4096u);
  }
  STAMP(3);
  u32 so = WS_SC + (u32)((G * 16 + 2 * w) * 256 + 2 * lane) * 8u;
  {
    u32 qo = WS_QL + (u32)b * 65536u + (u32)w * 8192u + (u32)lane * 16u;
    u32 srw = L_SRED + (u32)((w * 16 + (int)gq * 4) * 33 + (lane & 15)) * 4u;
    u32 srr = L_SRED + (u32)((t >> 5) * 33 + (t & 31)) * 4u;
    u32 sco = WS_SC + (u32)((G * 16 + (t >> 5)) * 256 + j * 32 + (t & 31)) * 8u;
    u32 vk = gq * 2048u + (((u32)lane & 15u) ^ (gq << 2)) * 16u;
    u32 ck = (u32)(b * SK + g * 256 + slot * 32) * 2048u + (u32)j * 256u, cl = lb + L_RA + (u32)slot * 8192u;
    int pav = pa;
    GRAN(d);
    PIN(qo); PIN(srw); PIN(srr); PIN(sco); PIN(so); PIN(pav); PIN(vk); PINS(ck); PINS(cl);
    v4u gl[8];
    const v4f z = {0.f, 0.f, 0.f, 0.f};
    v4f acc0, acc1;
    fl_wait<F_QP, 4>(fr);
    PISSUE(gl, 8, qo, 1024u)
#define BSTEP(s) { const v4u av = frag(v4u{gl[2 * s][0], gl[2 * s][2], gl[2 * s + 1][0], gl[2 * s + 1][2]}, pav); \
      acc0 = __builtin_amdgcn_mfma_f32_16x16x32_bf16(as_bf8(av), as_bf8(bv[s]), s ? acc0 : z, 0, 0, 0); \
      acc1 = __builtin_amdgcn_mfma_f32_16x16x32_bf16(as_bf8(av), as_bf8(bv[4 + s]), s ? acc1 : z, 0, 0, 0); }
    u32 bad = 0;
#pragma unroll
    for (int s = 0; s < 4; ++s) { bad |= PBAD(gl[2 * s]) | PBAD(gl[2 * s + 1]); BSTEP(s) }
    if (__builtin_expect(ANY(bad), 0)) {
      RETRY8(gl, qo, 2u)
      v4u aw[4];
#pragma unroll
      for (int s = 0; s < 4; ++s) aw[s] = frag(v4u{gl[2 * s][0], gl[2 * s][2], gl[2 * s + 1][0], gl[2 * s + 1][2]}, pav);
      SCHED();
#pragma unroll
      for (int s = 0; s < 4; ++s) {
        acc0 = __builtin_amdgcn_mfma_f32_16x16x32_bf16(as_bf8(aw[s]), as_bf8(bv[s]), s ? acc0 : z, 0, 0, 0);
        acc1 = __builtin_amdgcn_mfma_f32_16x16x32_bf16(as_bf8(aw[s]), as_bf8(bv[4 + s]), s ? acc1 : z, 0, 0, 0);
      }
    }
    asm volatile("" :: "v"(pre0));
    STAMP(4);
    float* sr = reinterpret_cast<float*>(lds + srw);
#pragma unroll
    for (int q = 0; q < 4; ++q) { sr[q * 33] = acc0[q]; sr[q * 33 + 16] = acc1[q]; }
    FL_ADD(F_B);
    fl_wait<F_B, 8>(fr);
    STAMP(5);
    const float* sp = reinterpret_cast<const float*>(lds + srr);
    float s = ((sp[0] + sp[528]) + (sp[2 * 528] + sp[3 * 528])) + ((sp[4 * 528] + sp[5 * 528]) + (sp[6 * 528] + sp[7 * 528]));
    s *= 0.0625f * 1.4426950408889634f;
    d[0] = __float_as_uint(s);
    d[2] = dppu<0xB1>(d[0]);
    STM(d, sco, m_ev);
    FL_ADD(F_SR);
    col_reads(mk_rsrc(ckv, 2u * SK * DC * 2u), vk, ck, cl);
    if (w == 3) pre0 = PRE_RD(WS_PV + (u32)(((b * 16 + (lane >> 2)) * 16 + g) * 512 + j * 64) * 8u + (u32)(lane & 3) * 128u);
  }
  const u32 bq = (u32)w >> 2;
  u32 pvo = WS_PV + (((bq * 16u + (u32)h1i) * 16u + (u32)(w & 3) * 4u + gq) * 512u + (u32)cs * 32u + (u32)(lane & 15) * 2u) * 8u;
  {
    const u32 pf = qq + 4u * (gq & 1u);
    u32 pw = L_P + gq * 1024u + qq * 256u + (((u32)(2 * w) ^ pf) * 16u) + pp * 4u;
    u32 po0 = L_P + gq * 256u + ((((u32)lane & 15u) ^ gq) * 16u), po1 = po0 ^ 64u;
    u32 k0 = L_RA + off_b(8 * gq + qq, 2 * w + (pp >> 1)) + 8 * (pp & 1);
    u32 k1 = L_RA + off_b(8 * gq + 4 + qq, 2 * w + (pp >> 1)) + 8 * (pp & 1);
    const int hi8 = (lane >> 3) & 1, od = lane & 1;
    u32 go = WS_PV + (u32)(((b * 16 + (lane >> 4) * 4 + 2 * hi8 + od) * 16 + g) * 512 + j * 64 + w * 8 + (lane & 6)) * 8u;
    u32 pw1 = pw ^ 16u;
    GRAN(d);
    PIN(pw); PIN(pw1); PIN(po0); PIN(po1); PIN(k0); PIN(k1); PIN(go); PIN(pvo);
    v4u gs[4];
    bf8 kb[8];
    if (CDLY) __builtin_amdgcn_s_sleep(CDLY);
    PISSUE(gs, 4, so, 1024u)
    FL_ADD(F_E2);
    asm volatile("s_waitcnt vmcnt(4)" ::: "memory");
    FL_ADD(F_COL);
    fl_wait<F_COL, 8>(fr);
#pragma unroll
    for (int ks = 0; ks < 8; ++ks) kb[ks] = tr_pair(lds, k0 + ks * 8192, k1 + ks * 8192);
    asm volatile("" : "+v"(kb[0]), "+v"(kb[1]), "+v"(kb[2]), "+v"(kb[3]), "+v"(kb[4]), "+v"(kb[5]), "+v"(kb[6]), "+v"(kb[7]));
    PCHECK4(gs, so, 3u, 0, 1024, 0, 2048, 0, 3072)
    asm volatile("" :: "v"(pre0));
    STAMP(6);
    float sv[8];
#pragma unroll
    for (int q = 0; q < 4; ++q) { sv[2 * q] = __uint_as_float(gs[q][0]); sv[2 * q + 1] = __uint_as_float(gs[q][2]); }
    float m0 = fmaxf(fmaxf(sv[0], sv[1]), fmaxf(sv[2], sv[3])), m1 = fmaxf(fmaxf(sv[4], sv[5]), fmaxf(sv[6], sv[7])), mj;
    wave_max2(m0, m1, mj);
    u32 pk[4];
#pragma unroll
    for (int q = 0; q < 4; ++q) {
      const float m = q < 2 ? m0 : m1;
      pk[q] = pk2(__builtin_amdgcn_exp2f(sv[2 * q] - m), __builtin_amdgcn_exp2f(sv[2 * q + 1] - m));
      *reinterpret_cast<u32*>(lds + ((q >> 1) ? pw1 : pw) + (q & 1) * 4096) = pk[q];
    }
    FL_ADD(F_B + 256);
    if (w == j) {
      float l = j32_add((lo_f(pk[0]) + hi_f(pk[0])) + (lo_f(pk[1]) + hi_f(pk[1])), (lo_f(pk[2]) + hi_f(pk[2])) + (lo_f(pk[3]) + hi_f(pk[3])));
      l = x16_add(l);
      l = ror_add<8>(l); l = ror_add<4>(l); l = ror_add<2>(l); l = ror_add<1>(l);
      if ((lane & 31) == 0) ST2(WS_ML + (u32)((b * 16 + 2 * w + (lane >> 5)) * 16 + g) * 16u, __float_as_uint(mj), __float_as_uint(l));
    }
    STAMP(22);
    fl_wait<F_B + 256, 8>(fr);
    STAMP(7);
    v4u pf8[8];
#pragma unroll
    for (int ks = 0; ks < 8; ++ks) pf8[ks] = lds16(lds, ((ks & 1) ? po1 : po0) + ks * 1024);
    SCHED();
    v4f acc = {0.f, 0.f, 0.f, 0.f};
#pragma unroll
    for (int ks = 0; ks < 8; ++ks) acc = __builtin_amdgcn_mfma_f32_16x16x32_bf16(as_bf8(pf8[ks]), kb[ks], acc, 0, 0, 0);
    const float s0 = hi8 ? acc[0] : acc[2], s1 = hi8 ? acc[1] : acc[3];
    const float n0 = dppf<0x128>(s0), n1 = dppf<0x128>(s1);
    const u32 g0 = pk2(hi8 ? n0 : acc[0], hi8 ? acc[2] : n0);
    const u32 g1 = pk2(hi8 ? n1 : acc[1], hi8 ? acc[3] : n1);
    const u32 gn = dppu<0xB1>(od ? g0 : g1);
    d[0] = od ? gn : g0; d[2] = od ? g1 : gn;
    STA(d, go);
  }
  if (w == 3) pre0 = PRE_RD(lane < 32 ? WS_CP + ((u32)(h1i * 16 + (lane >> 1)) * 16u + (u32)cs) * 256u + (u32)(lane & 1) * 128u
                                      : WS_OG + (u32)(((lane >> 4) & 1) * 128 + hd) * 3584u + (u32)ch * 1792u + (u32)((lane & 15) < 14 ? (lane & 15) : 0) * 128u);
  STAMP(8);
  u32 cvo = WS_CP + (((u32)h1i * 2u + bq) * 8u + (u32)ds) * 4096u + (u32)(lane >> 2) * 256u + (u32)(w & 3) * 64u + pp * 16u;
  {
    u32 aw = L_AT + (bq * 16u + (u32)(w & 3) * 4u + gq) * 144u + (u32)(lane & 15) * 8u;
    u32 ar = L_AT + (u32)(lane & 31) * 144u + (u32)(lane >> 5) * 16u;
    u32 wr = L_WG + (u32)(lane >> 5) * 16u;
    u32 bo = L_UV + ((gq >> 1) * 4u + (qq >> 1) + 8u * (qq & 1u)) * 512u
             + ((((u32)w * 4u + (gq & 1u) * 2u + (pp >> 1)) ^ (((qq >> 1) << 2) | ((qq & 1u) << 3))) * 16u) + (pp & 1u) * 8u;
    u32 co = WS_CP + ((((u32)h1i * 2u + (u32)(lane >> 5)) * 8u + (u32)w) * 16u + (u32)cs) * 256u + (u32)(lane & 31) * 8u;
    u32 mlo = WS_ML + (((gq & 1u) * 16u + (u32)h1i) * 16u + (u32)(lane & 15)) * 16u;
    u32 wgw = L_WG + (u32)(lane & 31) * 4u;
    GRAN(d);
    PIN(aw); PIN(ar); PIN(wr); PIN(bo); PIN(co); PIN(mlo); PIN(cvo); PIN(wgw);
    v4u pv[1], ml[1];
    bf8 ub[4];
    if (DDLY) __builtin_amdgcn_s_sleep(DDLY);
    if (w == MLW) { PISSUE(ml, 1, mlo, 0u) }
    PISSUE(pv, 1, pvo, 0u)
    const bool have = fl_read<F_PROG>(fr) >= P_UV;
    if (have) {
#pragma unroll
      for (int T = 0; T < 4; ++T) ub[T] = tr_pair(lds, bo + T * 8192, bo + T * 8192 + 1024);
    }
    if (w == MLW) {
      PCHECK1(ml, 1, "v[100:103]", mlo, 7u)
      const float mg = __uint_as_float(ml[0][0]);
      const float eg = __builtin_amdgcn_exp2f(mg - row_max(mg));
      float ls = eg * __uint_as_float(ml[0][2]);
      ls = ror_add<8>(ls); ls = ror_add<4>(ls); ls = ror_add<2>(ls); ls = ror_add<1>(ls);
      const float rl = __builtin_amdgcn_rcpf(ls);
      *reinterpret_cast<float*>(lds + wgw) = fdiv(eg, ls, rl);
    }
    PCHECK1(pv, 0, "v[96:99]", pvo, 4u)
    asm volatile("" :: "v"(pre0));
    STAMP(9);
    *reinterpret_cast<v2u*>(lds + aw) = v2u{pv[0][0], pv[0][2]};
    FL_ADD(F_B + 512);
    if (!have) {
      fl_wait<F_PROG, P_UV>(fr);
#pragma unroll
      for (int T = 0; T < 4; ++T) ub[T] = tr_pair(lds, bo + T * 8192, bo + T * 8192 + 1024);
    }
    STAMP(23);
    fl_wait<F_B + 512, 8>(fr);
    STAMP(10);
    v16f acc;
#pragma unroll
    for (int q = 0; q < 16; ++q) acc[q] = 0.f;
    v4u at[4];
#pragma unroll
    for (int T = 0; T < 4; ++T) at[T] = lds16(lds, ar + T * 32);
    v4f wt[4];
#pragma unroll
    for (int q = 0; q < 4; ++q) wt[q] = lds16f(lds, wr + q * 32);
    SCHED();
#pragma unroll
    for (int T = 0; T < 4; ++T) acc = __builtin_amdgcn_mfma_f32_32x32x16_bf16(as_bf8(at[T]), ub[T], acc, 0, 0, 0);
    float c[2];
#pragma unroll
    for (int e = 0; e < 2; ++e) {
      float u0 = wt[2 * e][0] * acc[8 * e], u1 = wt[2 * e][1] * acc[8 * e + 1];
      u0 = fmaf(wt[2 * e][2], acc[8 * e + 2], u0); u1 = fmaf(wt[2 * e][3], acc[8 * e + 3], u1);
      u0 = fmaf(wt[2 * e + 1][0], acc[8 * e + 4], u0); u1 = fmaf(wt[2 * e + 1][1], acc[8 * e + 5], u1);
      u0 = fmaf(wt[2 * e + 1][2], acc[8 * e + 6], u0); u1 = fmaf(wt[2 * e + 1][3], acc[8 * e + 7], u1);
      c[e] = u0 + u1;
    }
    d[0] = __float_as_uint(j32_add(c[0], c[1]));
    d[2] = dppu<0xB1>(d[0]);
    STM(d, co, m_ev);
  }
  STAMP(11);
  u32 oo = WS_OG + ((u32)(fb * 128 + (t >> 2)) * M_ + (u32)(fc * 8 + (t & 3) * 2)) * 8u;
  {
    u32 vw = L_VST + bq * 64u + (u32)(w & 3) * 16u + pp * 4u + (gq & 1u) * 2u;
    u32 va = L_VST + (u32)(lane & 1) * 64u + gq * 16u;
    u32 og = WS_OG + (u32)(((lane >> 5) * 128 + hd) * M_ + ch * 224 + w * 32 + (lane & 31)) * 8u;
    const u32 wc = w < 7 ? (u32)w : 0u;
    u32 bo0 = L_WO + (8u * gq + qq) * 448u + wc * 64u + pp * 8u;
    u32 bo1 = L_W2 + ((8u * gq + qq) & 15u) * 448u + wc * 64u + pp * 8u;
    u64 m_o = w < 7 ? m_ev : 0ull;
    GRAN(d);
    PIN(vw); PIN(va); PIN(og); PIN(bo0); PIN(bo1); PIN(oo); PINS(m_o);
    v4u cv[1];
    if (FDLY) __builtin_amdgcn_s_sleep(FDLY);
    PISSUE(cv, 1, cvo, 0u)
    fl_wait<F_PROG, P_END>(fr);
    const bf8 a1 = as_bf8(lds16(lds, va + (L_VH - L_VST)));
    const v4f z = {0.f, 0.f, 0.f, 0.f};
    v4f oa = __builtin_amdgcn_mfma_f32_16x16x32_bf16(a1, tr_pair(lds, bo1, bo1 + 1792), z, 0, 0, 0);
    v4f ob = __builtin_amdgcn_mfma_f32_16x16x32_bf16(a1, tr_pair(lds, bo1 + 32, bo1 + 32 + 1792), z, 0, 0, 0);
    bf8 wa = tr_pair(lds, bo0, bo0 + 1792), wb = tr_pair(lds, bo0 + 32, bo0 + 32 + 1792);
    asm volatile("" : "+v"(oa), "+v"(ob), "+v"(wa), "+v"(wb));
    SCHED();
    PCHECK1(cv, 0, "v[96:99]", cvo, 6u)
    float c = x32_add(j16_add(__uint_as_float(cv[0][0]), __uint_as_float(cv[0][2])));
    c = ror_add<4>(c); c = ror_add<8>(c);
    *reinterpret_cast<u16*>(lds + vw) = f2bf(c);
    STAMP(15);
    FL_ADD(F_B + 768);
    fl_wait<F_B + 768, 8>(fr);
    STAMP(16);
    const bf8 a0 = as_bf8(lds16(lds, va));
    oa = __builtin_amdgcn_mfma_f32_16x16x32_bf16(a0, wa, oa, 0, 0, 0);
    ob = __builtin_amdgcn_mfma_f32_16x16x32_bf16(a0, wb, ob, 0, 0, 0);
    const float o0 = (gq & 1u) ? ob[0] : oa[0], o1 = (gq & 1u) ? ob[1] : oa[1];
    d[0] = __float_as_uint(lane >= 32 ? o1 : o0);
    d[2] = dppu<0xB1>(d[0]);
    STM(d, og, m_o);
  }
  STAMP(12);
  if (i < 112) {
    u32 fw = L_FR + (pp * 2u + (gq & 1u)) * 32u + (u32)w * 4u;
    GAS u16* op = (GAS u16*)(out + fb * M_ + fc * 8 + (lane >> 3));
    float xr = *reinterpret_cast<const float*>(lds + L_XR + (lane >> 3) * 4);
    PIN(fw); PIN(op); PIN(xr);
    v4u ov[1];
    if (EDLY) __builtin_amdgcn_s_sleep(EDLY);
    PISSUE(ov, 1, oo, 0u)
    PCHECK1(ov, 0, "v[96:99]", oo, 5u)
    float c = x32_add(j16_add(__uint_as_float(ov[0][0]), __uint_as_float(ov[0][2])));
    c = ror_add<4>(c); c = ror_add<8>(c);
    *reinterpret_cast<float*>(lds + fw) = c;
    FL_ADD(F_B + 1024);
    if (w == 0) {
      fl_wait<F_B + 1024, 8>(fr);
      STAMP(13);
      float a = *reinterpret_cast<const float*>(lds + L_FR + lane * 4);
      a += dppf<0xB1>(a); a += dppf<0x4E>(a); a += dppf<0x141>(a);
      if ((lane & 7) == 0) *op = f2bf(xr + a);
      if (i == 0 && lane == 0) __hip_atomic_store((gu32*)(ws + WS_EP), tag, RLX_AGENT);
      STAMP(14);
    }
  }
  STAMP_FLUSH;
}

extern "C" void kernel_launch(void* const* d_in, const int* in_sizes, int n_in,
                              void* d_out, int out_size, void* d_ws, size_t ws_size,
                              hipStream_t stream) {
  const u16* x = (const u16*)d_in[0];
  const u16* ckv = (const u16*)d_in[1];
  const u16* wq = (const u16*)d_in[2];
  const u16* wuk = (const u16*)d_in[3];
  const u16* wuv = (const u16*)d_in[4];
  const u16* wo = (const u16*)d_in[5];
  const u16* w1 = (const u16*)d_in[7];
  const u16* b1 = (const u16*)d_in[8];
  const u16* w2 = (const u16*)d_in[9];
  const u16* b2 = (const u16*)d_in[10];
  mla_decode<<<256, 576, 0, stream>>>(x, ckv, wq, wuk, wuv, wo, w1, b1, w2, b2, (char*)d_ws, (u16*)d_out);
}
```

```cpp
#include <hip/hip_runtime.h>
#include <hip/hip_bf16.h>
#include <stdint.h>

typedef unsigned short u16;
typedef unsigned int u32;
typedef unsigned long long u64;
typedef __attribute__((ext_vector_type(4))) unsigned v4u;
typedef __attribute__((ext_vector_type(2))) unsigned v2u;
typedef __attribute__((ext_vector_type(4))) float v4f;
typedef __attribute__((ext_vector_type(8))) __bf16 bf8;
typedef __attribute__((ext_vector_type(4))) short s4;
typedef __attribute__((ext_vector_type(8))) short s8;

#define GAS __attribute__((address_space(1)))
#define LAS __attribute__((address_space(3)))
typedef GAS u32 gu32;
typedef GAS u64 gu64;
#define RLX_AGENT __ATOMIC_RELAXED, __HIP_MEMORY_SCOPE_AGENT

#define M_ 448
#define F_ 1792
#define DC 1024
#define SK 4096

constexpr u32 SPIN_CAP = 1u << 18;

constexpr u32 WS_EP = 0;
constexpr u32 WS_QL = 65536;
constexpr u32 WS_SC = 262144;
constexpr u32 WS_PV = 1310720;
constexpr u32 WS_ML = 3407872;
constexpr u32 WS_CP = 3416064;
constexpr u32 WS_OG = 4464640;
constexpr u32 WS_QG = WS_OG + 2 * 128 * 448 * 8;
constexpr u32 WS_HP = WS_QG + 262144;
constexpr u32 WS_END = WS_HP + 112 * 7 * 256;

#define STAMP(k) do {} while (0)
#define STAMP_DECL
#define STAMP_FLUSH do {} while (0)

#define BAR() do { asm volatile("s_waitcnt lgkmcnt(0)" ::: "memory"); __builtin_amdgcn_s_barrier(); asm volatile("" ::: "memory"); } while (0)

__device__ __forceinline__ float bf2f(u16 v) { return __uint_as_float(((u32)v) << 16); }
__device__ __forceinline__ u16 f2bf(float f) { __hip_bfloat16 b = __float2bfloat16(f); return *reinterpret_cast<u16*>(&b); }
__device__ __forceinline__ float rbf(float f) { return bf2f(f2bf(f)); }
__device__ __forceinline__ float lo_f(u32 v) { return __uint_as_float(v << 16); }
__device__ __forceinline__ float hi_f(u32 v) { return __uint_as_float(v & 0xffff0000u); }
typedef __attribute__((ext_vector_type(2))) float v2f;
typedef __attribute__((ext_vector_type(2))) __bf16 bf2;
__device__ __forceinline__ u32 pk2(float a, float b) { return __builtin_bit_cast(u32, __builtin_convertvector(v2f{a, b}, bf2)); }
__device__ __forceinline__ v4u ld16(const u16* p) { return __builtin_nontemporal_load((const GAS v4u*)p); }
__device__ __forceinline__ v4u ldp16(const u16* p) { return *(const GAS v4u*)p; }
__device__ __forceinline__ u16 ldh(const u16* p) { return *(const GAS u16*)p; }
__device__ __forceinline__ bf8 as_bf8(v4u v) { return __builtin_bit_cast(bf8, v); }

template <int CTRL>
__device__ __forceinline__ float dppf(float v) {
  return __int_as_float(__builtin_amdgcn_update_dpp(0, __float_as_int(v), CTRL, 0xf, 0xf, true));
}
template <int N> __device__ __forceinline__ float ror_add(float v) { return v + dppf<0x120 + N>(v); }
template <int N> __device__ __forceinline__ float ror_max(float v) { return fmaxf(v, dppf<0x120 + N>(v)); }
__device__ __forceinline__ float x16_add(float v) {
  const u32 b = __float_as_uint(v);
  auto p = __builtin_amdgcn_permlane16_swap(b, b, false, false);
  return __uint_as_float(p[0]) + __uint_as_float(p[1]);
}
__device__ __forceinline__ float x32_add(float v) {
  const u32 b = __float_as_uint(v);
  auto p = __builtin_amdgcn_permlane32_swap(b, b, false, false);
  return __uint_as_float(p[0]) + __uint_as_float(p[1]);
}
__device__ __forceinline__ float x16_max(float v) {
  const u32 b = __float_as_uint(v);
  auto p = __builtin_amdgcn_permlane16_swap(b, b, false, false);
  return fmaxf(__uint_as_float(p[0]), __uint_as_float(p[1]));
}
__device__ __forceinline__ float x32_max(float v) {
  const u32 b = __float_as_uint(v);
  auto p = __builtin_amdgcn_permlane32_swap(b, b, false, false);
  return fmaxf(__uint_as_float(p[0]), __uint_as_float(p[1]));
}
__device__ __forceinline__ float wave_sum(float v) {
  v = ror_add<8>(v); v = ror_add<4>(v); v = ror_add<2>(v); v = ror_add<1>(v);
  return x32_add(x16_add(v));
}
__device__ __forceinline__ float wave_max(float v) {
  v = ror_max<8>(v); v = ror_max<4>(v); v = ror_max<2>(v); v = ror_max<1>(v);
  return x32_max(x16_max(v));
}

__device__ __forceinline__ u32 swz(u32 row) { return ((row & 3u) << 2) | ((row >> 2) & 3u); }
__device__ __forceinline__ u32 off_b(u32 row, u32 ch) { return 256u * row + 16u * (ch ^ swz(row)); }

constexpr u32 L_RA = 0;
constexpr u32 L_UV = 65536;
constexpr u32 L_WO = 98304;
constexpr u32 L_WQ = L_WO;
constexpr u32 L_W1 = L_WO + 14336;
constexpr u32 L_W2 = 126976;
constexpr u32 L_XN = 134144;
constexpr u32 L_P = L_XN + 1792;
constexpr u32 L_QST = L_P;
constexpr u32 L_SRED = L_P;
constexpr u32 L_AT = L_P + 16896;
constexpr u32 L_WG = L_AT + 4608;
constexpr u32 L_VST = L_WG + 128;
constexpr u32 L_VH = L_VST + 128;
constexpr u32 L_FR = L_VH + 128;
constexpr u32 L_BI = L_FR + 256;
constexpr u32 L_XR = L_BI + 64;
constexpr u32 L_TAG = L_XR + 32;
constexpr u32 L_FLAG = L_TAG + 32;
constexpr u32 L_END = L_FLAG + 14 * 256;
static_assert(L_END <= 163840, "LDS");
constexpr u32 F_PROG = 0;
constexpr u32 F_COL = 256;
constexpr u32 F_UK = 512;
constexpr u32 F_RO = 768;
constexpr u32 F_SR = 1024;
constexpr u32 F_B = 1280;
constexpr u32 F_QP = 2560;
constexpr u32 F_XN = 2816;
constexpr u32 F_QS = 3072;
constexpr u32 F_E2 = 3328;
#define CPW 1
#define CPM 0
#define CPF 64
#define CPE 2
#define CPT 1
#define KBH 1
#define SELFPRE 15
#define MLW 5
#define TLBW 0
#define EARLYQ 1
#define GPOS 1
#define GE1 0
#define GE2 8
#define GE3 0
#define LORD 0
constexpr int P_WQ = 14, P_UK = 46, P_ROWS = 110, P_W1 = 118, P_UV = 150, P_W2 = 157, P_WO = 171;
constexpr int P_END = 171;
#define CPOS 0
#define AXQ 0
#define AXW 2
#define AXR 0
#define BORD 1
#define UG 0
#define WINU 15
#define UVA 16
#define W1POS 0
#define E3G 1
#define ROT 0
#define LEAD 8
#define PMIN 0
#define KM 0
#define UKA 0
#define G2 0
#define XC 1
#define EC 0
#define EARLYX 8
#define PREE 0
#define PM 127
#define PREP 0
#define PSEL ((lane >> 3) == w)
#define LGATE 0

constexpr int P_UVC = W1POS == 1 ? P_UV - 8 : P_UV;
#define WIN 15
#define WIN2 16
#define WINC 16
#define WIN3 16
constexpr int WMAX = WIN > WIN2 ? (WIN > WIN3 ? WIN : WIN3) : (WIN2 > WIN3 ? WIN2 : WIN3);
#define HOIST 0
#define HU 7
#define WINL 19
#define RX1 0
#define RX4 0
#define RX8 0
#define TLW 1
#define LATE (xcd >= 3)
#define WINLU WINL
#define AUX_COL 2
#define PSLP 2
#define QDLY 18
#define BDLY 22
#define CDLY 14
#define DDLY 12
#define FDLY 16
#define EDLY 12
#define QGATE 4
#define PSTR 128
#define PRE_AUX 0
#define PRE_SC 0
#define POUT 0
#define PRE_RD(off) __builtin_amdgcn_raw_buffer_load_b32(rw, (off), 0, PRE_AUX)
#define PRE 110
#define CPRIO 2
#define SPRIO 3
constexpr u32 LSPIN = 1u << 22;

#define SPIN_FAIL(code) if (++spins > SPIN_CAP) { if (lane == 0) __hip_atomic_store(tmo, (code), RLX_AGENT); break; } __builtin_amdgcn_s_sleep(PSLP); asm volatile("" ::: "memory");

#define PIN(v) asm volatile("" : "+v"(v))
#define PINS(v) asm volatile("" : "+s"(v))
#define PISSUE(v, N, off, stride) _Pragma("unroll") for (int s_ = 0; s_ < (N); ++s_) v[s_] = __builtin_amdgcn_raw_buffer_load_b128(rw, (off) + ((s_ * (stride)) & 4095u), (s_ * (stride)) & ~4095u, 16);
#define PBAD(v) __builtin_amdgcn_bitop3_b32((v)[1], (v)[3], tag, 0x7E)
#define ANY(x) (__builtin_amdgcn_ballot_w64((x) != 0u) != 0ull)
#define RB "96"
#define RD(k) "v[" RB "+4*" #k ":" RB "+4*" #k "+3]"
#define RT(t, k) "v_bitop3_b32 " t ", v[" RB "+4*" #k "+1], v[" RB "+4*" #k "+3], %[tag] bitop3:0x7e\n"
#define RL(k, hi, lo) "s_movk_i32 %[x], " #hi "\n buffer_load_dwordx4 " RD(k) ", %[off], %[rs], %[x] offen offset:" #lo " sc1\n"
#define RIF(g, body) "s_bitcmp1_b32 %[pe], " #g "\n s_cbranch_scc0 1" #g "f\n" body "1" #g ":\n"
#define RM(t, bit) "v_cmp_ne_u32_e32 vcc, 0, " t "\n s_cmp_lg_u64 vcc, 0\n s_cselect_b32 %[x], " #bit ", 0\n s_or_b32 %[pe], %[pe], %[x]\n"
#define STR_(x) #x
#define STR(x) STR_(x)
#define RHEAD "s_nop 7\n s_setprio 0\n s_mov_b32 %[n], 0x40000\n s_branch 3f\n 1:\n s_sleep " STR(PSLP) "\n"
#define RTAIL4 "v_or3_b32 %[u], %[t0], %[t1], %[t2]\n v_or_b32_e32 %[u], %[u], %[t3]\n v_cmp_ne_u32_e32 vcc, 0, %[u]\n s_cbranch_vccz 2f\n s_mov_b32 %[pe], 0\n" \
               RM("%[t0]", 1) RM("%[t1]", 2) RM("%[t2]", 4) RM("%[t3]", 8) "s_sub_u32 %[n], %[n], 1\n s_cbranch_scc0 1b\n 2:\n s_setprio " STR(CPRIO)
#define RTMO(code) if (__builtin_expect(n_ == ~0u, 0)) { if (lane == 0) __hip_atomic_store(tmo, (code), RLX_AGENT); }
#define RETRY1(v0, K, REG, of, code) { u32 n_, t_; \
    asm volatile("s_setprio 0\n s_mov_b32 %[n], 0x40000\n 1:\n s_sleep " STR(PSLP) "\n buffer_load_dwordx4 " RD(K) ", %[off], %[rs], 0 offen sc1\n s_sub_u32 %[n], %[n], 1\n" \
                 "s_waitcnt vmcnt(0)\n s_cbranch_scc1 2f\n" RT("%[t]", K) "v_cmp_ne_u32_e32 vcc, 0, %[t]\n s_cbranch_vccnz 1b\n 2:\n s_setprio " STR(CPRIO) \
                 : "+{" REG "}"(v0), [n] "=&s"(n_), [t] "=&v"(t_) : [off] "v"(of), [rs] "s"(rw4), [tag] "s"(tag) : "vcc", "scc", "memory"); RTMO(code) }
#define RETRY4(v, of, code, h1, l1, h2, l2, h3, l3) { u32 n_, x_, pe_, t0_, t1_, t2_, t3_, u_; \
    asm volatile(RHEAD RIF(0, RL(0, 0, 0)) RIF(1, RL(1, h1, l1)) RIF(2, RL(2, h2, l2)) RIF(3, RL(3, h3, l3)) "s_waitcnt vmcnt(0)\n 3:\n" \
                 RT("%[t0]", 0) RT("%[t1]", 1) RT("%[t2]", 2) RT("%[t3]", 3) RTAIL4 \
                 : "+{v[96:99]}"(v[0]), "+{v[100:103]}"(v[1]), "+{v[104:107]}"(v[2]), "+{v[108:111]}"(v[3]), [n] "=&s"(n_), [x] "=&s"(x_), [pe] "=&s"(pe_), \
                   [t0] "=&v"(t0_), [t1] "=&v"(t1_), [t2] "=&v"(t2_), [t3] "=&v"(t3_), [u] "=&v"(u_) \
                 : [off] "v"(of), [rs] "s"(rw4), [tag] "s"(tag) : "vcc", "scc", "memory"); RTMO(code) }
#define RT2(t, k0, k1) RT(t, k0) RT("%[u]", k1) "v_or_b32_e32 " t ", " t ", %[u]\n"
#define RETRY8(v, of, code) { u32 n_, x_, pe_, t0_, t1_, t2_, t3_, u_; \
    asm volatile(RHEAD RIF(0, RL(0, 0, 0) RL(1, 0, 1024)) RIF(1, RL(2, 0, 2048) RL(3, 0, 3072)) RIF(2, RL(4, 4096, 0) RL(5, 4096, 1024)) RIF(3, RL(6, 4096, 2048) RL(7, 4096, 3072)) \
                 "s_waitcnt vmcnt(0)\n 3:\n" RT2("%[t0]", 0, 1) RT2("%[t1]", 2, 3) RT2("%[t2]", 4, 5) RT2("%[t3]", 6, 7) RTAIL4 \
                 : "+{v[96:99]}"(v[0]), "+{v[100:103]}"(v[1]), "+{v[104:107]}"(v[2]), "+{v[108:111]}"(v[3]), \
                   "+{v[112:115]}"(v[4]), "+{v[116:119]}"(v[5]), "+{v[120:123]}"(v[6]), "+{v[124:127]}"(v[7]), [n] "=&s"(n_), [x] "=&s"(x_), [pe] "=&s"(pe_), \
                   [t0] "=&v"(t0_), [t1] "=&v"(t1_), [t2] "=&v"(t2_), [t3] "=&v"(t3_), [u] "=&v"(u_) \
                 : [off] "v"(of), [rs] "s"(rw4), [tag] "s"(tag) : "vcc", "scc", "memory"); RTMO(code) }
#define PCHECK1(v, K, REG, off, code) if (__builtin_expect(ANY(PBAD(v[0])), RX1)) RETRY1(v[0], K, REG, off, code)
#define PCHECK4(v, off, code, h1, l1, h2, l2, h3, l3) if (__builtin_expect(ANY((PBAD(v[0]) | PBAD(v[1])) | (PBAD(v[2]) | PBAD(v[3]))), RX4)) RETRY4(v, off, code, h1, l1, h2, l2, h3, l3)
#define STM(d, off, mask) asm volatile("s_mov_b64 exec, %3\n buffer_store_dwordx4 %0, %1, %2, 0 offen sc1\n s_mov_b64 exec, -1\n s_nop 0" :: "v"(d), "v"(off), "s"(rw4), "s"(mask) : "memory")
#define STA(d, off) asm volatile("buffer_store_dwordx4 %0, %1, %2, 0 offen sc1\n s_nop 1" :: "v"(d), "v"(off), "s"(rw4) : "memory")
#define GRAN(d) v4u d = {0u, tag, 0u, tag}; PIN(d)
#define SCHED() __builtin_amdgcn_sched_barrier(0)

#define DPPMAX(n) "s_nop 1\n v_max_f32_dpp %0, %0, %0 row_ror:" #n " row_mask:0xf bank_mask:0xf\n"
__device__ __forceinline__ void wave_max2(float& a, float& b, float& jn) {
  asm volatile("s_nop 1\n v_permlane32_swap_b32 %0, %1\n v_max_f32 %0, %0, %1\n v_mov_b32 %1, %0\n"
               "s_nop 1\n v_permlane16_swap_b32 %0, %1\n v_max_f32 %0, %0, %1\n"
               DPPMAX(8) DPPMAX(4) DPPMAX(2) DPPMAX(1)
               "v_mov_b32 %2, %0\n v_mov_b32 %1, %0\n s_nop 1\n v_permlane32_swap_b32 %0, %1\n s_nop 1"
               : "+v"(a), "+v"(b), "=&v"(jn));
}
__device__ __forceinline__ float row_max(float a) {
  asm volatile(DPPMAX(8) DPPMAX(4) DPPMAX(2) DPPMAX(1) "s_nop 1" : "+v"(a));
  return a;
}
__device__ __forceinline__ float j32_add(float a, float b) {
  auto p = __builtin_amdgcn_permlane32_swap(__float_as_uint(a), __float_as_uint(b), false, false);
  return __uint_as_float(p[0]) + __uint_as_float(p[1]);
}
__device__ __forceinline__ float j16_add(float a, float b) {
  auto p = __builtin_amdgcn_permlane16_swap(__float_as_uint(a), __float_as_uint(b), false, false);
  return __uint_as_float(p[0]) + __uint_as_float(p[1]);
}
typedef __attribute__((ext_vector_type(16))) float v16f;
#define COLNT " nt"
__device__ __forceinline__ v4u mk_rsrc(const void* p, u32 bytes) {
  const u64 a = (u64)(uintptr_t)p;
  return v4u{(u32)a, (u32)(a >> 32) & 0xffffu, bytes, 0x00020000u};
}
#define COL_REQ(V) "s_nop 0\n buffer_load_dwordx4 " V ", %2, %0 offen" COLNT " lds\n s_add_u32 m0, m0, 0x400\n s_add_u32 %0, %0, 0x2000\n"
__device__ __forceinline__ void col_reads(v4u rk4, u32 vk, u32 ck, u32 ld) {
  u32 t0, t1;
  asm volatile(
      "s_mov_b32 %1, m0\n s_mov_b32 m0, %8\n s_mov_b32 %0, %7\n"
      COL_REQ("%3") COL_REQ("%4") COL_REQ("%5") COL_REQ("%6") COL_REQ("%3") COL_REQ("%4") COL_REQ("%5") COL_REQ("%6")
      "s_mov_b32 m0, %1\n"
      : "=&s"(t0), "=&s"(t1)
      : "s"(rk4), "v"(vk), "v"(vk ^ 16u), "v"(vk ^ 32u), "v"(vk ^ 48u), "s"(ck), "s"(ld)
      : "memory", "scc");
}

__device__ __forceinline__ void st_gran(char* ws, u32 off, u32 tag, u32 val) {
  __hip_atomic_store((gu64*)(ws + off), ((u64)tag << 32) | val, RLX_AGENT);
}
#define ST2(off, a, b) __builtin_amdgcn_raw_buffer_store_b128(v4u{(a), tag, (b), tag}, rw, (off), 0, 16)
template <int CTRL> __device__ __forceinline__ u32 dppu(u32 v) { return (u32)__builtin_amdgcn_update_dpp(0, (int)v, CTRL, 0xf, 0xf, true); }
__device__ __forceinline__ bf8 tr_pair(char* lds, u32 a0, u32 a1) {
  const s4 b0 = __builtin_amdgcn_ds_read_tr16_b64_v4i16((LAS s4*)(lds + a0));
  const s4 b1 = __builtin_amdgcn_ds_read_tr16_b64_v4i16((LAS s4*)(lds + a1));
  const s8 bb = {b0[0], b0[1], b0[2], b0[3], b1[0], b1[1], b1[2], b1[3]};
  return __builtin_bit_cast(bf8, bb);
}
__device__ __forceinline__ v4u lds16(char* lds, u32 a) { return *reinterpret_cast<const v4u*>(lds + a); }
__device__ __forceinline__ v4f lds16f(char* lds, u32 a) { return *reinterpret_cast<const v4f*>(lds + a); }
__device__ __forceinline__ float fdiv(float a, float d, float r) {
  const float q = a * r;
  return fmaf(fmaf(-q, d, a), r, q);
}
__device__ __forceinline__ v4u frag(v4u v, int pa) {
  v4u o;
#pragma unroll
  for (int e = 0; e < 4; ++e) o[e] = (u32)__builtin_amdgcn_ds_bpermute(pa, (int)v[e]);
  return o;
}
__device__ __forceinline__ void fl_write(u32 a, u32 v) { asm volatile("ds_write_b32 %0, %1" :: "v"(a), "v"(v) : "memory"); }
#define FL_ADD(F) asm volatile("ds_add_u32 %0, %1 offset:%2" :: "v"(fa), "v"(one), "n"(F) : "memory")
template <u32 F> __device__ __forceinline__ int fl_read(u32 fr) {
  u32 v; asm volatile("ds_read_b32 %0, %1 offset:%2\n s_waitcnt lgkmcnt(0)" : "=v"(v) : "v"(fr), "n"(F) : "memory");
  return (int)__builtin_amdgcn_readfirstlane(v);
}
#define WSL 0
#define WSLP ""
#define FL_LOOP "s_mov_b32 %1, 0x400000\n 1:\n ds_read_b32 %0, %2 offset:%3\n s_sub_u32 %1, %1, 1\n s_cbranch_scc1 2f\n" WSLP \
                "s_waitcnt lgkmcnt(0)\n v_cmp_gt_i32_e32 vcc, %4, %0\n s_cbranch_vccnz 1b\n 2:\n s_waitcnt lgkmcnt(0)"
template <u32 F, int V> __device__ __forceinline__ void fl_wait(u32 fr) {
  u32 v, n;
  asm volatile(FL_LOOP : "=&v"(v), "=&s"(n) : "v"(fr), "n"(F), "n"(V) : "vcc", "scc", "memory");
}
template <u32 F> __device__ __forceinline__ void fl_waits(u32 fr, int V) {
  u32 v, n;
  asm volatile(FL_LOOP : "=&v"(v), "=&s"(n) : "v"(fr), "n"(F), "s"(V) : "vcc", "scc", "memory");
}
template <int K> __device__ __forceinline__ void drain(u32 fp, int q, int lo) {
  asm volatile("s_waitcnt vmcnt(%0)" :: "n"(K) : "memory");
  fl_write(fp, (u32)(q - K > lo ? q - K : lo));
  if constexpr (K > 0) drain<K - 1>(fp, q, lo);
}
template <u32 F, int V> __device__ __forceinline__ void gate(u32 fp, int q, int& lo) {
  if (fl_read<F>(fp) >= V) return;
  if (lo < q) drain<WMAX - 2>(fp, q, lo);
  lo = q;
  fl_wait<F, V>(fp);
}
template <int N, int U, int XM, int SEG, int AUX, int W>
__device__ __forceinline__ void stream(char* lds, u32 lb, const void* base, u32 bytes, u32 vb, u32 sb, u32 ss, u32 ld, int q0, int& lo, u32 rot = 0u) {
  const __amdgpu_buffer_rsrc_t rs = __builtin_amdgcn_make_buffer_rsrc((void*)base, 0, bytes, 0x00020000);
#pragma unroll 1
  for (u32 k = 0; k < (u32)(N / U); ++k) {
    const int qk = q0 + (int)(k * U);
    const u32 wv = SEG == 1 ? (k < 3u ? k + 1u : k == 3u ? 0u : k) : k;
    const u32 sk = SEG == 1 ? sb + (wv ^ rot) * 256u : sb + k * (U * ss);
    const u32 kl = XC ? wv : k;
#pragma unroll
    for (u32 n = 0; n < (u32)U; ++n) {
      const int q = qk + (int)n;
      asm volatile("s_waitcnt vmcnt(%0)" :: "n"(W - 1) : "memory");
      lo = q - (W - 1) > lo ? q - (W - 1) : lo;
      fl_write(lb + L_FLAG, (u32)lo);
      const u32 xm = XM == 5 ? ((n >> 1) << 6) | ((k & 3u) << 4) : XM == 1 ? (n & 3u) << 4 : XM == 2 ? ((2u * n) & 15u) << 4 : XM == 3 ? (((n >> 2) & 1u) << 3) << 4 : 0u;
      if (XM == 4) {
        const u32 c = vb + (k * U + n) * 64u, rr = (c * 2341u) >> 16;
        __builtin_amdgcn_raw_ptr_buffer_load_lds(rs, (LAS void*)(lds + ld + k * (U * 1024u) + n * 1024u), 16, c * 16u + rr * 448u, sb, 0, AUX);
      } else
      __builtin_amdgcn_raw_ptr_buffer_load_lds(rs, (LAS void*)(lds + ld + kl * (U * 1024u) + n * 1024u), 16, vb ^ xm, sk + n * ss, 0, AUX);
    }
  }
}

__global__ __launch_bounds__(576) void mla_decode(const u16* x, const u16* wq, char* ws, const u16* wuk,
                                                  const u16* ckv, const u16* b1, const u16* b2, const u16* w1,
                                                  const u16* wuv, const u16* w2, const u16* wo, u16* out) {
  __shared__ __attribute__((aligned(16))) char lds[L_END];
  const int t = threadIdx.x, lane = t & 63;
  const int w = __builtin_amdgcn_readfirstlane(t >> 6);
  const int i = blockIdx.x, xcd = i & 7, r = i >> 3;
  const int h1i = xcd * 2 + (r >> 4), cs = r & 15;
  const int G = xcd * 4 + (r >> 3), b = G >> 4, g = G & 15, j = r & 7;
  const int ch = r & 1, hd = xcd * 16 + (r >> 1), ds = hd & 7;
  const int fsl = hd < 112 ? hd : 0;
  const u32 rot = XC ? (u32)j : ROT == 0 ? 0u : ROT == 1 ? (u32)j : ROT == 2 ? (u32)xcd : ROT == 3 ? (u32)(j ^ xcd) : (u32)((j + 1) & 7);
  STAMP_DECL
  STAMP(0);
  u32 cpf0 = 0, cpf1 = 0;
  if (w >= 4 && w < 8) {
    u64 pc; u32 len;
    asm volatile("s_getpc_b64 %0\n.Lmla_pc:\n\ts_mov_b32 %1, .Lmla_end-.Lmla_pc" : "=s"(pc), "=s"(len));
    len = len < 65536u ? len : 0u;
    const u32 o = (u32)(t - 256) * 64u, o2 = o + 16384u;
    cpf0 = *(const GAS u32*)(pc + (o < len ? o : len));
    cpf1 = *(const GAS u32*)(pc + (o2 < len ? o2 : len));
  }
  SCHED();
  asm volatile("" : "+s"(x), "+s"(ckv), "+s"(wq), "+s"(wuk), "+s"(wuv), "+s"(wo), "+s"(w1), "+s"(b1), "+s"(w2), "+s"(b2), "+s"(ws), "+s"(out));
  gu32* tmo = (gu32*)(ws + WS_EP + 128);
  const __amdgpu_buffer_rsrc_t rw = __builtin_amdgcn_make_buffer_rsrc(ws, 0, WS_END, 0x00020000);
  const u32 gq = lane >> 4, qq = (lane & 15) >> 2, pp = lane & 3;

  const u32 lb = (u32)(uintptr_t)(LAS char*)lds;
  u32 fr = lb + L_FLAG, fa = lb + L_FLAG + (u32)lane * 4u, one = 1u;

#define S_HEAD { __builtin_amdgcn_s_setprio(SPRIO); \
    if (lane < 14) fl_write(lb + L_FLAG + lane * 256, 0u); \
    STAMP(26); \
    int lo = 0; \
    stream<14, HU, 0, 0, AXQ, 16>(lds, lb, wq, M_ * 8192u, (u32)(lane >> 3) * 8192u + (((u32)lane & 7u) ^ ((((u32)lane >> 3) & 3u) << 1)) * 16u, (u32)(cs & 3) * (112u * 8192u) + (u32)h1i * 512u + (u32)(cs >> 2) * 128u, 8u * 8192u, L_WQ, 0, lo); \
    STAMP(28); \
    BAR(); }
  const int w8 = w;
  if (w8 == 8) {
    S_HEAD
    int lo = 0;
    const u32 hb = (u32)lane >> 5;
    const u32 vk = gq * 2048u + (((u32)lane & 15u) ^ (gq << 2)) * 16u;
#define S_UK(n, o, W) stream<n, 8, 2, 0, AXW, W>(lds, lb, wuk, 1024u * 8192u, hb * 8192u + (((u32)lane & 31u) ^ hb) * 16u, (u32)(cs * 64) * 8192u + (u32)h1i * 512u + (o) * 16384u, 16384u, L_UV + (o) * 1024u, P_UK - 32 + (o), lo);
    if (LATE) { S_UK(32 - UKA, UKA, WINLU) } else
    { S_UK(32 - UKA, UKA, WINU) }
    if (LATE) stream<64, 8, 1, 1, AXR, WINL>(lds, lb, ckv, 2u * SK * DC * 2u, vk, (u32)(b * SK + g * 256 + j * 32) * 2048u, 8192u, L_RA, P_UK, lo, rot); else
    stream<64, 8, 1, 1, AXR, WIN>(lds, lb, ckv, 2u * SK * DC * 2u, vk, (u32)(b * SK + g * 256 + j * 32) * 2048u, 8192u, L_RA, P_UK, lo, rot);
    STAMP(25);
    const bool hp = xcd < 7 && r < 28;
    const u32 fg = (u32)xcd * 4u + (u32)(r & 3), ks = (u32)r >> 2;
    const u32 sw = (((u32)lane & 7u) ^ ((((u32)lane >> 3) & 3u) << 1)) * 16u;
#define S_W1(q) stream<8, 8, 0, 0, AXW, WIN2>(lds, lb, w1, hp ? M_ * F_ * 2u : 0u, (u32)(lane >> 3) * (F_ * 2u) + sw, hp ? ks * (64u * F_ * 2u) + fg * 128u : 0u, 8u * F_ * 2u, L_W1, q, lo);
#define S_UV(n, o, q) stream<n, 8, 3, 0, AXW, WIN2>(lds, lb, wuv, 1024u * 8192u, hb * 8192u + (((u32)lane & 31u) ^ (hb << 2)) * 16u, (u32)(cs * 64) * 8192u + (u32)h1i * 512u + (o) * 16384u, 16384u, L_UV + (o) * 1024u, q, lo);
    constexpr int QA = P_ROWS + 8;
    S_W1(P_ROWS)
    gate<F_UK, 4>(fr, QA, lo);
    S_UV(UVA, 0, QA)
    drain<WMAX - 2>(fr, QA + UVA, lo);
    lo = QA + UVA;
    const u32 tag = (u32)__builtin_amdgcn_readfirstlane(*reinterpret_cast<const u32*>(lds + L_TAG));
    if (hp) {
      const u32 ao = L_XN + (u32)(lane & 1) * 896u + ks * 128u + gq * 16u;
      const bf8 a0 = as_bf8(lds16(lds, ao)), a1 = as_bf8(lds16(lds, ao + 64));
      float h0 = 0.f, h1 = 0.f;
#pragma unroll
      for (u32 T = 0; T < 4; ++T) {
        const u32 bo = L_W1 + (8u * gq + qq) * 128u + (((T * 2u + (pp >> 1)) ^ (qq << 1)) * 16u) + (pp & 1u) * 8u;
        v4f acc = {0.f, 0.f, 0.f, 0.f};
        acc = __builtin_amdgcn_mfma_f32_16x16x32_bf16(a0, tr_pair(lds, bo, bo + 512), acc, 0, 0, 0);
        acc = __builtin_amdgcn_mfma_f32_16x16x32_bf16(a1, tr_pair(lds, bo + 4096, bo + 4096 + 512), acc, 0, 0, 0);
        if (gq == T) { h0 = acc[0]; h1 = acc[1]; }
      }
      ST2(WS_HP + (((fg * 4u + gq) * 7u + ks) * 16u + ((u32)lane & 15u)) * 16u, __float_as_uint(h0), __float_as_uint(h1));
    }
    STAMP(30);
    fl_wait<G2 ? F_SR : F_E2, 8>(fr);
    S_UV(32 - UVA, UVA, QA + UVA)
    gate<F_E2, E3G ? 16 : 8>(fr, P_UV, lo);
    STAMP(31);
    stream<7, 7, 4, 0, AXW, WIN3>(lds, lb, w2, 1792u * 896u, (u32)lane, (u32)(fsl * 16) * 896u + (u32)ch * 448u, 0u, L_W2, P_W2 - 7, lo);
    STAMP(27);
    stream<14, 7, 4, 0, AXW, WIN3>(lds, lb, wo, 4096u * 896u, (u32)lane, (u32)(hd * 32) * 896u + (u32)ch * 448u, 0u, L_WO, P_W2, lo);
    {
      float a0 = 0.f, a1 = 0.f;
      if (hd < 112) {
        const u32 ho = WS_HP + (u32)hd * 1792u + (u32)lane * 16u;
        const u32 ho2 = ho + (lane < 48 ? 1024u : 768u);
        v4u u0, u1;
#pragma unroll 1
        for (u32 spins = 0;;) {
          u0 = __builtin_amdgcn_raw_buffer_load_b128(rw, ho, 0, 16);
          u1 = __builtin_amdgcn_raw_buffer_load_b128(rw, ho2, 0, 16);
          if (!ANY(PBAD(u0) | PBAD(u1))) break;
          SPIN_FAIL(8u)
        }
        a0 = __uint_as_float(u0[0]) + (lane < 48 ? __uint_as_float(u1[0]) : 0.f);
        a1 = __uint_as_float(u0[2]) + (lane < 48 ? __uint_as_float(u1[2]) : 0.f);
        a0 = x32_add(x16_add(a0)); a1 = x32_add(x16_add(a1));
      }
      const float hv = rbf(rbf(lane >= 32 ? a1 : a0) + *reinterpret_cast<const float*>(lds + L_BI + (lane & 15) * 4));
      const float ge = 0.5f * hv * (1.f + tanhf(0.7978845608028654f * (hv + 0.044715f * hv * hv * hv)));
      *reinterpret_cast<u16*>(lds + L_VH + lane * 2) = ((lane & 16) == 0 && hd < 112) ? f2bf(ge) : (u16)0;
      asm volatile("s_waitcnt lgkmcnt(0)" ::: "memory");
      STAMP(24);
    }
    drain<0>(fr, P_END, lo);
    STAMP(29);
    STAMP_FLUSH;
    return;
  }

  __builtin_amdgcn_s_setprio(CPRIO);
  u32 epv = __builtin_amdgcn_raw_buffer_load_b32(rw, WS_EP, 0, 0);
  const v4u rw4 = mk_rsrc(ws, WS_END);
  const int pa = (4 * (lane & 15) + (lane >> 4)) * 4;
  const int fb = i >= 56 ? 1 : 0, fc = i - fb * 56;
  u64 m_ev = 0x5555555555555555ull;
  PIN(fr); PIN(fa); PIN(one); PINS(m_ev);
  u32 tag, pre0 = 0, pre1 = 0, pre2 = 0;
  if (w == 0) {
    const int lc = lane < 56 ? lane : 0;
    const v4u xr0 = ldp16(x + lc * 8), xr1 = ldp16(x + M_ + lc * 8);
    STAMP(21);
    BAR();
    float xa[8], xb[8];
#pragma unroll
    for (int p = 0; p < 4; ++p) { xa[2 * p] = lo_f(xr0[p]); xa[2 * p + 1] = hi_f(xr0[p]); xb[2 * p] = lo_f(xr1[p]); xb[2 * p + 1] = hi_f(xr1[p]); }
    const float act = lane < 56 ? 1.f : 0.f;
    float s0 = 0.f, s1 = 0.f;
#pragma unroll
    for (int k = 0; k < 8; ++k) { s0 += xa[k]; s1 += xb[k]; }
    s0 = wave_sum(s0 * act); s1 = wave_sum(s1 * act);
    const float m0 = s0 * (1.f / M_), m1 = s1 * (1.f / M_);
    float v0 = 0.f, v1 = 0.f;
#pragma unroll
    for (int k = 0; k < 8; ++k) { const float d0 = xa[k] - m0, d1 = xb[k] - m1; v0 += d0 * d0; v1 += d1 * d1; }
    v0 = wave_sum(v0 * act); v1 = wave_sum(v1 * act);
    const float mb0 = rbf(m0), mb1 = rbf(m1);
    const float den0 = rbf(sqrtf(rbf(rbf(v0 * (1.f / M_)) + 1e-5f)));
    const float den1 = rbf(sqrtf(rbf(rbf(v1 * (1.f / M_)) + 1e-5f)));
    const float r0 = __builtin_amdgcn_rcpf(den0), r1 = __builtin_amdgcn_rcpf(den1);
    v4u na, nb;
#pragma unroll
    for (int p = 0; p < 4; ++p) {
      na[p] = pk2(fdiv(rbf(xa[2 * p] - mb0), den0, r0), fdiv(rbf(xa[2 * p + 1] - mb0), den0, r0));
      nb[p] = pk2(fdiv(rbf(xb[2 * p] - mb1), den1, r1), fdiv(rbf(xb[2 * p + 1] - mb1), den1, r1));
    }
    if (lane < 56) {
      *reinterpret_cast<v4u*>(lds + L_XN + lane * 16) = na;
      *reinterpret_cast<v4u*>(lds + L_XN + 896 + lane * 16) = nb;
    }
    FL_ADD(F_XN);
    STAMP(17);
  } else if (w < 4) {
    u32 bu = 0, bw = 0;
    if (w == 1) bu = ldh(b1 + fsl * 16 + (lane & 15));
    if (w == 2 && i < 112) { bu = ldh(x + fb * M_ + fc * 8 + (lane & 7)); bw = ldh(b2 + fc * 8 + (lane & 7)); }
    if (w == 3 && (lane < 8 ? (PM & 1) : (PM & 2))) pre0 = PRE_RD(lane < 8 ? WS_QL + (u32)(lane >> 2) * 65536u + (u32)(cs * 4 + (lane & 3)) * 1024u + (u32)h1i * 64u : WS_QG + (u32)h1i * 16384u + (u32)(cs & 3) * 4096u + (u32)(cs >> 2) * 1024u + (u32)(lane & 7) * 128u);
    BAR();
    PIN(bu); PIN(bw);
    if (w < 3 && lane < 16) *reinterpret_cast<float*>(lds + (w == 1 ? L_BI + lane * 4 : L_XR + (lane & 7) * 4)) = lo_f(bu) + lo_f(bw);
  }
  if (w < 4) {
    u32 ao = L_XN + (u32)(lane & 1) * 896u + (u32)(cs & 3) * 224u;
    const u32 ch = ((((u32)w * 2u + (pp >> 1)) ^ (qq << 1)) * 16u) + (pp & 1u) * 8u;
    u32 bo = L_WQ + (8u * gq + qq) * 128u + ch, bo2 = L_WQ + (96u + 4u * gq + qq) * 128u + ch;
    u32 qo = WS_QG + (u32)h1i * 16384u + (u32)(cs & 3) * 4096u + (u32)(cs >> 2) * 1024u + (u32)w * 256u + (u32)lane * 8u;
    u64 m_q = 0x55555555ull;
    tag = __builtin_amdgcn_readfirstlane(epv) + 1u;
    GRAN(d);
    PIN(bo); PIN(bo2); PIN(qo); PINS(m_q);
    if (w) fl_wait<F_XN, 1>(fr);
    v4u a0 = lds16(lds, ao + gq * 16u), a1 = lds16(lds, ao + gq * 16u + 64), a2 = lds16(lds, ao + gq * 16u + 128);
    s4 a3 = *reinterpret_cast<const s4*>(lds + ao + 192u + gq * 8u);
    asm volatile("" : "+v"(a0), "+v"(a1), "+v"(a2), "+v"(a3));
    fl_wait<F_PROG, P_WQ>(fr);
    STAMP(18);
    const bf8 b0 = tr_pair(lds, bo, bo + 512), b1f = tr_pair(lds, bo + 4096, bo + 4096 + 512), b2f = tr_pair(lds, bo + 8192, bo + 8192 + 512);
    const s4 b3 = __builtin_amdgcn_ds_read_tr16_b64_v4i16((LAS s4*)(lds + bo2));
    SCHED();
    v4f acc = {0.f, 0.f, 0.f, 0.f}, acd = {0.f, 0.f, 0.f, 0.f};
    acc = __builtin_amdgcn_mfma_f32_16x16x32_bf16(as_bf8(a0), b0, acc, 0, 0, 0);
    acc = __builtin_amdgcn_mfma_f32_16x16x32_bf16(as_bf8(a1), b1f, acc, 0, 0, 0);
    acc = __builtin_amdgcn_mfma_f32_16x16x32_bf16(as_bf8(a2), b2f, acc, 0, 0, 0);
    acd = __builtin_amdgcn_mfma_f32_16x16x16bf16_1k(a3, b3, acd, 0, 0, 0);
    d[0] = __float_as_uint(gq ? acc[1] + acd[1] : acc[0] + acd[0]);
    d[2] = dppu<0xB1>(d[0]);
    STM(d, qo, m_q);
    STAMP(1);
    FL_ADD(F_RO);
    asm volatile("" :: "v"(pre0));
    if (w == 3 && (lane < 32 ? (PM & 4) : (PM & 8))) pre0 = PRE_RD(lane < 32 ? WS_SC + (u32)((G * 16 + (lane >> 1)) * 256 + j * 32) * 8u + (u32)(lane & 1) * 128u : WS_ML + (u32)((b * 16 + 2 * j + (lane & 1)) * 16 + (g & 8)) * 16u);
  } else {
    v4u wk[8];
    BAR();
    asm volatile("v_readfirstlane_b32 %0, %1" : "=s"(tag) : "v"(epv));
    tag += 1u;
    *reinterpret_cast<u32*>(lds + L_TAG) = tag;
    u32 qo = WS_QG + (u32)h1i * 16384u + (u32)(w - 4) * 1024u + (u32)lane * 16u;
    u32 qw = L_QST + (u32)((lane >> 3) & 1) * 512u + (u32)(w - 4) * 128u + gq * 32u + (u32)(lane & 7) * 4u;
    u32 ao = L_QST + (u32)(lane & 1) * 512u + gq * 16u;
    const int bb = lane & 1, wq4 = w - 4;
    u32 qa = WS_QL + (u32)bb * 65536u + (u32)((cs * 2 + (wq4 >> 1)) * 2 + ((lane >> 2) & 1)) * 1024u + (u32)h1i * 64u
             + (u32)((wq4 & 1) * 2 + ((lane >> 3) & 1)) * 16u;
    u64 m_l = 0x3333ull;
    GRAN(d);
    PIN(qo); PIN(qw); PIN(ao); PIN(qa); PINS(m_l);
    fl_wait<F_RO, QGATE>(fr);
    if (QDLY) __builtin_amdgcn_s_sleep(QDLY);
    STAMP(19);
    v4u ga[4];
    PISSUE(ga, 4, qo, 4096u)
    fl_wait<F_PROG, P_UK>(fr);
    {
      const u32 ko = L_UV + (u32)((w - 4) * 16 + (lane & 15)) * 512u;
#pragma unroll
      for (int s = 0; s < 8; ++s) wk[s] = lds16(lds, ko + ((((u32)s * 4u + gq) ^ ((u32)lane & 15u)) * 16u));
    }
    FL_ADD(F_UK);
    PCHECK4(ga, qo, 1u, 4096, 0, 8192, 0, 12288, 0)
    asm volatile("" :: "v"(cpf0), "v"(cpf1));
    {
      const float q0 = (__uint_as_float(ga[0][0]) + __uint_as_float(ga[1][0])) + (__uint_as_float(ga[2][0]) + __uint_as_float(ga[3][0]));
      const float q1 = (__uint_as_float(ga[0][2]) + __uint_as_float(ga[1][2])) + (__uint_as_float(ga[2][2]) + __uint_as_float(ga[3][2]));
      *reinterpret_cast<u32*>(lds + qw) = pk2(q0, q1);
    }
    FL_ADD(F_QS);
    STAMP(20);
    fl_wait<F_QS, 4>(fr);
    v4u qf[8];
#pragma unroll
    for (int s = 0; s < 8; ++s) qf[s] = lds16(lds, ao + s * 64);
    SCHED();
    v4f acc = {0.f, 0.f, 0.f, 0.f};
#pragma unroll
    for (int s = 0; s < 8; ++s) acc = __builtin_amdgcn_mfma_f32_16x16x32_bf16(as_bf8(qf[s]), as_bf8(wk[s]), acc, 0, 0, 0);
    const float a0 = acc[0], a1 = acc[1];
    const float own = bb ? a1 : a0, oth = dppf<0xB1>(bb ? a0 : a1);
    d[0] = pk2(bb ? oth : own, bb ? own : oth);
    d[2] = dppu<0x4E>(d[0]);
    STM(d, qa, m_l);
    STAMP(2);
    if (BDLY) __builtin_amdgcn_s_sleep(BDLY);
    FL_ADD(F_QP);
  }
  v4u bv[8];
  const int slot = w == 0 ? 3 : w < 4 ? w - 1 : w;
  u32 ro[4];
  {
    const u32 key = (u32)lane & 15u;
#pragma unroll
    for (int s = 0; s < 4; ++s) { ro[s] = L_RA + (KM ? (u32)w * 256u + key * 2048u : (u32)(XC ? w : slot) * 8192u + key * 256u) + ((((u32)s * 4u + gq) ^ swz(key)) * 16u); PIN(ro[s]); }
  }
#define ROWS_IN { fl_waits<F_PROG>(fr, P_UK + 8 * (slot + 1)); \
    _Pragma("unroll") for (int s = 0; s < 8; ++s) bv[s] = lds16(lds, ro[s & 3] + (u32)(s >> 2) * 4096u); STAMP(3); }
  u32 so = WS_SC + (u32)((G * 16 + 2 * w) * 256 + 2 * lane) * 8u;
  {
    u32 qo = WS_QL + (u32)b * 65536u + ((u32)w ^ rot) * 8192u + (u32)lane * 16u;
    u32 srw = L_SRED + (u32)((w * 16 + (int)gq * 4) * 33 + (lane & 15)) * 4u;
    u32 srr = L_SRED + (u32)((t >> 5) * 33 + (t & 31)) * 4u;
    u32 sco = WS_SC + (u32)((G * 16 + (t >> 5)) * 256 + j * 32 + (t & 31)) * 8u;
    u32 vk = gq * 2048u + (((u32)lane & 15u) ^ (gq << 2)) * 16u;
    u32 ck = (u32)(b * SK + g * 256 + (XC ? w ^ j : slot) * 32) * 2048u + (u32)j * 256u, cl = lb + L_RA + (u32)(XC ? w : slot) * 8192u;
    int pav = pa;
    GRAN(d);
    PIN(qo); PIN(srw); PIN(srr); PIN(sco); PIN(so); PIN(pav); PIN(vk); PINS(ck); PINS(cl);
    v4u gl[8];
    const v4f z = {0.f, 0.f, 0.f, 0.f};
    v4f acc0, acc1;
    fl_wait<F_QP, 4>(fr);
    fl_waits<F_PROG>(fr, P_UK + 8 * (slot + 1) - LEAD > PMIN ? P_UK + 8 * (slot + 1) - LEAD : PMIN);
    PISSUE(gl, 8, qo, 1024u)
    ROWS_IN
#define BSTEP(s) { const v4u av = frag(v4u{gl[2 * s][0], gl[2 * s][2], gl[2 * s + 1][0], gl[2 * s + 1][2]}, pav); \
      acc0 = __builtin_amdgcn_mfma_f32_16x16x32_bf16(as_bf8(av), as_bf8(bv[s]), s ? acc0 : z, 0, 0, 0); \
      acc1 = __builtin_amdgcn_mfma_f32_16x16x32_bf16(as_bf8(av), as_bf8(bv[4 + s]), s ? acc1 : z, 0, 0, 0); }
    u32 bad = 0;
#pragma unroll
    for (int s = 0; s < 4; ++s) { bad |= PBAD(gl[2 * s]) | PBAD(gl[2 * s + 1]); BSTEP(s) }
    if (__builtin_expect(ANY(bad), RX8)) {
      RETRY8(gl, qo, 2u)
      v4u aw[4];
#pragma unroll
      for (int s = 0; s < 4; ++s) aw[s] = frag(v4u{gl[2 * s][0], gl[2 * s][2], gl[2 * s + 1][0], gl[2 * s + 1][2]}, pav);
      SCHED();
#pragma unroll
      for (int s = 0; s < 4; ++s) {
        acc0 = __builtin_amdgcn_mfma_f32_16x16x32_bf16(as_bf8(aw[s]), as_bf8(bv[s]), s ? acc0 : z, 0, 0, 0);
        acc1 = __builtin_amdgcn_mfma_f32_16x16x32_bf16(as_bf8(aw[s]), as_bf8(bv[4 + s]), s ? acc1 : z, 0, 0, 0);
      }
    }
    asm volatile("" :: "v"(pre0));
    STAMP(4);
    float* sr = reinterpret_cast<float*>(lds + srw);
#pragma unroll
    for (int q = 0; q < 4; ++q) { sr[q * 33] = acc0[q]; sr[q * 33 + 16] = acc1[q]; }
    FL_ADD(F_B);
    bool cdone = w == 0;
    fl_wait<F_B, 8>(fr);
    STAMP(5);
    const float* sp = reinterpret_cast<const float*>(lds + srr);
    float s = ((sp[0] + sp[528]) + (sp[2 * 528] + sp[3 * 528])) + ((sp[4 * 528] + sp[5 * 528]) + (sp[6 * 528] + sp[7 * 528]));
    s *= 0.0625f * 1.4426950408889634f;
    d[0] = __float_as_uint(s);
    d[2] = dppu<0xB1>(d[0]);
    STM(d, sco, m_ev);
    FL_ADD(F_SR);
    if (!cdone)
    col_reads(mk_rsrc(ckv, 2u * SK * DC * 2u), vk, ck, cl);
#if !(PREE & 1) && !(PREP & 1)
    if (w == 3 && (PM & 16)) pre0 = PRE_RD(WS_PV + (u32)(((b * 16 + (lane >> 2)) * 16 + g) * 512 + j * 64) * 8u + (u32)(lane & 3) * 128u);
#endif
  }
  const u32 bq = (u32)w >> 2;
  u32 pvo = WS_PV + (((bq * 16u + (u32)h1i) * 16u + (u32)(w & 3) * 4u + gq) * 512u + (u32)cs * 32u + (u32)(lane & 15) * 2u) * 8u;
  {
    const u32 jx = XC ? (u32)j : 0u;
    const u32 pf = qq + 4u * ((gq ^ jx) & 1u);
    u32 pw = L_P + (gq ^ (jx & 3u)) * 1024u + qq * 256u + (((u32)(2 * w) ^ pf) * 16u) + pp * 4u;
    u32 pwb = pw + ((jx & 4u) ^ 4u) * 1024u;
    pw += (jx & 4u) * 1024u;
    u32 po0 = L_P + gq * 256u + ((((u32)lane & 15u) ^ gq) * 16u), po1 = po0 ^ 64u;
    u32 k0 = L_RA + off_b(8 * gq + qq, 2 * w + (pp >> 1)) + 8 * (pp & 1);
    u32 k1 = L_RA + off_b(8 * gq + 4 + qq, 2 * w + (pp >> 1)) + 8 * (pp & 1);
    const int hi8 = (lane >> 3) & 1, od = lane & 1;
    u32 go = WS_PV + (u32)(((b * 16 + (lane >> 4) * 4 + 2 * hi8 + od) * 16 + g) * 512 + j * 64 + w * 8 + (lane & 6)) * 8u;
    u32 pw1 = pw ^ 16u, pwb1 = pwb ^ 16u;
    PIN(pwb); PIN(pwb1);
    GRAN(d);
    PIN(pw); PIN(pw1); PIN(po0); PIN(po1); PIN(k0); PIN(k1); PIN(go); PIN(pvo);
    v4u gs[4];
    bf8 kb[8];
    if (CDLY) __builtin_amdgcn_s_sleep(CDLY);
    PISSUE(gs, 4, so, 1024u)
    FL_ADD(F_E2);
    asm volatile("s_waitcnt vmcnt(%0)" :: "n"(4 + (PREP & 1)) : "memory");
    FL_ADD(F_COL);
    fl_wait<F_COL, 8>(fr);
#pragma unroll
    for (int ks = 0; ks < 8; ++ks) kb[ks] = tr_pair(lds, k0 + ks * 8192, k1 + ks * 8192);
    asm volatile("" : "+v"(kb[0]), "+v"(kb[1]), "+v"(kb[2]), "+v"(kb[3]), "+v"(kb[4]), "+v"(kb[5]), "+v"(kb[6]), "+v"(kb[7]));
    PCHECK4(gs, so, 3u, 0, 1024, 0, 2048, 0, 3072)
    asm volatile("" :: "v"(pre0));
    STAMP(6);
    float sv[8];
#pragma unroll
    for (int q = 0; q < 4; ++q) { sv[2 * q] = __uint_as_float(gs[q][0]); sv[2 * q + 1] = __uint_as_float(gs[q][2]); }
    float m0 = fmaxf(fmaxf(sv[0], sv[1]), fmaxf(sv[2], sv[3])), m1 = fmaxf(fmaxf(sv[4], sv[5]), fmaxf(sv[6], sv[7])), mj;
    wave_max2(m0, m1, mj);
    u32 pk[4];
#pragma unroll
    for (int q = 0; q < 4; ++q) {
      const float m = q < 2 ? m0 : m1;
      pk[q] = pk2(__builtin_amdgcn_exp2f(sv[2 * q] - m), __builtin_amdgcn_exp2f(sv[2 * q + 1] - m));
      *reinterpret_cast<u32*>(lds + (q == 0 ? pw : q == 1 ? pwb : q == 2 ? pw1 : pwb1)) = pk[q];
    }
    FL_ADD(F_B + 256);
    if (w == j) {
      float l = j32_add((lo_f(pk[0]) + hi_f(pk[0])) + (lo_f(pk[1]) + hi_f(pk[1])), (lo_f(pk[2]) + hi_f(pk[2])) + (lo_f(pk[3]) + hi_f(pk[3])));
      l = x16_add(l);
      l = ror_add<8>(l); l = ror_add<4>(l); l = ror_add<2>(l); l = ror_add<1>(l);
      if ((lane & 31) == 0) ST2(WS_ML + (u32)((b * 16 + 2 * w + (lane >> 5)) * 16 + g) * 16u, __float_as_uint(mj), __float_as_uint(l));
    }
    STAMP(22);
    fl_wait<F_B + 256, 8>(fr);
    STAMP(7);
    v4u pf8[8];
#pragma unroll
    for (int ks = 0; ks < 8; ++ks) pf8[ks] = lds16(lds, ((ks & 1) ? po1 : po0) + ks * 1024);
    SCHED();
    v4f acc = {0.f, 0.f, 0.f, 0.f};
#pragma unroll
    for (int ks = 0; ks < 8; ++ks) acc = __builtin_amdgcn_mfma_f32_16x16x32_bf16(as_bf8(pf8[ks]), kb[ks], acc, 0, 0, 0);
    const float s0 = hi8 ? acc[0] : acc[2], s1 = hi8 ? acc[1] : acc[3];
    const float n0 = dppf<0x128>(s0), n1 = dppf<0x128>(s1);
    const u32 g0 = pk2(hi8 ? n0 : acc[0], hi8 ? acc[2] : n0);
    const u32 g1 = pk2(hi8 ? n1 : acc[1], hi8 ? acc[3] : n1);
    const u32 gn = dppu<0xB1>(od ? g0 : g1);
    d[0] = od ? gn : g0; d[2] = od ? g1 : gn;
    STA(d, go);
  }
#if !(PREE & 2) && !(PREP & 2)
  if (w == 3 && (lane < 32 ? (PM & 32) : (PM & 64))) pre0 = PRE_RD(lane < 32 ? WS_CP + ((u32)(h1i * 16 + (lane >> 1)) * 16u + (u32)cs) * 256u + (u32)(lane & 1) * 128u
                                      : WS_OG + (u32)(((lane >> 4) & 1) * 128 + hd) * 3584u + (u32)ch * 1792u + (u32)((lane & 15) < 14 ? (lane & 15) : 0) * 128u);
#endif
  STAMP(8);
  u32 cvo = WS_CP + (((u32)h1i * 2u + bq) * 8u + (u32)ds) * 4096u + (u32)(lane >> 2) * 256u + (u32)(w & 3) * 64u + pp * 16u;
  {
    u32 aw = L_AT + (bq * 16u + (u32)(w & 3) * 4u + gq) * 144u + (u32)(lane & 15) * 8u;
    u32 ar = L_AT + (u32)(lane & 31) * 144u + (u32)(lane >> 5) * 16u;
    u32 wr = L_WG + (u32)(lane >> 5) * 16u;
    u32 bo = L_UV + ((gq >> 1) * 4u + (qq >> 1) + 8u * (qq & 1u)) * 512u
             + ((((u32)w * 4u + (gq & 1u) * 2u + (pp >> 1)) ^ (((qq >> 1) << 2) | ((qq & 1u) << 3))) * 16u) + (pp & 1u) * 8u;
    u32 co = WS_CP + ((((u32)h1i * 2u + (u32)(lane >> 5)) * 8u + (u32)w) * 16u + (u32)cs) * 256u + (u32)(lane & 31) * 8u;
    u32 mlo = WS_ML + (((gq & 1u) * 16u + (u32)h1i) * 16u + (u32)(lane & 15)) * 16u;
    u32 wgw = L_WG + (u32)(lane & 31) * 4u;
    GRAN(d);
    PIN(aw); PIN(ar); PIN(wr); PIN(bo); PIN(co); PIN(mlo); PIN(cvo); PIN(wgw);
    v4u pv[1], ml[1];
    bf8 ub[4];
    if (DDLY) __builtin_amdgcn_s_sleep(DDLY);
    if (w == MLW) { PISSUE(ml, 1, mlo, 0u) }
    PISSUE(pv, 1, pvo, 0u)
    FL_ADD(F_E2);
    const bool have = fl_read<F_PROG>(fr) >= P_UVC;
    if (have) {
#pragma unroll
      for (int T = 0; T < 4; ++T) ub[T] = tr_pair(lds, bo + T * 8192, bo + T * 8192 + 1024);
    }
    if (w == MLW) {
      PCHECK1(ml, 1, "v[100:103]", mlo, 7u)
      const float mg = __uint_as_float(ml[0][0]);
      const float eg = __builtin_amdgcn_exp2f(mg - row_max(mg));
      float ls = eg * __uint_as_float(ml[0][2]);
      ls = ror_add<8>(ls); ls = ror_add<4>(ls); ls = ror_add<2>(ls); ls = ror_add<1>(ls);
      const float rl = __builtin_amdgcn_rcpf(ls);
      *reinterpret_cast<float*>(lds + wgw) = fdiv(eg, ls, rl);
    }
    PCHECK1(pv, 0, "v[96:99]", pvo, 4u)
    asm volatile("" :: "v"(pre0));
    STAMP(9);
    *reinterpret_cast<v2u*>(lds + aw) = v2u{pv[0][0], pv[0][2]};
    FL_ADD(F_B + 512);
    if (!have) {
      fl_wait<F_PROG, P_UVC>(fr);
#pragma unroll
      for (int T = 0; T < 4; ++T) ub[T] = tr_pair(lds, bo + T * 8192, bo + T * 8192 + 1024);
    }
    STAMP(23);
    fl_wait<F_B + 512, 8>(fr);
    STAMP(10);
    v16f acc;
#pragma unroll
    for (int q = 0; q < 16; ++q) acc[q] = 0.f;
    v4u at[4];
#pragma unroll
    for (int T = 0; T < 4; ++T) at[T] = lds16(lds, ar + T * 32);
    v4f wt[4];
#pragma unroll
    for (int q = 0; q < 4; ++q) wt[q] = lds16f(lds, wr + q * 32);
    SCHED();
#pragma unroll
    for (int T = 0; T < 4; ++T) acc = __builtin_amdgcn_mfma_f32_32x32x16_bf16(as_bf8(at[T]), ub[T], acc, 0, 0, 0);
    float c[2];
#pragma unroll
    for (int e = 0; e < 2; ++e) {
      float u0 = wt[2 * e][0] * acc[8 * e], u1 = wt[2 * e][1] * acc[8 * e + 1];
      u0 = fmaf(wt[2 * e][2], acc[8 * e + 2], u0); u1 = fmaf(wt[2 * e][3], acc[8 * e + 3], u1);
      u0 = fmaf(wt[2 * e + 1][0], acc[8 * e + 4], u0); u1 = fmaf(wt[2 * e + 1][1], acc[8 * e + 5], u1);
      u0 = fmaf(wt[2 * e + 1][2], acc[8 * e + 6], u0); u1 = fmaf(wt[2 * e + 1][3], acc[8 * e + 7], u1);
      c[e] = u0 + u1;
    }
    d[0] = __float_as_uint(j32_add(c[0], c[1]));
    d[2] = dppu<0xB1>(d[0]);
    STM(d, co, m_ev);
  }
  STAMP(11);
  u32 oo = WS_OG + ((u32)(fb * 128 + (t >> 2)) * M_ + (u32)(fc * 8 + (t & 3) * 2)) * 8u;
  {
    u32 vw = L_VST + bq * 64u + (u32)(w & 3) * 16u + pp * 4u + (gq & 1u) * 2u;
    u32 va = L_VST + (u32)(lane & 1) * 64u + gq * 16u;
    u32 og = WS_OG + (u32)(((lane >> 5) * 128 + hd) * M_ + ch * 224 + w * 32 + (lane & 31)) * 8u;
    const u32 wc = w < 7 ? (u32)w : 0u;
    u32 bo0 = L_WO + (8u * gq + qq) * 448u + wc * 64u + pp * 8u;
    u32 bo1 = L_W2 + ((8u * gq + qq) & 15u) * 448u + wc * 64u + pp * 8u;
    u64 m_o = w < 7 ? m_ev : 0ull;
    GRAN(d);
    PIN(vw); PIN(va); PIN(og); PIN(bo0); PIN(bo1); PIN(oo); PINS(m_o);
    v4u cv[1];
    if (FDLY) __builtin_amdgcn_s_sleep(FDLY);
    PISSUE(cv, 1, cvo, 0u)
    fl_wait<F_PROG, P_END>(fr);
    const bf8 a1 = as_bf8(lds16(lds, va + (L_VH - L_VST)));
    const v4f z = {0.f, 0.f, 0.f, 0.f};
    v4f oa = __builtin_amdgcn_mfma_f32_16x16x32_bf16(a1, tr_pair(lds, bo1, bo1 + 1792), z, 0, 0, 0);
    v4f ob = __builtin_amdgcn_mfma_f32_16x16x32_bf16(a1, tr_pair(lds, bo1 + 32, bo1 + 32 + 1792), z, 0, 0, 0);
    bf8 wa = tr_pair(lds, bo0, bo0 + 1792), wb = tr_pair(lds, bo0 + 32, bo0 + 32 + 1792);
    asm volatile("" : "+v"(oa), "+v"(ob), "+v"(wa), "+v"(wb));
    SCHED();
    PCHECK1(cv, 0, "v[96:99]", cvo, 6u)
    asm volatile("" :: "v"(pre1), "v"(pre2));
    float c = x32_add(j16_add(__uint_as_float(cv[0][0]), __uint_as_float(cv[0][2])));
    c = ror_add<4>(c); c = ror_add<8>(c);
    *reinterpret_cast<u16*>(lds + vw) = f2bf(c);
    STAMP(15);
    FL_ADD(F_B + 768);
    fl_wait<F_B + 768, 8>(fr);
    STAMP(16);
    const bf8 a0 = as_bf8(lds16(lds, va));
    oa = __builtin_amdgcn_mfma_f32_16x16x32_bf16(a0, wa, oa, 0, 0, 0);
    ob = __builtin_amdgcn_mfma_f32_16x16x32_bf16(a0, wb, ob, 0, 0, 0);
    const float o0 = (gq & 1u) ? ob[0] : oa[0], o1 = (gq & 1u) ? ob[1] : oa[1];
    d[0] = __float_as_uint(lane >= 32 ? o1 : o0);
    d[2] = dppu<0xB1>(d[0]);
    STM(d, og, m_o);
  }
  STAMP(12);
  if (i < 112) {
    u32 fw = L_FR + (pp * 2u + (gq & 1u)) * 32u + (u32)w * 4u;
    GAS u16* op = (GAS u16*)(out + fb * M_ + fc * 8 + (lane >> 3));
    float xr = *reinterpret_cast<const float*>(lds + L_XR + (lane >> 3) * 4);
    PIN(fw); PIN(op); PIN(xr);
    v4u ov[1];
    if (EDLY) __builtin_amdgcn_s_sleep(EDLY);
    PISSUE(ov, 1, oo, 0u)
    if (__builtin_expect(ANY(PBAD(ov[0])), 0)) { RETRY1(ov[0], 0, "v[96:99]", oo, 5u) asm volatile(".Lmla_end:"); }
    float c = x32_add(j16_add(__uint_as_float(ov[0][0]), __uint_as_float(ov[0][2])));
    c = ror_add<4>(c); c = ror_add<8>(c);
    *reinterpret_cast<float*>(lds + fw) = c;
    FL_ADD(F_B + 1024);
    if (w == 0) {
      fl_wait<F_B + 1024, 8>(fr);
      STAMP(13);
      float a = *reinterpret_cast<const float*>(lds + L_FR + lane * 4);
      a += dppf<0xB1>(a); a += dppf<0x4E>(a); a += dppf<0x141>(a);
      if ((lane & 7) == 0) *op = f2bf(xr + a);
      if (i == 0 && lane == 0) __hip_atomic_store((gu32*)(ws + WS_EP), tag, RLX_AGENT);
      STAMP(14);
    }
  }
  STAMP_FLUSH;
}

extern "C" void kernel_launch(void* const* d_in, const int* in_sizes, int n_in,
                              void* d_out, int out_size, void* d_ws, size_t ws_size,
                              hipStream_t stream) {
  const u16* x = (const u16*)d_in[0];
  const u16* ckv = (const u16*)d_in[1];
  const u16* wq = (const u16*)d_in[2];
  const u16* wuk = (const u16*)d_in[3];
  const u16* wuv = (const u16*)d_in[4];
  const u16* wo = (const u16*)d_in[5];
  const u16* w1 = (const u16*)d_in[7];
  const u16* b1 = (const u16*)d_in[8];
  const u16* w2 = (const u16*)d_in[9];
  const u16* b2 = (const u16*)d_in[10];
  mla_decode<<<256, 576, 0, stream>>>(x, wq, (char*)d_ws, wuk, ckv, b1, b2, w1, wuv, w2, wo, (u16*)d_out);
}
```
